```python
import torch

def _softmax(x, axis=-1):
    return torch.softmax(x, dim=axis)
def _gelu(x):
    return torch.nn.functional.gelu(x, approximate="tanh")
def _silu(x):
    return torch.nn.functional.silu(x)
def _sigmoid(x):
    return torch.sigmoid(x)
def _layernorm(x, axis=-1, eps=1e-5):
    m = x.mean(dim=axis, keepdim=True)
    v = x.var(dim=axis, keepdim=True, unbiased=False)
    return (x - m) / torch.sqrt(v + eps)
def _scatter_add(tgt, idx, src, axis=0):
    return tgt.clone().index_add_(axis, idx, src)
def _segment_sum(src, seg, n):
    out = torch.zeros((n,) + src.shape[1:], dtype=src.dtype)
    return out.index_add_(0, seg, src)
def _pad(x, pw):
    flat = tuple(v for pair in reversed(pw) for v in pair)
    return torch.nn.functional.pad(x, flat)

kernel_name = 'sequential_preLN__gqa_block_cached__glu_gated_geglu__decode'

def setup_inputs():

    _w = (lambda a: (lambda t: t.to(torch.bfloat16) if t.is_floating_point() else t)(torch.as_tensor(a)))
    def _A_setup():
        B, Sq, D = 1, 1, 1024
        Hq, Hkv, Sk, Dh = 32, 4, 65536, 128
        assert Hq % Hkv == 0, (Hq, Hkv)
        return {
            "x": _w((lambda *s: torch.randn(*s, dtype=torch.bfloat16) * 0.05)(B, Sq, D)),
            "k_cache": _w((lambda *s: torch.randn(*s, dtype=torch.bfloat16) * 3.2)(B, Hkv, Sk, Dh)),
            "v_cache": _w((lambda *s: torch.randn(*s, dtype=torch.bfloat16) * 1.6)(B, Hkv, Sk, Dh)),
            "wq": _w((lambda *s: torch.randn(*s, dtype=torch.bfloat16) * 3.2)(D, Hq * Dh) * (D ** -0.5)),
            "wo": _w((lambda *s: torch.randn(*s, dtype=torch.bfloat16) * 1.6)(Hq * Dh, D) * ((Hq * Dh) ** -0.5)),
        }
    _A_in = _A_setup()

    def _B_setup():
        B, S, Dm, Df = 1, 1, 1024, 3584
        return {
            "x": _w((lambda *s: torch.randn(*s, dtype=torch.bfloat16) * 0.05)(B, S, Dm)),
            "w_gate": _w((lambda *s: torch.randn(*s, dtype=torch.bfloat16) * 1.6)(Dm, Df) * (Dm ** -0.5)),
            "w_up": _w((lambda *s: torch.randn(*s, dtype=torch.bfloat16) * 1.6)(Dm, Df) * (Dm ** -0.5)),
            "w_down": _w((lambda *s: torch.randn(*s, dtype=torch.bfloat16) * 1.6)(Df, Dm) * (Df ** -0.5)),
        }
    _B_in = _B_setup()

    return {
        "A_x": _A_in["x"],
        "A_k_cache": _A_in["k_cache"],
        "A_v_cache": _A_in["v_cache"],
        "A_wq": _A_in["wq"],
        "A_wo": _A_in["wo"],
        "B_x": _B_in["x"],
        "B_w_gate": _B_in["w_gate"],
        "B_w_up": _B_in["w_up"],
        "B_w_down": _B_in["w_down"],
    }

def reference(A_x, A_k_cache, A_v_cache, A_wq, A_wo, B_x, B_w_gate, B_w_up, B_w_down):
    def _A_ref(x, k_cache, v_cache, wq, wo):
        B, Sq, D = x.shape
        Hq, Dh = 32, 128
        Hkv = k_cache.shape[1]
        rep = Hq // Hkv
        q = (lambda x, *d: x.permute(*d))((x @ wq).reshape(B, Sq, Hq, Dh), 0, 2, 1, 3)
        k = (lambda x, n, axis: torch.repeat_interleave(x, n, dim=axis))(k_cache, rep, axis=1)
        v = (lambda x, n, axis: torch.repeat_interleave(x, n, dim=axis))(v_cache, rep, axis=1)
        scores = torch.einsum("bhqd,bhkd->bhqk", q, k) * (Dh ** -0.5)
        p = _softmax(scores, axis=-1)
        o = torch.einsum("bhqk,bhkd->bhqd", p, v)
        return (lambda x, *d: x.permute(*d))(o, 0, 2, 1, 3).reshape(B, Sq, Hq * Dh) @ wo

    def _B_ref(x, w_gate, w_up, w_down):
        g = torch.einsum("bsd,df->bsf", x, w_gate)
        u = torch.einsum("bsd,df->bsf", x, w_up)
        h = _gelu(g) * u
        return torch.einsum("bsf,fd->bsd", h, w_down)

    x = A_x
    h = x + _A_ref(_layernorm(x, axis=-1), A_k_cache, A_v_cache, A_wq, A_wo)
    h2 = h + _B_ref(_layernorm(h, axis=-1), B_w_gate, B_w_up, B_w_down)
    return h2
```

```cpp
#define MIMIC 1
#include <hip/hip_runtime.h>
#include <hip/hip_bf16.h>
#include <stdint.h>

typedef unsigned v4u __attribute__((ext_vector_type(4)));
typedef unsigned v2u __attribute__((ext_vector_type(2)));
typedef float f32x4 __attribute__((ext_vector_type(4)));
typedef short bf16x8 __attribute__((ext_vector_type(8)));
#define GAS __attribute__((address_space(1)))

__device__ __forceinline__ unsigned bf16_rne(float f) {
  unsigned u = __builtin_bit_cast(unsigned, f);
  u += 0x7FFFu + ((u >> 16) & 1u);
  return u >> 16;
}
__device__ __forceinline__ float bf2f(unsigned b) { return __builtin_bit_cast(float, b << 16); }
__device__ __forceinline__ float rbf(float f) { return bf2f(bf16_rne(f)); }
__device__ __forceinline__ float mr(float f) { return MIMIC ? rbf(f) : f; }
__device__ __forceinline__ float lo16(unsigned u) { return __builtin_bit_cast(float, u << 16); }
__device__ __forceinline__ float hi16(unsigned u) { return __builtin_bit_cast(float, u & 0xFFFF0000u); }
__device__ __forceinline__ f32x4 mfma(v4u a, v4u b, f32x4 c) {
  return __builtin_amdgcn_mfma_f32_16x16x32_bf16(__builtin_bit_cast(bf16x8, a), __builtin_bit_cast(bf16x8, b), c, 0, 0, 0);
}
__device__ __forceinline__ float wave_sum(float v) {
#pragma unroll
  for (int o = 32; o >= 1; o >>= 1) v += __shfl_xor(v, o, 64);
  return v;
}

__device__ void ln1024(float* xs, float* red) {
  const int t = threadIdx.x;
  float v[4];
  float s = 0.f;
#pragma unroll
  for (int j = 0; j < 4; ++j) { v[j] = xs[t * 4 + j]; s += v[j]; }
  s = wave_sum(s);
  if ((t & 63) == 0) red[t >> 6] = s;
  __syncthreads();
  const float mean = (red[0] + red[1] + red[2] + red[3]) * (1.f / 1024.f);
  __syncthreads();
  float q = 0.f;
#pragma unroll
  for (int j = 0; j < 4; ++j) { const float d = v[j] - mean; q += d * d; }
  q = wave_sum(q);
  if ((t & 63) == 0) red[t >> 6] = q;
  __syncthreads();
  const float var = (red[0] + red[1] + red[2] + red[3]) * (1.f / 1024.f);
  __syncthreads();
  const float mb = rbf(mean), vb = rbf(var);
  const float sd = rbf(sqrtf(rbf(vb + 1e-5f)));
#pragma unroll
  for (int j = 0; j < 4; ++j) xs[t * 4 + j] = rbf(rbf(v[j] - mb) / sd);
  __syncthreads();
}

__global__ void __launch_bounds__(256) k_qproj(const unsigned short* x, const unsigned short* wq, float* q) {
  __shared__ float xs[1024];
  __shared__ float red[4];
  __shared__ float part[128][17];
  const int t = threadIdx.x, c = blockIdx.x;
  for (int i = t; i < 1024; i += 256) xs[i] = bf2f(x[i]);
  __syncthreads();
  v4u w[8];
  const int rg = t >> 1, half = t & 1;
#pragma unroll
  for (int j = 0; j < 8; ++j) w[j] = *(const v4u*)(wq + (size_t)(rg + 128 * j) * 4096 + c * 16 + half * 8);
  ln1024(xs, red);
  float acc[8] = {0, 0, 0, 0, 0, 0, 0, 0};
#pragma unroll
  for (int j = 0; j < 8; ++j) {
    const float xv = xs[rg + 128 * j];
#pragma unroll
    for (int e = 0; e < 4; ++e) { acc[2 * e] += xv * lo16(w[j][e]); acc[2 * e + 1] += xv * hi16(w[j][e]); }
  }
#pragma unroll
  for (int e = 0; e < 8; ++e) part[rg][half * 8 + e] = acc[e];
  __syncthreads();
  if (t < 16) {
    float s = 0.f;
    for (int r = 0; r < 128; ++r) s += part[r][t];
    q[c * 16 + t] = s;
  }
}

__global__ void __launch_bounds__(512, 2) k_attn(const unsigned short* K, const unsigned short* V, const float* q, float* po, float* pml) {
  __shared__ float so[8][8][128];
  __shared__ float sml[8][8][2];
  const int t = threadIdx.x, lane = t & 63, w = t >> 6, b = blockIdx.x, g = b >> 6, s = b & 63;
  const int r16 = lane & 15, g4 = lane >> 4;
  const size_t key0 = (size_t)g * 65536 + s * 1024 + w * 128;
  v4u kf[8][4];
#pragma unroll
  for (int kt = 0; kt < 8; ++kt)
#pragma unroll
    for (int u = 0; u < 4; ++u)
      kf[kt][u] = __builtin_nontemporal_load((const GAS v4u*)(K + (key0 + 16 * kt + r16) * 128 + 32 * u + 8 * g4));
  v4u qf[4];
#pragma unroll
  for (int u = 0; u < 4; ++u) {
    const float* qp = q + (g * 8 + (r16 & 7)) * 128 + 32 * u + 8 * g4;
    unsigned h[8];
#pragma unroll
    for (int j = 0; j < 8; ++j) {
      const float v = qp[j];
      const unsigned hb = bf16_rne(v);
      h[j] = (r16 < 8) ? hb : (MIMIC ? 0u : bf16_rne(v - bf2f(hb)));
    }
    qf[u] = v4u{h[0] | (h[1] << 16), h[2] | (h[3] << 16), h[4] | (h[5] << 16), h[6] | (h[7] << 16)};
  }
  f32x4 sc[8];
  const float scale = 0.08838834764831845f;
#pragma unroll
  for (int kt = 0; kt < 8; ++kt) {
    f32x4 a = {0.f, 0.f, 0.f, 0.f};
#pragma unroll
    for (int u = 0; u < 4; ++u) a = mfma(kf[kt][u], qf[u], a);
#pragma unroll
    for (int r = 0; r < 4; ++r) {
      float v = a[r];
      v = rbf(rbf(v) * scale);
      a[r] = v;
    }
    sc[kt] = a;
  }
  v4u vf[4][8];
#pragma unroll
  for (int T = 0; T < 4; ++T)
#pragma unroll
    for (int j = 0; j < 8; ++j)
      vf[T][j] = __builtin_nontemporal_load((const GAS v4u*)(V + (key0 + 32 * T + 16 * (j >> 2) + 4 * g4 + (j & 3)) * 128 + 8 * r16));
  float m = -INFINITY;
#pragma unroll
  for (int kt = 0; kt < 8; ++kt)
#pragma unroll
    for (int r = 0; r < 4; ++r) m = fmaxf(m, sc[kt][r]);
  m = fmaxf(m, __shfl_xor(m, 16, 64));
  m = fmaxf(m, __shfl_xor(m, 32, 64));
  float l = 0.f;
  v4u pf[4];
#pragma unroll
  for (int T = 0; T < 4; ++T) {
    unsigned h[8];
#pragma unroll
    for (int j = 0; j < 8; ++j) {
      const float p = __expf(sc[2 * T + (j >> 2)][j & 3] - m);
      const unsigned hb = bf16_rne(p);
      h[j] = (r16 < 8) ? hb : bf16_rne(p - bf2f(hb));
      l += p;
    }
    pf[T] = v4u{h[0] | (h[1] << 16), h[2] | (h[3] << 16), h[4] | (h[5] << 16), h[6] | (h[7] << 16)};
  }
  if (r16 >= 8) {
#pragma unroll
    for (int T = 0; T < 4; ++T) pf[T] = v4u{0, 0, 0, 0};
  }
  l += __shfl_xor(l, 16, 64);
  l += __shfl_xor(l, 32, 64);
  f32x4 o[8];
#pragma unroll
  for (int e = 0; e < 8; ++e) o[e] = f32x4{0.f, 0.f, 0.f, 0.f};
#pragma unroll
  for (int T = 0; T < 4; ++T) {
#pragma unroll
    for (int e2 = 0; e2 < 4; ++e2) {
      v4u blo, bhi;
#pragma unroll
      for (int jj = 0; jj < 4; ++jj) {
        const unsigned a0 = vf[T][2 * jj][e2], a1 = vf[T][2 * jj + 1][e2];
        blo[jj] = __builtin_amdgcn_perm(a1, a0, 0x05040100u);
        bhi[jj] = __builtin_amdgcn_perm(a1, a0, 0x07060302u);
      }
      o[2 * e2] = mfma(pf[T], blo, o[2 * e2]);
      o[2 * e2 + 1] = mfma(pf[T], bhi, o[2 * e2 + 1]);
    }
  }
#pragma unroll
  for (int e = 0; e < 8; ++e)
#pragma unroll
    for (int r = 0; r < 4; ++r) {
      float v = o[e][r];
      v += __shfl_xor(v, 32, 64);
      if (g4 < 2) so[w][4 * g4 + r][8 * r16 + e] = v;
    }
  if (lane < 8) { sml[w][lane][0] = m; sml[w][lane][1] = l; }
  __syncthreads();
  {
    const int h = t >> 6, d = (t & 63) * 2;
    float M = -INFINITY;
#pragma unroll
    for (int ww = 0; ww < 8; ++ww) M = fmaxf(M, sml[ww][h][0]);
    float L = 0.f, o0 = 0.f, o1 = 0.f;
#pragma unroll
    for (int ww = 0; ww < 8; ++ww) {
      const float f = __expf(sml[ww][h][0] - M);
      L += f * sml[ww][h][1];
      o0 += f * so[ww][h][d];
      o1 += f * so[ww][h][d + 1];
    }
    float* dst = po + ((size_t)(g * 8 + h) * 64 + s) * 128 + d;
    dst[0] = o0; dst[1] = o1;
    if ((t & 63) == 0) { pml[((g * 8 + h) * 64 + s) * 2] = M; pml[((g * 8 + h) * 64 + s) * 2 + 1] = L; }
  }
}

__global__ void __launch_bounds__(256) k_oproj(const float* po, const float* pml, const unsigned short* wo, float* hp) {
  __shared__ float os[16];
  const int t = threadIdx.x, c = blockIdx.x, lane = t & 63;
  const int h = c >> 3, d0 = (c & 7) * 16;
  v2u w[16];
#pragma unroll
  for (int r = 0; r < 16; ++r) w[r] = *(const v2u*)(wo + (size_t)(16 * c + r) * 1024 + t * 4);
  if (t < 64) {
    const float m = pml[(h * 64 + lane) * 2], l = pml[(h * 64 + lane) * 2 + 1];
    float M = m;
#pragma unroll
    for (int o = 32; o >= 1; o >>= 1) M = fmaxf(M, __shfl_xor(M, o, 64));
    const float f = __expf(m - M);
    const float L = wave_sum(f * l);
#pragma unroll
    for (int j = 0; j < 16; ++j) {
      const float v = wave_sum(f * po[((size_t)h * 64 + lane) * 128 + d0 + j]);
      if (lane == 0) os[j] = mr(v / L);
    }
  }
  __syncthreads();
  float a0 = 0, a1 = 0, a2 = 0, a3 = 0;
#pragma unroll
  for (int r = 0; r < 16; ++r) {
    const float ov = os[r];
    a0 += ov * lo16(w[r][0]); a1 += ov * hi16(w[r][0]); a2 += ov * lo16(w[r][1]); a3 += ov * hi16(w[r][1]);
  }
  *(f32x4*)(hp + (size_t)c * 1024 + t * 4) = f32x4{a0, a1, a2, a3};
}

__global__ void __launch_bounds__(256) k_reduce(const float* parts, int nparts, const unsigned short* xb, const float* xf, float* hout, unsigned short* out) {
  __shared__ f32x4 red[4];
  const int t = threadIdx.x, c = blockIdx.x;
  f32x4 v = {0.f, 0.f, 0.f, 0.f};
  if (t < nparts) v = *(const f32x4*)(parts + (size_t)t * 1024 + c * 4);
#pragma unroll
  for (int j = 0; j < 4; ++j) v[j] = wave_sum(v[j]);
  if ((t & 63) == 0) red[t >> 6] = v;
  __syncthreads();
  if (t < 4) {
    const float s = red[0][t] + red[1][t] + red[2][t] + red[3][t];
    const float base = xb ? bf2f(xb[c * 4 + t]) : xf[c * 4 + t];
    const float r = base + mr(s);
    if (hout) hout[c * 4 + t] = mr(r);
    if (out) out[c * 4 + t] = (unsigned short)bf16_rne(r);
  }
}

__global__ void __launch_bounds__(256) k_ffn(const float* h, const unsigned short* wg, const unsigned short* wu, const unsigned short* wd, float* dp) {
  __shared__ float xs[1024];
  __shared__ float red[4];
  __shared__ float part[2][128][17];
  __shared__ float act[16];
  const int t = threadIdx.x, c = blockIdx.x;
  v4u g[8], u[8];
  const int rg = t >> 1, half = t & 1;
#pragma unroll
  for (int j = 0; j < 8; ++j) {
    g[j] = *(const v4u*)(wg + (size_t)(rg + 128 * j) * 3584 + c * 16 + half * 8);
    u[j] = *(const v4u*)(wu + (size_t)(rg + 128 * j) * 3584 + c * 16 + half * 8);
  }
  v2u w[16];
#pragma unroll
  for (int r = 0; r < 16; ++r) w[r] = *(const v2u*)(wd + (size_t)(16 * c + r) * 1024 + t * 4);
  for (int i = t; i < 1024; i += 256) xs[i] = h[i];
  __syncthreads();
  ln1024(xs, red);
  float ag[8] = {0, 0, 0, 0, 0, 0, 0, 0}, au[8] = {0, 0, 0, 0, 0, 0, 0, 0};
#pragma unroll
  for (int j = 0; j < 8; ++j) {
    const float xv = xs[rg + 128 * j];
#pragma unroll
    for (int e = 0; e < 4; ++e) {
      ag[2 * e] += xv * lo16(g[j][e]); ag[2 * e + 1] += xv * hi16(g[j][e]);
      au[2 * e] += xv * lo16(u[j][e]); au[2 * e + 1] += xv * hi16(u[j][e]);
    }
  }
#pragma unroll
  for (int e = 0; e < 8; ++e) { part[0][rg][half * 8 + e] = ag[e]; part[1][rg][half * 8 + e] = au[e]; }
  __syncthreads();
  if (t < 16) {
    float sg = 0.f, su = 0.f;
    for (int r = 0; r < 128; ++r) { sg += part[0][r][t]; su += part[1][r][t]; }
    sg = mr(sg); su = mr(su);
    const float inner = 0.7978845608028654f * (sg + 0.044715f * sg * sg * sg);
    const float ge = mr(0.5f * sg * (1.f + tanhf(inner)));
    act[t] = mr(ge * su);
  }
  __syncthreads();
  float a0 = 0, a1 = 0, a2 = 0, a3 = 0;
#pragma unroll
  for (int r = 0; r < 16; ++r) {
    const float ov = act[r];
    a0 += ov * lo16(w[r][0]); a1 += ov * hi16(w[r][0]); a2 += ov * lo16(w[r][1]); a3 += ov * hi16(w[r][1]);
  }
  *(f32x4*)(dp + (size_t)c * 1024 + t * 4) = f32x4{a0, a1, a2, a3};
}

extern "C" void kernel_launch(void* const* d_in, const int* in_sizes, int n_in,
                              void* d_out, int out_size, void* d_ws, size_t ws_size,
                              hipStream_t stream) {
  const unsigned short* x = (const unsigned short*)d_in[0];
  const unsigned short* K = (const unsigned short*)d_in[1];
  const unsigned short* V = (const unsigned short*)d_in[2];
  const unsigned short* wq = (const unsigned short*)d_in[3];
  const unsigned short* wo = (const unsigned short*)d_in[4];
  const unsigned short* wg = (const unsigned short*)d_in[6];
  const unsigned short* wu = (const unsigned short*)d_in[7];
  const unsigned short* wd = (const unsigned short*)d_in[8];
  float* ws = (float*)d_ws;
  float* q = ws;
  float* pml = ws + 4096;
  float* h = ws + 8192;
  float* po = ws + 16384;
  float* hp = po + 262144;
  float* dp = hp + 262144;
  k_qproj<<<256, 256, 0, stream>>>(x, wq, q);
  k_attn<<<256, 512, 0, stream>>>(K, V, q, po, pml);
  k_oproj<<<256, 256, 0, stream>>>(po, pml, wo, hp);
  k_reduce<<<256, 256, 0, stream>>>(hp, 256, x, nullptr, h, nullptr);
  k_ffn<<<224, 256, 0, stream>>>(h, wg, wu, wd, dp);
  k_reduce<<<256, 256, 0, stream>>>(dp, 224, nullptr, h, nullptr, (unsigned short*)d_out);
}
```

```cpp
#include <hip/hip_runtime.h>
#include <stdint.h>

typedef unsigned v4u __attribute__((ext_vector_type(4)));
typedef unsigned v2u __attribute__((ext_vector_type(2)));
typedef float f32x4 __attribute__((ext_vector_type(4)));
typedef float f32x2 __attribute__((ext_vector_type(2)));
typedef __bf16 bf16x8_t __attribute__((ext_vector_type(8)));
typedef __bf16 bf16x2_t __attribute__((ext_vector_type(2)));
typedef short s16x4 __attribute__((ext_vector_type(4)));
#define LAS __attribute__((address_space(3)))
#define GAS __attribute__((address_space(1)))

constexpr unsigned WS_QP = 0x010000;
constexpr unsigned WS_QF = 0x030000;
constexpr unsigned WS_ML = 0x050000;
constexpr unsigned WS_ST = 0x058000;
constexpr unsigned WS_PA = 0x100000;
constexpr unsigned WS_PB = 0x200000;
constexpr unsigned WS_PC = 0x280000;
constexpr unsigned WS_PD = 0x380000;
constexpr unsigned WS_END = 0x400000;

constexpr unsigned L_CTL = 131072;
constexpr unsigned L_FULL = L_CTL;
constexpr unsigned L_PROG = L_CTL + 16;
constexpr unsigned L_XF = L_CTL + 32;
constexpr unsigned L_ONES = L_CTL + 128;
constexpr unsigned L_VEC = L_CTL + 256;
constexpr unsigned L_XN = L_CTL + 1024;
constexpr unsigned L_Q = L_CTL + 2048;
constexpr unsigned LDS_BYTES = L_CTL + 4096;

constexpr int U_KV = 8, U_WO = 136, U_G = 144, U_U = 152, U_D = 160, U_END = 168;
constexpr unsigned LDS_CAP = 1u << 21, GLB_CAP = 1u << 16;
#define INFL 4
#define INFL0 6

__device__ __forceinline__ float asf(unsigned u) { return __builtin_bit_cast(float, u); }
__device__ __forceinline__ unsigned asu(float f) { return __builtin_bit_cast(unsigned, f); }
__device__ __forceinline__ unsigned pk2(float lo, float hi) {
  return __builtin_bit_cast(unsigned, __builtin_convertvector(f32x2{lo, hi}, bf16x2_t));
}
__device__ __forceinline__ float rbf(float f) { return asf(pk2(f, f) << 16); }
__device__ __forceinline__ float lo16(unsigned u) { return asf(u << 16); }
__device__ __forceinline__ float hi16(unsigned u) { return asf(u & 0xFFFF0000u); }
__device__ __forceinline__ f32x4 mfma(v4u a, v4u b, f32x4 c) {
  return __builtin_amdgcn_mfma_f32_16x16x32_bf16(__builtin_bit_cast(bf16x8_t, a), __builtin_bit_cast(bf16x8_t, b), c, 0, 0, 0);
}
template <int CTRL> __device__ __forceinline__ float dpp(float x) {
  return asf((unsigned)__builtin_amdgcn_mov_dpp((int)asu(x), CTRL, 0xf, 0xf, true));
}
constexpr int XOR1 = 0xB1, XOR2 = 0x4E, ROR4 = 0x124, ROR8 = 0x128;
__device__ __forceinline__ float xrow_max(float x) {
  auto s = __builtin_amdgcn_permlane16_swap(asu(x), asu(x), false, false);
  x = fmaxf(asf(s[0]), asf(s[1]));
  auto t = __builtin_amdgcn_permlane32_swap(asu(x), asu(x), false, false);
  return fmaxf(asf(t[0]), asf(t[1]));
}
__device__ __forceinline__ float xrow_sum(float x) {
  auto s = __builtin_amdgcn_permlane16_swap(asu(x), asu(x), false, false);
  x = asf(s[0]) + asf(s[1]);
  auto t = __builtin_amdgcn_permlane32_swap(asu(x), asu(x), false, false);
  return asf(t[0]) + asf(t[1]);
}
__device__ __forceinline__ float row_sum(float x) {
  x += dpp<XOR1>(x); x += dpp<XOR2>(x); x += dpp<ROR4>(x); x += dpp<ROR8>(x); return x;
}
__device__ __forceinline__ float row_max(float x) {
  x = fmaxf(x, dpp<XOR1>(x)); x = fmaxf(x, dpp<XOR2>(x)); x = fmaxf(x, dpp<ROR4>(x)); x = fmaxf(x, dpp<ROR8>(x)); return x;
}
__device__ __forceinline__ float red_scatter(f32x4 a) {
  auto s0 = __builtin_amdgcn_permlane32_swap(asu(a[0]), asu(a[2]), false, false);
  auto s1 = __builtin_amdgcn_permlane32_swap(asu(a[1]), asu(a[3]), false, false);
  const float b0 = asf(s0[0]) + asf(s0[1]), b1 = asf(s1[0]) + asf(s1[1]);
  auto t = __builtin_amdgcn_permlane16_swap(asu(b0), asu(b1), false, false);
  float c = asf(t[0]) + asf(t[1]);
  c += dpp<ROR4>(c); c += dpp<ROR8>(c);
  return c;
}
template <int N> __device__ __forceinline__ void waitvm() { asm volatile("s_waitcnt vmcnt(%0)" ::"n"(N) : "memory"); }
__device__ __forceinline__ void lds_st32(unsigned a, unsigned v) { asm volatile("ds_write_b32 %0, %1" ::"v"(a), "v"(v) : "memory"); }
__device__ __forceinline__ void lds_st16(unsigned a, unsigned v) { asm volatile("ds_write_b16 %0, %1" ::"v"(a), "v"(v) : "memory"); }
__device__ __forceinline__ void lds_st64(unsigned a, v2u v) { asm volatile("ds_write_b64 %0, %1" ::"v"(a), "v"(v) : "memory"); }
__device__ __forceinline__ unsigned lds_ld32_wait(unsigned a) {
  unsigned r; asm volatile("ds_read_b32 %0, %1\n\ts_waitcnt lgkmcnt(0)" : "=v"(r) : "v"(a) : "memory"); return r;
}
__device__ __forceinline__ v4u lds_ld128_wait(unsigned a) {
  v4u r; asm volatile("ds_read_b128 %0, %1\n\ts_waitcnt lgkmcnt(0)" : "=v"(r) : "v"(a) : "memory"); return r;
}
__device__ __forceinline__ v4u lds128(unsigned a) { return *(const LAS v4u*)(uintptr_t)a; }
__device__ __forceinline__ v2u ldstr(unsigned a) {
  return __builtin_bit_cast(v2u, __builtin_amdgcn_ds_read_tr16_b64_v4i16((LAS s16x4*)(uintptr_t)a));
}
__device__ __forceinline__ v4u ldstr2(unsigned a0, unsigned a1) {
  const v2u x = ldstr(a0), y = ldstr(a1); return v4u{x[0], x[1], y[0], y[1]};
}
__device__ __forceinline__ void dma4(__amdgpu_buffer_rsrc_t r, unsigned so, unsigned m, unsigned o0, unsigned o1, unsigned o2, unsigned o3) {
  asm volatile("s_mov_b32 m0, %0\n\ts_nop 0\n\t"
               "buffer_load_dwordx4 %1, %5, %6 offen nt lds\n\t"
               "buffer_load_dwordx4 %2, %5, %6 offen offset:1024 nt lds\n\t"
               "buffer_load_dwordx4 %3, %5, %6 offen offset:2048 nt lds\n\t"
               "buffer_load_dwordx4 %4, %5, %6 offen offset:3072 nt lds"
               ::"s"(m), "v"(o0), "v"(o1), "v"(o2), "v"(o3), "s"(r), "s"(so) : "memory");
}
__device__ __forceinline__ __amdgpu_buffer_rsrc_t mkrs(const void* p, unsigned bytes) {
  return __builtin_amdgcn_make_buffer_rsrc((void*)p, (short)0, (int)bytes, 0x00020000);
}
__device__ __forceinline__ v4u gld(__amdgpu_buffer_rsrc_t rs, unsigned vo, unsigned so) { return __builtin_amdgcn_raw_buffer_load_b128(rs, (int)vo, (int)so, 16); }
__device__ __forceinline__ void gst(__amdgpu_buffer_rsrc_t rs, unsigned vo, v4u v) { __builtin_amdgcn_raw_buffer_store_b128(v, rs, (int)vo, 0, 16); }
__device__ __forceinline__ v4u tag4(f32x4 a, unsigned T) {
  v4u r;
#pragma unroll
  for (int e = 0; e < 4; ++e) r[e] = (asu(a[e]) & ~0xFFu) | (T & 0xFFu);
  return r;
}
__device__ __forceinline__ bool none(bool p) { return __builtin_amdgcn_ballot_w64(p) == 0; }

__device__ __forceinline__ void wait_full(int need, int& seen, bool& dead) {
  if (seen >= need) return;
  for (unsigned spins = 0;; ++spins) {
    seen = (int)__builtin_amdgcn_readfirstlane(lds_ld32_wait(L_FULL));
    if (seen >= need) return;
    if (dead || spins > LDS_CAP) { dead = true; return; }
    __builtin_amdgcn_s_sleep(1);
  }
}
__device__ __forceinline__ void exch(unsigned flags, unsigned a, v4u& f0, v4u& f1, bool& dead) {
  for (unsigned spins = 0;; ++spins) {
    v4u fl;
    asm volatile("ds_read_b128 %0, %3\n\tds_read_b128 %1, %4\n\tds_read_b128 %2, %4 offset:64\n\ts_waitcnt lgkmcnt(0)"
                 : "=&v"(fl), "=&v"(f0), "=&v"(f1) : "v"(flags), "v"(a) : "memory");
    if (__builtin_amdgcn_readfirstlane(fl[0] & fl[1] & fl[2] & fl[3])) return;
    if (dead || spins > LDS_CAP) { dead = true; return; }
  }
}
template <int N, unsigned ST, bool X, unsigned MASK>
__device__ __forceinline__ void gather(__amdgpu_buffer_rsrc_t rs, unsigned vo, unsigned vlast, unsigned xo, unsigned T, v4u (&v)[N], v4u& x, bool& dead) {
  unsigned pend = (1u << (N + (X ? 1 : 0))) - 1u;
  const unsigned Tm = T & MASK;
  for (unsigned spins = 0;; ++spins) {
    if (X) { if (pend >> N & 1u) x = gld(rs, xo, 0); }
#pragma unroll
    for (int i = 0; i < N; ++i) if (pend >> i & 1u) v[i] = gld(rs, i == N - 1 ? vlast : vo, i * ST);
    if (X) { if (pend >> N & 1u) { if (none(((x[1] ^ T) | (x[3] ^ T)) != 0u)) pend &= ~(1u << N); } }
#pragma unroll
    for (int i = 0; i < N; ++i) if (pend >> i & 1u) {
      v[i] ^= Tm;
      if (none(((v[i][0] | v[i][1] | v[i][2] | v[i][3]) & MASK) != 0u)) pend &= ~(1u << i);
    }
    if (!pend) return;
    if (dead || spins > GLB_CAP) { dead = true; return; }
    __builtin_amdgcn_s_sleep(1);
    asm volatile("" ::: "memory");
  }
}
__device__ __forceinline__ f32x4 asf4(v4u v) { return __builtin_bit_cast(f32x4, v); }

#define STAMP(i) do {} while (0)

__global__ void __launch_bounds__(320, 2)
mk(const unsigned short* __restrict__ x, const void* __restrict__ K, const void* __restrict__ V, const void* __restrict__ wq,
   const void* __restrict__ wo, const void* __restrict__ wg, const void* __restrict__ wu, const void* __restrict__ wd,
   unsigned short* out, unsigned* ws) {
  const int b = blockIdx.x;
  const int lane = threadIdx.x & 63;
  const int wv = __builtin_amdgcn_readfirstlane(threadIdx.x >> 6);
  const int g4 = lane >> 4, c16 = lane & 15;
  STAMP(0);

  if (wv == 0) {
    unsigned oK[4], oV[4], chV[4];
#pragma unroll
    for (int j = 0; j < 4; ++j) {
      oK[j] = g4 * 256 + 16 * (c16 ^ (4 * j + g4));
      chV[j] = 16 * (c16 ^ (8 * (j & 1) + 2 * g4));
      oV[j] = g4 * 256 + chV[j];
    }
    lds_st32(L_CTL + 4 * lane, lane < 32 ? 0u : 0x3F803F80u);
    int u = 0, freed = 0, full = 0;
    bool dead = false;
    auto step = [&](__amdgpu_buffer_rsrc_t rs, unsigned so, unsigned o0, unsigned o1, unsigned o2, unsigned o3) {
      if (u - 32 >= freed) {
        for (unsigned spins = 0;; ++spins) {
          const v4u p = lds_ld128_wait(L_PROG);
          freed = (int)__builtin_amdgcn_readfirstlane(min(min(p[0], p[1]), min(p[2], p[3])));
          if (u - 32 < freed) break;
          if (spins == 0) { waitvm<0>(); full = u; lds_st32(L_FULL, (unsigned)full); }
          if (dead || spins > LDS_CAP) { dead = true; break; }
          __builtin_amdgcn_s_sleep(1);
        }
      }
      dma4(rs, so, (unsigned)(u & 31) * 4096u, o0, o1, o2, o3);
      ++u;
      waitvm<4 * INFL>();
      if (u - INFL > full) { full = u - INFL; lds_st32(L_FULL, (unsigned)full); }
    };
    {
      const __amdgpu_buffer_rsrc_t r = mkrs(wq, 1024u * 4096u * 2u);
      unsigned o[4];
#pragma unroll
      for (int j = 0; j < 4; ++j) o[j] = (4 * j + g4) * 8192 + chV[j] - 1024 * j;
      const unsigned base = (unsigned)(b >> 5) * 128u * 8192u + (unsigned)(b & 31) * 256u;
#pragma unroll 1
      for (int i = 0; i < 8; ++i) {
        dma4(r, base + i * 16u * 8192u, (unsigned)u * 4096u, o[0], o[1], o[2], o[3]);
        ++u;
        if (i == 1) { asm volatile("s_waitcnt lgkmcnt(0)" ::: "memory"); __builtin_amdgcn_s_barrier(); }
        waitvm<4 * INFL0>();
      }
    }
    {
      const __amdgpu_buffer_rsrc_t rk = mkrs(K, 1u << 26), rv = mkrs(V, 1u << 26);
      unsigned so = (unsigned)b << 18;
#pragma unroll 1
      for (int t = 0; t < 32; ++t, so += 8192) {
        step(rk, so, oK[0], oK[1], oK[2], oK[3]);
        step(rk, so + 4096, oK[0], oK[1], oK[2], oK[3]);
        step(rv, so, oV[0], oV[1], oV[2], oV[3]);
        step(rv, so + 4096, oV[0], oV[1], oV[2], oV[3]);
      }
    }
    STAMP(1);
    unsigned o2k[4];
#pragma unroll
    for (int j = 0; j < 4; ++j) o2k[j] = (4 * j + g4) * 2048 + chV[j] - 1024 * j;
    {
      const __amdgpu_buffer_rsrc_t r = mkrs(wo, 4096u * 1024u * 2u);
      const unsigned base = (unsigned)(b & 63) * 64u * 2048u + (unsigned)(b >> 6) * 512u;
#pragma unroll 1
      for (int i = 0; i < 8; ++i) step(r, base + (i >> 1) * 16u * 2048u + (i & 1) * 256u, o2k[0], o2k[1], o2k[2], o2k[3]);
    }
    STAMP(2);
    if (b < 224) {
      unsigned o[4];
#pragma unroll
      for (int j = 0; j < 4; ++j) o[j] = (4 * j + g4) * 7168 + chV[j] - 1024 * j;
      const unsigned base = (unsigned)(b & 15) * 64u * 7168u + (unsigned)(b >> 4) * 512u;
      const __amdgpu_buffer_rsrc_t rg = mkrs(wg, 1024u * 3584u * 2u), ru = mkrs(wu, 1024u * 3584u * 2u);
#pragma unroll 1
      for (int i = 0; i < 8; ++i) step(rg, base + (i >> 1) * 16u * 7168u + (i & 1) * 256u, o[0], o[1], o[2], o[3]);
#pragma unroll 1
      for (int i = 0; i < 8; ++i) step(ru, base + (i >> 1) * 16u * 7168u + (i & 1) * 256u, o[0], o[1], o[2], o[3]);
      STAMP(3);
      const __amdgpu_buffer_rsrc_t rd = mkrs(wd, 3584u * 1024u * 2u);
      const unsigned cbD = (unsigned)b / 56u, rbD = (unsigned)b - 56u * cbD;
      const unsigned bd = rbD * 64u * 2048u + cbD * 512u;
#pragma unroll 1
      for (int i = 0; i < 8; ++i) step(rd, bd + (i >> 1) * 16u * 2048u + (i & 1) * 256u, o2k[0], o2k[1], o2k[2], o2k[3]);
    }
    STAMP(4);
    if (INFL > 4) { waitvm<16>(); if (u - 4 > full) { full = u - 4; lds_st32(L_FULL, (unsigned)full); } }
    waitvm<12>(); if (u - 3 > full) { full = u - 3; lds_st32(L_FULL, (unsigned)full); }
    waitvm<8>(); if (u - 2 > full) { full = u - 2; lds_st32(L_FULL, (unsigned)full); }
    waitvm<4>(); if (u - 1 > full) { full = u - 1; lds_st32(L_FULL, (unsigned)full); }
    waitvm<0>(); lds_st32(L_FULL, (unsigned)u);
    STAMP(5);
    return;
  }

  const int w = wv - 1;
  const __amdgpu_buffer_rsrc_t rs = mkrs(ws, WS_END);
  const unsigned T = *(const GAS unsigned*)ws + 1u;
  bool dead = false;
  int seen = 0;
  const int sub = lane >> 2, p4 = lane & 3;
  const unsigned trrow = 256u * sub + 8u * p4;
  const int r16 = b & 15;
  float xres = 0.f;

  {
    const v4u xa = *(const GAS v4u*)(x + 16 * lane), xb = *(const GAS v4u*)(x + 16 * lane + 8);
    const unsigned xp = *(const GAS unsigned*)(x + 128 * (b >> 5) + 2 * lane);
    xres = lo16(x[64 * r16 + 16 * g4 + 4 * w + p4]);
    __builtin_amdgcn_s_barrier();
    float s = 0.f;
#pragma unroll
    for (int e = 0; e < 4; ++e) s += (lo16(xa[e]) + hi16(xa[e])) + (lo16(xb[e]) + hi16(xb[e]));
    const float mean = xrow_sum(row_sum(s)) * (1.f / 1024.f);
    float q = 0.f;
#pragma unroll
    for (int e = 0; e < 4; ++e) {
      const float d0 = lo16(xa[e]) - mean, d1 = hi16(xa[e]) - mean, d2 = lo16(xb[e]) - mean, d3 = hi16(xb[e]) - mean;
      q += (d0 * d0 + d1 * d1) + (d2 * d2 + d3 * d3);
    }
    const float var = xrow_sum(row_sum(q)) * (1.f / 1024.f);
    const float mb = rbf(mean), sd = rbf(sqrtf(rbf(rbf(var) + 1e-5f)));
    const unsigned xn = pk2(rbf(lo16(xp) - mb) / sd, rbf(hi16(xp) - mb) / sd);
    lds_st32(L_XN + w * 256 + (lane >> 4) * 64 + ((lane >> 1) & 3) * 16 + ((lane >> 3) & 1) * 8 + (lane & 1) * 4, xn);
  }
  STAMP(1);
  {
    asm volatile("s_waitcnt lgkmcnt(0)" ::: "memory");
    v4u xf[4];
#pragma unroll
    for (int ks = 0; ks < 4; ++ks) xf[ks] = lds128(L_XN + w * 256 + ks * 64 + g4 * 16);
    wait_full(8, seen, dead);
    f32x4 d[2];
#pragma unroll
    for (int i = 0; i < 2; ++i) {
      const unsigned a = trrow + 32u * ((2 * w + i) ^ (sub & 7));
      d[i] = f32x4{0.f, 0.f, 0.f, 0.f};
#pragma unroll
      for (int ks = 0; ks < 4; ++ks) d[i] = mfma(ldstr2(a + ks * 8192, a + ks * 8192 + 4096), xf[ks], d[i]);
    }
    const f32x4 v = (c16 & 1) ? d[1] : d[0];
    if (c16 < 2) gst(rs, WS_QP + ((b & 31) * 8 + (b >> 5)) * 512 + (32 * w + 16 * c16 + 4 * g4) * 4, tag4(v, T));
    asm volatile("s_waitcnt lgkmcnt(0)" ::: "memory");
    lds_st32(L_PROG + 4 * w, 8u);
  }
  STAMP(2);
  {
    v4u v[1], dummy;
    const unsigned vo = WS_QP + ((b & 31) * 8 + (lane >> 3)) * 512 + w * 128 + (lane & 7) * 16;
    gather<1, 0, false, 0xFFu>(rs, vo, vo, 0, T, v, dummy, dead);
    v4u r;
#pragma unroll
    for (int e = 0; e < 4; ++e) {
      float a = asf(v[0][e]);
      a += dpp<ROR8>(a);
      a = xrow_sum(a);
      r[e] = (pk2(a, a) << 16) | (T & 0xFFFFu);
    }
    if (lane < 8) gst(rs, WS_QF + ((b >> 5) * 32 + (b & 31)) * 512 + w * 128 + lane * 16, r);
  }
  STAMP(3);
  v4u qf[4];
  {
    v4u v[1], dummy;
    const unsigned vo = WS_QF + ((b & 7) * 32 + (b >> 6) * 8 + 2 * w) * 512 + lane * 16;
    gather<1, 0, false, 0xFFFFu>(rs, vo, vo, 0, T, v, dummy, dead);
    lds_st64(L_Q + w * 512 + lane * 8, v2u{(v[0][0] >> 16) | v[0][1], (v[0][2] >> 16) | v[0][3]});
    lds_st32(L_XF + 4 * w, 1u);
    exch(L_XF, L_Q + (c16 & 7) * 256 + g4 * 16, qf[0], qf[1], dead);
    qf[2] = lds128(L_Q + (c16 & 7) * 256 + g4 * 16 + 128);
    qf[3] = lds128(L_Q + (c16 & 7) * 256 + g4 * 16 + 192);
  }
  STAMP(4);

  {
    unsigned ka[4], va[2];
#pragma unroll
    for (int u = 0; u < 4; ++u) ka[u] = 256u * c16 + 16u * ((4 * u + g4) ^ c16);
#pragma unroll
    for (int i = 0; i < 2; ++i) va[i] = 8192u + trrow + 32u * ((2 * w + i) ^ (sub & 7));
    const bool up = c16 >= 8;
    const float LOG2E = 1.4426950408889634f, scale = 0.08838834764831845f;
    float m = -INFINITY, l = 0.f;
    f32x4 acc[2] = {f32x4{0.f, 0.f, 0.f, 0.f}, f32x4{0.f, 0.f, 0.f, 0.f}};
#pragma unroll 1
    for (int t = 0; t < 32; ++t) {
      const int u0 = U_KV + 4 * t;
      const unsigned tb = (unsigned)(u0 & 31) * 4096u;
      wait_full(u0 + 2, seen, dead);
      f32x4 s0 = {0.f, 0.f, 0.f, 0.f}, s1 = {0.f, 0.f, 0.f, 0.f};
#pragma unroll
      for (int u = 0; u < 4; ++u) {
        s0 = mfma(lds128(tb + ka[u]), qf[u], s0);
        s1 = mfma(lds128(tb + ka[u] + 4096), qf[u], s1);
      }
      float s[4];
#pragma unroll
      for (int r = 0; r < 4; ++r) s[r] = rbf(rbf(up ? s1[r] : s0[r]) * scale);
      const float mn = fmaxf(m, xrow_max(fmaxf(fmaxf(s[0], s[1]), fmaxf(s[2], s[3]))));
      const float al = __builtin_amdgcn_exp2f((m - mn) * LOG2E);
      m = mn;
      float p[4];
#pragma unroll
      for (int r = 0; r < 4; ++r) p[r] = __builtin_amdgcn_exp2f((s[r] - mn) * LOG2E);
      l = l * al + ((p[0] + p[1]) + (p[2] + p[3]));
      const unsigned p01 = pk2(p[0], p[1]), p23 = pk2(p[2], p[3]);
      const v4u pb = up ? v4u{0u, 0u, p01, p23} : v4u{p01, p23, 0u, 0u};
      acc[0] *= al; acc[1] *= al;
      wait_full(u0 + 4, seen, dead);
      const v4u v0 = ldstr2(tb + va[0], tb + va[0] + 4096), v1 = ldstr2(tb + va[1], tb + va[1] + 4096);
      acc[0] = mfma(v0, pb, acc[0]);
      acc[1] = mfma(v1, pb, acc[1]);
      asm volatile("s_waitcnt lgkmcnt(0)" ::: "memory");
      lds_st32(L_PROG + 4 * w, (unsigned)(u0 + 4));
    }
    STAMP(5);
    const float M = fmaxf(m, dpp<ROR8>(m));
    const float f = __builtin_amdgcn_exp2f((m - M) * LOG2E);
    float lt = xrow_sum(l) * f;
    lt += dpp<ROR8>(lt);
    f32x4 o;
#pragma unroll
    for (int e = 0; e < 4; ++e) {
      const float a0 = acc[0][e] * f, a1 = acc[1][e] * f;
      o[e] = (up ? a1 : a0) + dpp<ROR8>(up ? a0 : a1);
    }
    const int hh = (b >> 6) * 8 + (c16 & 7);
    gst(rs, WS_PA + ((hh * 2 + (w >> 1)) * 64 + (b & 63)) * 256 + (32 * (w & 1) + 16 * (c16 >> 3) + 4 * g4) * 4, tag4(o, T));
    if (w == 0 && lane < 8) gst(rs, WS_ML + (hh * 64 + (b & 63)) * 16, v4u{asu(M), T, asu(lt), T});
  }
  STAMP(6);

  unsigned wa[4];
#pragma unroll
  for (int i = 0; i < 4; ++i) wa[i] = (w >> 1) * 4096u + trrow + 32u * ((4 * (w & 1) + i) ^ (sub & 7));
  const unsigned gvo = sub * 256u + w * 64u + p4 * 16u;

  {
    v4u v[4], ml;
    const unsigned vo = WS_PA + (b & 63) * 16384u + gvo;
    gather<4, 4096, true, 0xFFu>(rs, vo, vo, WS_ML + (((b & 63) >> 1) * 64 + 16 * p4 + sub) * 16, T, v, ml, dead);
    STAMP(7);
    const float LOG2E = 1.4426950408889634f;
    const float ms = asf(ml[0]);
    const float M = xrow_max(row_max(ms));
    const float f = __builtin_amdgcn_exp2f((ms - M) * LOG2E);
    const float L = xrow_sum(row_sum(f * asf(ml[2])));
    f32x4 a = asf4(v[0]) * dpp<0x00>(f);
    a += asf4(v[1]) * dpp<0x55>(f);
    a += asf4(v[2]) * dpp<0xAA>(f);
    a += asf4(v[3]) * dpp<0xFF>(f);
    const float o = red_scatter(a) / L;
    if (c16 < 4) lds_st16(L_VEC + (w >> 1) * 64 + p4 * 16 + (w & 1) * 8 + g4 * 2, pk2(o, o));
    lds_st32(L_XF + 16 + 4 * w, 1u);
    wait_full(U_G, seen, dead);
    v4u wf[2][4];
#pragma unroll
    for (int ks = 0; ks < 2; ++ks)
#pragma unroll
      for (int i = 0; i < 4; ++i) {
        const unsigned a0 = wa[i] + ((U_WO & 31) + 4 * ks) * 4096u;
        wf[ks][i] = ldstr2(a0, a0 + 8192);
      }
    v4u f0, f1;
    exch(L_XF + 16, L_VEC + g4 * 16, f0, f1, dead);
    f32x4 r;
#pragma unroll
    for (int i = 0; i < 4; ++i) {
      f32x4 d = mfma(f0, wf[0][i], f32x4{0.f, 0.f, 0.f, 0.f});
      d = mfma(f1, wf[1][i], d);
      r[i] = d[0];
    }
    if (lane < 16) gst(rs, WS_PB + ((4 * (b >> 6) + w) * 64 + (b & 63)) * 256 + lane * 16, tag4(r, T));
  }
  STAMP(8);

  if (b < 240) {
    float h;
    {
      v4u v[4], dummy;
      const unsigned vo = WS_PB + r16 * 16384u + gvo;
      gather<4, 4096, false, 0xFFu>(rs, vo, vo, 0, T, v, dummy, dead);
      STAMP(9);
      const f32x4 a = (asf4(v[0]) + asf4(v[1])) + (asf4(v[2]) + asf4(v[3]));
      h = rbf(xres + rbf(red_scatter(a)));
    }
    if (b < 224) {
      {
        if (c16 < 4) lds_st16(L_VEC + 128 + (g4 >> 1) * 64 + w * 16 + (g4 & 1) * 8 + p4 * 2, asu(h) >> 16);
        lds_st32(L_XF + 32 + 4 * w, 1u);
        wait_full(U_D, seen, dead);
        v4u gf[2][4], uf[2][4];
#pragma unroll
        for (int ks = 0; ks < 2; ++ks)
#pragma unroll
          for (int i = 0; i < 4; ++i) {
            const unsigned a0 = wa[i] + ((U_G & 31) + 4 * ks) * 4096u, a1 = wa[i] + ((U_U & 31) + 4 * ks) * 4096u;
            gf[ks][i] = ldstr2(a0, a0 + 8192);
            uf[ks][i] = ldstr2(a1, a1 + 8192);
          }
        v4u f0, f1;
        exch(L_XF + 32, (c16 & 1) ? L_ONES + g4 * 16 : L_VEC + 128 + g4 * 16, f0, f1, dead);
        f32x4 r = {0.f, 0.f, 0.f, 0.f};
        f32x4 st = mfma(f0, f0, f32x4{0.f, 0.f, 0.f, 0.f});
        st = mfma(f1, f1, st);
#pragma unroll
        for (int i = 0; i < 4; ++i) {
          f32x4 dg = mfma(f0, gf[0][i], f32x4{0.f, 0.f, 0.f, 0.f});
          dg = mfma(f1, gf[1][i], dg);
          f32x4 du = mfma(f0, uf[0][i], f32x4{0.f, 0.f, 0.f, 0.f});
          du = mfma(f1, uf[1][i], du);
          if (i == 0 || g4 == i) r = f32x4{dg[0], dg[1], du[0], du[1]};
        }
        const unsigned rb = 4 * (b >> 4) + w;
        if (lane == 0) gst(rs, WS_ST + (rb * 16 + r16) * 16, v4u{asu(st[1]), T, asu(st[0]), T});
        gst(rs, WS_PC + ((rb * 16 + r16) * 64 + lane) * 16, tag4(r, T));
      }
      STAMP(10);
      {
        const unsigned cb = (unsigned)b / 56u, rb = (unsigned)b - 56u * cb;
        v4u v[4], sv;
        const unsigned vo = WS_PC + ((rb * 16 + g4) * 64 + 16 * w + c16) * 16;
        gather<4, 4096, true, 0xFFu>(rs, vo, vo, WS_ST + (rb * 16 + c16) * 16, T, v, sv, dead);
        STAMP(11);
        const float mean = row_sum(asf(sv[0])) * (1.f / 1024.f);
        const float var = fmaxf(row_sum(asf(sv[2])) * (1.f / 1024.f) - mean * mean, 0.f);
        const float mb = rbf(mean), sd = rbf(sqrtf(rbf(rbf(var) + 1e-5f)));
        const f32x4 a = (asf4(v[0]) + asf4(v[1])) + (asf4(v[2]) + asf4(v[3]));
        const float G = xrow_sum(a[0] - mb * a[1]), U = xrow_sum(a[2] - mb * a[3]);
        const float g = rbf(G / sd), u = rbf(U / sd);
        const float y = 0.7978845608028654f * (g + 0.044715f * g * g * g);
        const float ge = rbf(g / (1.f + __builtin_amdgcn_exp2f(y * (-2.f * 1.4426950408889634f))));
        const float act = ge * u;
        if (lane < 16) lds_st16(L_VEC + 256 + (w >> 1) * 64 + (c16 >> 2) * 16 + (w & 1) * 8 + (c16 & 3) * 2, pk2(act, act));
        lds_st32(L_XF + 48 + 4 * w, 1u);
        wait_full(U_END, seen, dead);
        v4u wf[2][4];
#pragma unroll
        for (int ks = 0; ks < 2; ++ks)
#pragma unroll
          for (int i = 0; i < 4; ++i) {
            const unsigned a0 = wa[i] + ((U_D & 31) + 4 * ks) * 4096u;
            wf[ks][i] = ldstr2(a0, a0 + 8192);
          }
        v4u f0, f1;
        exch(L_XF + 48, L_VEC + 256 + g4 * 16, f0, f1, dead);
        f32x4 r;
#pragma unroll
        for (int i = 0; i < 4; ++i) {
          f32x4 d = mfma(f0, wf[0][i], f32x4{0.f, 0.f, 0.f, 0.f});
          d = mfma(f1, wf[1][i], d);
          r[i] = d[0];
        }
        if (lane < 16) gst(rs, WS_PD + ((4 * cb + w) * 64 + rb) * 256 + lane * 16, tag4(r, T));
      }
      STAMP(12);
    } else {
      v4u v[4], dummy;
      const unsigned vo = WS_PD + r16 * 16384u + gvo;
      gather<4, 4096, false, 0xFFu>(rs, vo, vo - (sub >= 8 ? 2048u : 0u), 0, T, v, dummy, dead);
      STAMP(11);
      if (sub >= 8) v[3] = v4u{0u, 0u, 0u, 0u};
      const f32x4 a = (asf4(v[0]) + asf4(v[1])) + (asf4(v[2]) + asf4(v[3]));
      const float res = h + rbf(red_scatter(a));
      if (c16 < 4) out[64 * r16 + 16 * g4 + 4 * w + p4] = (unsigned short)pk2(res, res);
      if (b == 224 && w == 0 && lane == 0) __hip_atomic_store((GAS unsigned*)ws, T, __ATOMIC_RELAXED, __HIP_MEMORY_SCOPE_AGENT);
      STAMP(12);
    }
  }
}

extern "C" void kernel_launch(void* const* d_in, const int* in_sizes, int n_in, void* d_out, int out_size, void* d_ws,
                              size_t ws_size, hipStream_t stream) {
  (void)in_sizes; (void)n_in; (void)out_size; (void)ws_size;
  (void)hipFuncSetAttribute((const void*)mk, hipFuncAttributeMaxDynamicSharedMemorySize, (int)LDS_BYTES);
  mk<<<256, 320, LDS_BYTES, stream>>>((const unsigned short*)d_in[0], d_in[1], d_in[2], d_in[3], d_in[4], d_in[6], d_in[7], d_in[8],
                                      (unsigned short*)d_out, (unsigned*)d_ws);
}
```

```cpp
#define PROBE 0
#include <hip/hip_runtime.h>
#include <stdint.h>

typedef unsigned v4u __attribute__((ext_vector_type(4)));
typedef unsigned v2u __attribute__((ext_vector_type(2)));
typedef float f32x4 __attribute__((ext_vector_type(4)));
typedef float f32x2 __attribute__((ext_vector_type(2)));
typedef __bf16 bf16x8_t __attribute__((ext_vector_type(8)));
typedef __bf16 bf16x2_t __attribute__((ext_vector_type(2)));
typedef short s16x4 __attribute__((ext_vector_type(4)));
#define LAS __attribute__((address_space(3)))
#define GAS __attribute__((address_space(1)))

constexpr unsigned WS_QP = 0x010000;
constexpr unsigned WS_QF = 0x030000;
constexpr unsigned WS_ML = 0x050000;
constexpr unsigned WS_ST = 0x058000;
constexpr unsigned WS_PA = 0x100000;
constexpr unsigned WS_PB = 0x200000;
constexpr unsigned WS_PC = 0x280000;
constexpr unsigned WS_PD = 0x380000;
constexpr unsigned WS_END = 0x400000;

constexpr unsigned L_CTL = 131072;
constexpr unsigned L_FULL = L_CTL;
constexpr unsigned L_PROG = L_CTL + 16;
constexpr unsigned L_XF = L_CTL + 32;
constexpr unsigned L_ONES = L_CTL + 128;
constexpr unsigned L_VEC = L_CTL + 256;
constexpr unsigned L_XN = L_CTL + 1024;
constexpr unsigned L_Q = L_CTL + 2048;
constexpr unsigned LDS_BYTES = L_CTL + 4096;

constexpr int U_KV = 8, U_WO = 136, U_G = 144, U_U = 152, U_D = 160, U_END = 168;
constexpr unsigned LDS_CAP = 1u << 21, GLB_CAP = 1u << 16;
#define INFL 4
#define INFL0 6

__device__ __forceinline__ float asf(unsigned u) { return __builtin_bit_cast(float, u); }
__device__ __forceinline__ unsigned asu(float f) { return __builtin_bit_cast(unsigned, f); }
__device__ __forceinline__ unsigned pk2(float lo, float hi) {
  return __builtin_bit_cast(unsigned, __builtin_convertvector(f32x2{lo, hi}, bf16x2_t));
}
__device__ __forceinline__ float rbf(float f) { return asf(pk2(f, f) << 16); }
__device__ __forceinline__ float lo16(unsigned u) { return asf(u << 16); }
__device__ __forceinline__ float hi16(unsigned u) { return asf(u & 0xFFFF0000u); }
__device__ __forceinline__ f32x4 mfma(v4u a, v4u b, f32x4 c) {
  return __builtin_amdgcn_mfma_f32_16x16x32_bf16(__builtin_bit_cast(bf16x8_t, a), __builtin_bit_cast(bf16x8_t, b), c, 0, 0, 0);
}
template <int CTRL> __device__ __forceinline__ float dpp(float x) {
  return asf((unsigned)__builtin_amdgcn_mov_dpp((int)asu(x), CTRL, 0xf, 0xf, true));
}
constexpr int XOR1 = 0xB1, XOR2 = 0x4E, ROR4 = 0x124, ROR8 = 0x128;
__device__ __forceinline__ float xrow_max(float x) {
  auto s = __builtin_amdgcn_permlane16_swap(asu(x), asu(x), false, false);
  x = fmaxf(asf(s[0]), asf(s[1]));
  auto t = __builtin_amdgcn_permlane32_swap(asu(x), asu(x), false, false);
  return fmaxf(asf(t[0]), asf(t[1]));
}
__device__ __forceinline__ float xrow_sum(float x) {
  auto s = __builtin_amdgcn_permlane16_swap(asu(x), asu(x), false, false);
  x = asf(s[0]) + asf(s[1]);
  auto t = __builtin_amdgcn_permlane32_swap(asu(x), asu(x), false, false);
  return asf(t[0]) + asf(t[1]);
}
__device__ __forceinline__ float row_sum(float x) {
  x += dpp<XOR1>(x); x += dpp<XOR2>(x); x += dpp<ROR4>(x); x += dpp<ROR8>(x); return x;
}
__device__ __forceinline__ float row_max(float x) {
  x = fmaxf(x, dpp<XOR1>(x)); x = fmaxf(x, dpp<XOR2>(x)); x = fmaxf(x, dpp<ROR4>(x)); x = fmaxf(x, dpp<ROR8>(x)); return x;
}
__device__ __forceinline__ float red_scatter(f32x4 a) {
  auto s0 = __builtin_amdgcn_permlane32_swap(asu(a[0]), asu(a[2]), false, false);
  auto s1 = __builtin_amdgcn_permlane32_swap(asu(a[1]), asu(a[3]), false, false);
  const float b0 = asf(s0[0]) + asf(s0[1]), b1 = asf(s1[0]) + asf(s1[1]);
  auto t = __builtin_amdgcn_permlane16_swap(asu(b0), asu(b1), false, false);
  float c = asf(t[0]) + asf(t[1]);
  c += dpp<ROR4>(c); c += dpp<ROR8>(c);
  return c;
}
template <int N> __device__ __forceinline__ void waitvm() { asm volatile("s_waitcnt vmcnt(%0)" ::"n"(N) : "memory"); }
__device__ __forceinline__ void lds_st32(unsigned a, unsigned v) { asm volatile("ds_write_b32 %0, %1" ::"v"(a), "v"(v) : "memory"); }
__device__ __forceinline__ void lds_st16(unsigned a, unsigned v) { asm volatile("ds_write_b16 %0, %1" ::"v"(a), "v"(v) : "memory"); }
__device__ __forceinline__ void lds_st64(unsigned a, v2u v) { asm volatile("ds_write_b64 %0, %1" ::"v"(a), "v"(v) : "memory"); }
__device__ __forceinline__ unsigned lds_ld32_wait(unsigned a) {
  unsigned r; asm volatile("ds_read_b32 %0, %1\n\ts_waitcnt lgkmcnt(0)" : "=v"(r) : "v"(a) : "memory"); return r;
}
__device__ __forceinline__ v4u lds_ld128_wait(unsigned a) {
  v4u r; asm volatile("ds_read_b128 %0, %1\n\ts_waitcnt lgkmcnt(0)" : "=v"(r) : "v"(a) : "memory"); return r;
}
__device__ __forceinline__ v4u lds128(unsigned a) { return *(const LAS v4u*)(uintptr_t)a; }
__device__ __forceinline__ v2u ldstr(unsigned a) {
  return __builtin_bit_cast(v2u, __builtin_amdgcn_ds_read_tr16_b64_v4i16((LAS s16x4*)(uintptr_t)a));
}
__device__ __forceinline__ v4u ldstr2(unsigned a0, unsigned a1) {
  const v2u x = ldstr(a0), y = ldstr(a1); return v4u{x[0], x[1], y[0], y[1]};
}
__device__ __forceinline__ void dma4(__amdgpu_buffer_rsrc_t r, unsigned so, unsigned m, unsigned o0, unsigned o1, unsigned o2, unsigned o3) {
  asm volatile("s_mov_b32 m0, %0\n\ts_nop 0\n\t"
               "buffer_load_dwordx4 %1, %5, %6 offen nt lds\n\t"
               "buffer_load_dwordx4 %2, %5, %6 offen offset:1024 nt lds\n\t"
               "buffer_load_dwordx4 %3, %5, %6 offen offset:2048 nt lds\n\t"
               "buffer_load_dwordx4 %4, %5, %6 offen offset:3072 nt lds"
               ::"s"(m), "v"(o0), "v"(o1), "v"(o2), "v"(o3), "s"(r), "s"(so) : "memory");
}
__device__ __forceinline__ __amdgpu_buffer_rsrc_t mkrs(const void* p, unsigned bytes) {
  return __builtin_amdgcn_make_buffer_rsrc((void*)p, (short)0, (int)bytes, 0x00020000);
}
__device__ __forceinline__ v4u gld(__amdgpu_buffer_rsrc_t rs, unsigned vo, unsigned so) { return __builtin_amdgcn_raw_buffer_load_b128(rs, (int)vo, (int)so, 16); }
__device__ __forceinline__ void gst(__amdgpu_buffer_rsrc_t rs, unsigned vo, v4u v) { __builtin_amdgcn_raw_buffer_store_b128(v, rs, (int)vo, 0, 16); }
__device__ __forceinline__ v4u tag4(f32x4 a, unsigned T) {
  v4u r;
#pragma unroll
  for (int e = 0; e < 4; ++e) r[e] = (asu(a[e]) & ~0xFFu) | (T & 0xFFu);
  return r;
}
__device__ __forceinline__ bool none(bool p) { return __builtin_amdgcn_ballot_w64(p) == 0; }

__device__ __forceinline__ void wait_full(int need, int& seen, bool& dead) {
  if (seen >= need) return;
  for (unsigned spins = 0;; ++spins) {
    seen = (int)__builtin_amdgcn_readfirstlane(lds_ld32_wait(L_FULL));
    if (seen >= need) return;
    if (dead || spins > LDS_CAP) { dead = true; return; }
    __builtin_amdgcn_s_sleep(1);
  }
}
__device__ __forceinline__ void exch(unsigned flags, unsigned a, v4u& f0, v4u& f1, bool& dead) {
  for (unsigned spins = 0;; ++spins) {
    v4u fl;
    asm volatile("ds_read_b128 %0, %3\n\tds_read_b128 %1, %4\n\tds_read_b128 %2, %4 offset:64\n\ts_waitcnt lgkmcnt(0)"
                 : "=&v"(fl), "=&v"(f0), "=&v"(f1) : "v"(flags), "v"(a) : "memory");
    if (__builtin_amdgcn_readfirstlane(fl[0] & fl[1] & fl[2] & fl[3])) return;
    if (dead || spins > LDS_CAP) { dead = true; return; }
  }
}
template <int N, unsigned ST, bool X, unsigned MASK>
__device__ __forceinline__ void gather(__amdgpu_buffer_rsrc_t rs, unsigned vo, unsigned vlast, unsigned xo, unsigned T, v4u (&v)[N], v4u& x, bool& dead) {
  unsigned pend = (1u << (N + (X ? 1 : 0))) - 1u;
  const unsigned Tm = T & MASK;
  for (unsigned spins = 0;; ++spins) {
    if (X) { if (pend >> N & 1u) x = gld(rs, xo, 0); }
#pragma unroll
    for (int i = 0; i < N; ++i) if (pend >> i & 1u) v[i] = gld(rs, i == N - 1 ? vlast : vo, i * ST);
    if (X) { if (pend >> N & 1u) { if (none(((x[1] ^ T) | (x[3] ^ T)) != 0u)) pend &= ~(1u << N); } }
#pragma unroll
    for (int i = 0; i < N; ++i) if (pend >> i & 1u) {
      v[i] ^= Tm;
      if (none(((v[i][0] | v[i][1] | v[i][2] | v[i][3]) & MASK) != 0u)) pend &= ~(1u << i);
    }
    if (!pend) return;
    if (dead || spins > GLB_CAP) { dead = true; return; }
    __builtin_amdgcn_s_sleep(1);
    asm volatile("" ::: "memory");
  }
}
__device__ __forceinline__ f32x4 asf4(v4u v) { return __builtin_bit_cast(f32x4, v); }

#define STAMP(i) do { ts[i] = __builtin_amdgcn_s_memrealtime(); } while (0)

__global__ void __launch_bounds__(320, 2)
mk(const unsigned short* __restrict__ x, const void* __restrict__ K, const void* __restrict__ V, const void* __restrict__ wq,
   const void* __restrict__ wo, const void* __restrict__ wg, const void* __restrict__ wu, const void* __restrict__ wd,
   unsigned short* out, unsigned* ws) {
  const int b = blockIdx.x;
  const int lane = threadIdx.x & 63;
  const int wv = __builtin_amdgcn_readfirstlane(threadIdx.x >> 6);
  const int g4 = lane >> 4, c16 = lane & 15;
  unsigned long long ts[16];
#pragma unroll
  for (int i = 0; i < 16; ++i) ts[i] = 0;
  STAMP(0);

  if (wv == 0) {
    unsigned oK[4], oV[4], chV[4];
#pragma unroll
    for (int j = 0; j < 4; ++j) {
      oK[j] = g4 * 256 + 16 * (c16 ^ (4 * j + g4));
      chV[j] = 16 * (c16 ^ (8 * (j & 1) + 2 * g4));
      oV[j] = g4 * 256 + chV[j];
    }
    lds_st32(L_CTL + 4 * lane, lane < 32 ? 0u : 0x3F803F80u);
    int u = 0, freed = 0, full = 0;
    bool dead = false;
    auto step = [&](__amdgpu_buffer_rsrc_t rs, unsigned so, unsigned o0, unsigned o1, unsigned o2, unsigned o3) {
      if (u - 32 >= freed) {
        for (unsigned spins = 0;; ++spins) {
          const v4u p = lds_ld128_wait(L_PROG);
          freed = (int)__builtin_amdgcn_readfirstlane(min(min(p[0], p[1]), min(p[2], p[3])));
          if (u - 32 < freed) break;
          if (spins == 0) { waitvm<0>(); full = u; lds_st32(L_FULL, (unsigned)full); }
          if (dead || spins > LDS_CAP) { dead = true; break; }
          __builtin_amdgcn_s_sleep(1);
        }
      }
      dma4(rs, so, (unsigned)(u & 31) * 4096u, o0, o1, o2, o3);
      ++u;
      waitvm<4 * INFL>();
      if (u - INFL > full) { full = u - INFL; lds_st32(L_FULL, (unsigned)full); }
    };
    {
      const __amdgpu_buffer_rsrc_t r = mkrs(wq, 1024u * 4096u * 2u);
      unsigned o[4];
#pragma unroll
      for (int j = 0; j < 4; ++j) o[j] = (4 * j + g4) * 8192 + chV[j] - 1024 * j;
      const unsigned base = (unsigned)(b >> 5) * 128u * 8192u + (unsigned)(b & 31) * 256u;
#pragma unroll 1
      for (int i = 0; i < 8; ++i) {
        dma4(r, base + i * 16u * 8192u, (unsigned)u * 4096u, o[0], o[1], o[2], o[3]);
        ++u;
        if (i == 1) { asm volatile("s_waitcnt lgkmcnt(0)" ::: "memory"); __builtin_amdgcn_s_barrier(); }
        waitvm<4 * INFL0>();
      }
    }
    {
      const __amdgpu_buffer_rsrc_t rk = mkrs(K, 1u << 26), rv = mkrs(V, 1u << 26);
      unsigned so = (unsigned)b << 18;
#pragma unroll 1
      for (int t = 0; t < 32; ++t, so += 8192) {
        step(rk, so, oK[0], oK[1], oK[2], oK[3]);
        step(rk, so + 4096, oK[0], oK[1], oK[2], oK[3]);
        step(rv, so, oV[0], oV[1], oV[2], oV[3]);
        step(rv, so + 4096, oV[0], oV[1], oV[2], oV[3]);
      }
    }
    STAMP(1);
    unsigned o2k[4];
#pragma unroll
    for (int j = 0; j < 4; ++j) o2k[j] = (4 * j + g4) * 2048 + chV[j] - 1024 * j;
    {
      const __amdgpu_buffer_rsrc_t r = mkrs(wo, 4096u * 1024u * 2u);
      const unsigned base = (unsigned)(b & 63) * 64u * 2048u + (unsigned)(b >> 6) * 512u;
#pragma unroll 1
      for (int i = 0; i < 8; ++i) step(r, base + (i >> 1) * 16u * 2048u + (i & 1) * 256u, o2k[0], o2k[1], o2k[2], o2k[3]);
    }
    STAMP(2);
    if (b < 224) {
      unsigned o[4];
#pragma unroll
      for (int j = 0; j < 4; ++j) o[j] = (4 * j + g4) * 7168 + chV[j] - 1024 * j;
      const unsigned base = (unsigned)(b & 15) * 64u * 7168u + (unsigned)(b >> 4) * 512u;
      const __amdgpu_buffer_rsrc_t rg = mkrs(wg, 1024u * 3584u * 2u), ru = mkrs(wu, 1024u * 3584u * 2u);
#pragma unroll 1
      for (int i = 0; i < 8; ++i) step(rg, base + (i >> 1) * 16u * 7168u + (i & 1) * 256u, o[0], o[1], o[2], o[3]);
#pragma unroll 1
      for (int i = 0; i < 8; ++i) step(ru, base + (i >> 1) * 16u * 7168u + (i & 1) * 256u, o[0], o[1], o[2], o[3]);
      STAMP(3);
      const __amdgpu_buffer_rsrc_t rd = mkrs(wd, 3584u * 1024u * 2u);
      const unsigned cbD = (unsigned)b / 56u, rbD = (unsigned)b - 56u * cbD;
      const unsigned bd = rbD * 64u * 2048u + cbD * 512u;
#pragma unroll 1
      for (int i = 0; i < 8; ++i) step(rd, bd + (i >> 1) * 16u * 2048u + (i & 1) * 256u, o2k[0], o2k[1], o2k[2], o2k[3]);
    }
    STAMP(4);
    if (INFL > 4) { waitvm<16>(); if (u - 4 > full) { full = u - 4; lds_st32(L_FULL, (unsigned)full); } }
    waitvm<12>(); if (u - 3 > full) { full = u - 3; lds_st32(L_FULL, (unsigned)full); }
    waitvm<8>(); if (u - 2 > full) { full = u - 2; lds_st32(L_FULL, (unsigned)full); }
    waitvm<4>(); if (u - 1 > full) { full = u - 1; lds_st32(L_FULL, (unsigned)full); }
    waitvm<0>(); lds_st32(L_FULL, (unsigned)u);
    STAMP(5);
    return;
  }

  const int w = wv - 1;
  const __amdgpu_buffer_rsrc_t rs = mkrs(ws, WS_END);
  const unsigned T = *(const GAS unsigned*)ws + 1u;
  bool dead = false;
  int seen = 0;
  const int sub = lane >> 2, p4 = lane & 3;
  const unsigned trrow = 256u * sub + 8u * p4;
  const int r16 = b & 15;
  float xres = 0.f;

  {
    const v4u xa = *(const GAS v4u*)(x + 16 * lane), xb = *(const GAS v4u*)(x + 16 * lane + 8);
    const unsigned xp = *(const GAS unsigned*)(x + 128 * (b >> 5) + 2 * lane);
    xres = lo16(x[64 * r16 + 16 * g4 + 4 * w + p4]);
    __builtin_amdgcn_s_barrier();
    float s = 0.f;
#pragma unroll
    for (int e = 0; e < 4; ++e) s += (lo16(xa[e]) + hi16(xa[e])) + (lo16(xb[e]) + hi16(xb[e]));
    const float mean = xrow_sum(row_sum(s)) * (1.f / 1024.f);
    float q = 0.f;
#pragma unroll
    for (int e = 0; e < 4; ++e) {
      const float d0 = lo16(xa[e]) - mean, d1 = hi16(xa[e]) - mean, d2 = lo16(xb[e]) - mean, d3 = hi16(xb[e]) - mean;
      q += (d0 * d0 + d1 * d1) + (d2 * d2 + d3 * d3);
    }
    const float var = xrow_sum(row_sum(q)) * (1.f / 1024.f);
    const float mb = rbf(mean), sd = rbf(sqrtf(rbf(rbf(var) + 1e-5f)));
    const unsigned xn = pk2(rbf(lo16(xp) - mb) / sd, rbf(hi16(xp) - mb) / sd);
    lds_st32(L_XN + w * 256 + (lane >> 4) * 64 + ((lane >> 1) & 3) * 16 + ((lane >> 3) & 1) * 8 + (lane & 1) * 4, xn);
  }
  STAMP(1);
  {
    asm volatile("s_waitcnt lgkmcnt(0)" ::: "memory");
    v4u xf[4];
#pragma unroll
    for (int ks = 0; ks < 4; ++ks) xf[ks] = lds128(L_XN + w * 256 + ks * 64 + g4 * 16);
    wait_full(8, seen, dead);
    f32x4 d[2];
#pragma unroll
    for (int i = 0; i < 2; ++i) {
      const unsigned a = trrow + 32u * ((2 * w + i) ^ (sub & 7));
      d[i] = f32x4{0.f, 0.f, 0.f, 0.f};
#pragma unroll
      for (int ks = 0; ks < 4; ++ks) d[i] = mfma(ldstr2(a + ks * 8192, a + ks * 8192 + 4096), xf[ks], d[i]);
    }
    const f32x4 v = (c16 & 1) ? d[1] : d[0];
    if (c16 < 2) gst(rs, WS_QP + ((b & 31) * 8 + (b >> 5)) * 512 + (32 * w + 16 * c16 + 4 * g4) * 4, tag4(v, T));
    asm volatile("s_waitcnt lgkmcnt(0)" ::: "memory");
    lds_st32(L_PROG + 4 * w, 8u);
  }
  STAMP(2);
  {
    v4u v[1], dummy;
    const unsigned vo = WS_QP + ((b & 31) * 8 + (lane >> 3)) * 512 + w * 128 + (lane & 7) * 16;
    gather<1, 0, false, 0xFFu>(rs, vo, vo, 0, T, v, dummy, dead);
    v4u r;
#pragma unroll
    for (int e = 0; e < 4; ++e) {
      float a = asf(v[0][e]);
      a += dpp<ROR8>(a);
      a = xrow_sum(a);
      r[e] = (pk2(a, a) << 16) | (T & 0xFFFFu);
    }
    if (lane < 8) gst(rs, WS_QF + ((b >> 5) * 32 + (b & 31)) * 512 + w * 128 + lane * 16, r);
  }
  STAMP(3);
  v4u qf[4];
  {
    v4u v[1], dummy;
    const unsigned vo = WS_QF + ((b & 7) * 32 + (b >> 6) * 8 + 2 * w) * 512 + lane * 16;
    gather<1, 0, false, 0xFFFFu>(rs, vo, vo, 0, T, v, dummy, dead);
    lds_st64(L_Q + w * 512 + lane * 8, v2u{(v[0][0] >> 16) | v[0][1], (v[0][2] >> 16) | v[0][3]});
    lds_st32(L_XF + 4 * w, 1u);
    exch(L_XF, L_Q + (c16 & 7) * 256 + g4 * 16, qf[0], qf[1], dead);
    qf[2] = lds128(L_Q + (c16 & 7) * 256 + g4 * 16 + 128);
    qf[3] = lds128(L_Q + (c16 & 7) * 256 + g4 * 16 + 192);
  }
  STAMP(4);

  {
    unsigned ka[4], va[2];
#pragma unroll
    for (int u = 0; u < 4; ++u) ka[u] = 256u * c16 + 16u * ((4 * u + g4) ^ c16);
#pragma unroll
    for (int i = 0; i < 2; ++i) va[i] = 8192u + trrow + 32u * ((2 * w + i) ^ (sub & 7));
    const bool up = c16 >= 8;
    const float LOG2E = 1.4426950408889634f, scale = 0.08838834764831845f;
    float m = -INFINITY, l = 0.f;
    f32x4 acc[2] = {f32x4{0.f, 0.f, 0.f, 0.f}, f32x4{0.f, 0.f, 0.f, 0.f}};
#pragma unroll 1
    for (int t = 0; t < 32; ++t) {
      const int u0 = U_KV + 4 * t;
      const unsigned tb = (unsigned)(u0 & 31) * 4096u;
      wait_full(u0 + 2, seen, dead);
      f32x4 s0 = {0.f, 0.f, 0.f, 0.f}, s1 = {0.f, 0.f, 0.f, 0.f};
#pragma unroll
      for (int u = 0; u < 4; ++u) {
        s0 = mfma(lds128(tb + ka[u]), qf[u], s0);
        s1 = mfma(lds128(tb + ka[u] + 4096), qf[u], s1);
      }
      float s[4];
#pragma unroll
      for (int r = 0; r < 4; ++r) s[r] = rbf(rbf(up ? s1[r] : s0[r]) * scale);
      const float mn = fmaxf(m, xrow_max(fmaxf(fmaxf(s[0], s[1]), fmaxf(s[2], s[3]))));
      const float al = __builtin_amdgcn_exp2f((m - mn) * LOG2E);
      m = mn;
      float p[4];
#pragma unroll
      for (int r = 0; r < 4; ++r) p[r] = __builtin_amdgcn_exp2f((s[r] - mn) * LOG2E);
      l = l * al + ((p[0] + p[1]) + (p[2] + p[3]));
      const unsigned p01 = pk2(p[0], p[1]), p23 = pk2(p[2], p[3]);
      const v4u pb = up ? v4u{0u, 0u, p01, p23} : v4u{p01, p23, 0u, 0u};
      acc[0] *= al; acc[1] *= al;
      wait_full(u0 + 4, seen, dead);
      const v4u v0 = ldstr2(tb + va[0], tb + va[0] + 4096), v1 = ldstr2(tb + va[1], tb + va[1] + 4096);
      acc[0] = mfma(v0, pb, acc[0]);
      acc[1] = mfma(v1, pb, acc[1]);
      asm volatile("s_waitcnt lgkmcnt(0)" ::: "memory");
      lds_st32(L_PROG + 4 * w, (unsigned)(u0 + 4));
    }
    STAMP(5);
    const float M = fmaxf(m, dpp<ROR8>(m));
    const float f = __builtin_amdgcn_exp2f((m - M) * LOG2E);
    float lt = xrow_sum(l) * f;
    lt += dpp<ROR8>(lt);
    f32x4 o;
#pragma unroll
    for (int e = 0; e < 4; ++e) {
      const float a0 = acc[0][e] * f, a1 = acc[1][e] * f;
      o[e] = (up ? a1 : a0) + dpp<ROR8>(up ? a0 : a1);
    }
    const int hh = (b >> 6) * 8 + (c16 & 7);
    gst(rs, WS_PA + ((hh * 2 + (w >> 1)) * 64 + (b & 63)) * 256 + (32 * (w & 1) + 16 * (c16 >> 3) + 4 * g4) * 4, tag4(o, T));
    if (w == 0 && lane < 8) gst(rs, WS_ML + (hh * 64 + (b & 63)) * 16, v4u{asu(M), T, asu(lt), T});
  }
  STAMP(6);

  unsigned wa[4];
#pragma unroll
  for (int i = 0; i < 4; ++i) wa[i] = (w >> 1) * 4096u + trrow + 32u * ((4 * (w & 1) + i) ^ (sub & 7));
  const unsigned gvo = sub * 256u + w * 64u + p4 * 16u;

  {
    v4u v[4], ml;
    const unsigned vo = WS_PA + (b & 63) * 16384u + gvo;
    gather<4, 4096, true, 0xFFu>(rs, vo, vo, WS_ML + (((b & 63) >> 1) * 64 + 16 * p4 + sub) * 16, T, v, ml, dead);
    STAMP(7);
    const float LOG2E = 1.4426950408889634f;
    const float ms = asf(ml[0]);
    const float M = xrow_max(row_max(ms));
    const float f = __builtin_amdgcn_exp2f((ms - M) * LOG2E);
    const float L = xrow_sum(row_sum(f * asf(ml[2])));
    f32x4 a = asf4(v[0]) * dpp<0x00>(f);
    a += asf4(v[1]) * dpp<0x55>(f);
    a += asf4(v[2]) * dpp<0xAA>(f);
    a += asf4(v[3]) * dpp<0xFF>(f);
    const float o = red_scatter(a) / L;
    if (c16 < 4) lds_st16(L_VEC + (w >> 1) * 64 + p4 * 16 + (w & 1) * 8 + g4 * 2, pk2(o, o));
    lds_st32(L_XF + 16 + 4 * w, 1u);
    wait_full(U_G, seen, dead);
    v4u wf[2][4];
#pragma unroll
    for (int ks = 0; ks < 2; ++ks)
#pragma unroll
      for (int i = 0; i < 4; ++i) {
        const unsigned a0 = wa[i] + ((U_WO & 31) + 4 * ks) * 4096u;
        wf[ks][i] = ldstr2(a0, a0 + 8192);
      }
    v4u f0, f1;
    exch(L_XF + 16, L_VEC + g4 * 16, f0, f1, dead);
    f32x4 r;
#pragma unroll
    for (int i = 0; i < 4; ++i) {
      f32x4 d = mfma(f0, wf[0][i], f32x4{0.f, 0.f, 0.f, 0.f});
      d = mfma(f1, wf[1][i], d);
      r[i] = d[0];
    }
    if (lane < 16) gst(rs, WS_PB + ((4 * (b >> 6) + w) * 64 + (b & 63)) * 256 + lane * 16, tag4(r, T));
  }
  STAMP(8);

  if (b < 240) {
    float h;
    {
      v4u v[4], dummy;
      const unsigned vo = WS_PB + r16 * 16384u + gvo;
      gather<4, 4096, false, 0xFFu>(rs, vo, vo, 0, T, v, dummy, dead);
      STAMP(9);
      const f32x4 a = (asf4(v[0]) + asf4(v[1])) + (asf4(v[2]) + asf4(v[3]));
      h = rbf(xres + rbf(red_scatter(a)));
    }
    if (b < 224) {
      {
        if (c16 < 4) lds_st16(L_VEC + 128 + (g4 >> 1) * 64 + w * 16 + (g4 & 1) * 8 + p4 * 2, asu(h) >> 16);
        lds_st32(L_XF + 32 + 4 * w, 1u);
        wait_full(U_D, seen, dead);
        v4u gf[2][4], uf[2][4];
#pragma unroll
        for (int ks = 0; ks < 2; ++ks)
#pragma unroll
          for (int i = 0; i < 4; ++i) {
            const unsigned a0 = wa[i] + ((U_G & 31) + 4 * ks) * 4096u, a1 = wa[i] + ((U_U & 31) + 4 * ks) * 4096u;
            gf[ks][i] = ldstr2(a0, a0 + 8192);
            uf[ks][i] = ldstr2(a1, a1 + 8192);
          }
        v4u f0, f1;
        exch(L_XF + 32, (c16 & 1) ? L_ONES + g4 * 16 : L_VEC + 128 + g4 * 16, f0, f1, dead);
        f32x4 r = {0.f, 0.f, 0.f, 0.f};
        f32x4 st = mfma(f0, f0, f32x4{0.f, 0.f, 0.f, 0.f});
        st = mfma(f1, f1, st);
#pragma unroll
        for (int i = 0; i < 4; ++i) {
          f32x4 dg = mfma(f0, gf[0][i], f32x4{0.f, 0.f, 0.f, 0.f});
          dg = mfma(f1, gf[1][i], dg);
          f32x4 du = mfma(f0, uf[0][i], f32x4{0.f, 0.f, 0.f, 0.f});
          du = mfma(f1, uf[1][i], du);
          if (i == 0 || g4 == i) r = f32x4{dg[0], dg[1], du[0], du[1]};
        }
        const unsigned rb = 4 * (b >> 4) + w;
        if (lane == 0) gst(rs, WS_ST + (rb * 16 + r16) * 16, v4u{asu(st[1]), T, asu(st[0]), T});
        gst(rs, WS_PC + ((rb * 16 + r16) * 64 + lane) * 16, tag4(r, T));
      }
      STAMP(10);
      {
        const unsigned cb = (unsigned)b / 56u, rb = (unsigned)b - 56u * cb;
        v4u v[4], sv;
        const unsigned vo = WS_PC + ((rb * 16 + g4) * 64 + 16 * w + c16) * 16;
        gather<4, 4096, true, 0xFFu>(rs, vo, vo, WS_ST + (rb * 16 + c16) * 16, T, v, sv, dead);
        STAMP(11);
        const float mean = row_sum(asf(sv[0])) * (1.f / 1024.f);
        const float var = fmaxf(row_sum(asf(sv[2])) * (1.f / 1024.f) - mean * mean, 0.f);
        const float mb = rbf(mean), sd = rbf(sqrtf(rbf(rbf(var) + 1e-5f)));
        const f32x4 a = (asf4(v[0]) + asf4(v[1])) + (asf4(v[2]) + asf4(v[3]));
        const float G = xrow_sum(a[0] - mb * a[1]), U = xrow_sum(a[2] - mb * a[3]);
        const float g = rbf(G / sd), u = rbf(U / sd);
        const float y = 0.7978845608028654f * (g + 0.044715f * g * g * g);
        const float ge = rbf(g / (1.f + __builtin_amdgcn_exp2f(y * (-2.f * 1.4426950408889634f))));
        const float act = ge * u;
        if (lane < 16) lds_st16(L_VEC + 256 + (w >> 1) * 64 + (c16 >> 2) * 16 + (w & 1) * 8 + (c16 & 3) * 2, pk2(act, act));
        lds_st32(L_XF + 48 + 4 * w, 1u);
        wait_full(U_END, seen, dead);
        v4u wf[2][4];
#pragma unroll
        for (int ks = 0; ks < 2; ++ks)
#pragma unroll
          for (int i = 0; i < 4; ++i) {
            const unsigned a0 = wa[i] + ((U_D & 31) + 4 * ks) * 4096u;
            wf[ks][i] = ldstr2(a0, a0 + 8192);
          }
        v4u f0, f1;
        exch(L_XF + 48, L_VEC + 256 + g4 * 16, f0, f1, dead);
        f32x4 r;
#pragma unroll
        for (int i = 0; i < 4; ++i) {
          f32x4 d = mfma(f0, wf[0][i], f32x4{0.f, 0.f, 0.f, 0.f});
          d = mfma(f1, wf[1][i], d);
          r[i] = d[0];
        }
        if (lane < 16) gst(rs, WS_PD + ((4 * cb + w) * 64 + rb) * 256 + lane * 16, tag4(r, T));
      }
      STAMP(12);
    } else {
      v4u v[4], dummy;
      const unsigned vo = WS_PD + r16 * 16384u + gvo;
      gather<4, 4096, false, 0xFFu>(rs, vo, vo - (sub >= 8 ? 2048u : 0u), 0, T, v, dummy, dead);
      STAMP(11);
      if (sub >= 8) v[3] = v4u{0u, 0u, 0u, 0u};
      const f32x4 a = (asf4(v[0]) + asf4(v[1])) + (asf4(v[2]) + asf4(v[3]));
      const float res = h + rbf(red_scatter(a));
      if (c16 < 4) out[64 * r16 + 16 * g4 + 4 * w + p4] = (unsigned short)pk2(res, res);
      if (b == 224 && w == 0 && lane == 0) __hip_atomic_store((GAS unsigned*)ws, T, __ATOMIC_RELAXED, __HIP_MEMORY_SCOPE_AGENT);
      STAMP(12);
    }
  }
  if (b == 224 && w == 0) {
    const unsigned long long tend = ts[PROBE] + 6000;
    for (int i = 0; i < (1 << 22); ++i) if (__builtin_amdgcn_s_memrealtime() >= tend) break;
  }
}

extern "C" void kernel_launch(void* const* d_in, const int* in_sizes, int n_in, void* d_out, int out_size, void* d_ws,
                              size_t ws_size, hipStream_t stream) {
  (void)in_sizes; (void)n_in; (void)out_size; (void)ws_size;
  (void)hipFuncSetAttribute((const void*)mk, hipFuncAttributeMaxDynamicSharedMemorySize, (int)LDS_BYTES);
  mk<<<256, 320, LDS_BYTES, stream>>>((const unsigned short*)d_in[0], d_in[1], d_in[2], d_in[3], d_in[4], d_in[6], d_in[7], d_in[8],
                                      (unsigned short*)d_out, (unsigned*)d_ws);
}
```

```cpp
#include <hip/hip_runtime.h>
#include <stdint.h>

typedef unsigned v4u __attribute__((ext_vector_type(4)));
typedef unsigned v2u __attribute__((ext_vector_type(2)));
typedef float f32x4 __attribute__((ext_vector_type(4)));
typedef float f32x2 __attribute__((ext_vector_type(2)));
typedef __bf16 bf16x8_t __attribute__((ext_vector_type(8)));
typedef __bf16 bf16x2_t __attribute__((ext_vector_type(2)));
typedef short s16x4 __attribute__((ext_vector_type(4)));
#define LAS __attribute__((address_space(3)))
#define GAS __attribute__((address_space(1)))

constexpr unsigned WS_QP = 0x010000;
constexpr unsigned WS_QF = 0x030000;
constexpr unsigned WS_ML = 0x050000;
constexpr unsigned WS_ST = 0x058000;
constexpr unsigned WS_PA = 0x100000;
constexpr unsigned WS_PB = 0x200000;
constexpr unsigned WS_PC = 0x280000;
constexpr unsigned WS_CS = 0x300000;
constexpr unsigned WS_PD = 0x380000;
constexpr unsigned WS_END = 0x400000;

constexpr unsigned L_CTL = 131072;
constexpr unsigned L_FULL = L_CTL;
constexpr unsigned L_PROG = L_CTL + 16;
constexpr unsigned L_XF = L_CTL + 32;
constexpr unsigned L_VEC = L_CTL + 256;
constexpr unsigned L_XN = L_CTL + 1024;
constexpr unsigned L_Q = L_CTL + 2048;
constexpr unsigned LDS_BYTES = L_CTL + 4096;

#define NPOST 16
#define INFL 4
#define INFL0 6
#define INFLP 4
#define HOLD 0
#define PRE 1
#define DLA 12
#define DLB 12
#define DLC 12
#define DLD 12
#define GAPA 0
#define GAPB 0
constexpr int U_WO = 8, U_G = 16, U_KVF = 40 - NPOST;
constexpr int U_U = NPOST >= 16 ? U_KVF + 128 : 24;
constexpr int U_D = NPOST >= 8 ? 160 : 32;
constexpr int BUDGET = 1 << 20;

__device__ __forceinline__ float asf(unsigned u) { return __builtin_bit_cast(float, u); }
__device__ __forceinline__ unsigned asu(float f) { return __builtin_bit_cast(unsigned, f); }
__device__ __forceinline__ unsigned pk2(float lo, float hi) {
  return __builtin_bit_cast(unsigned, __builtin_convertvector(f32x2{lo, hi}, bf16x2_t));
}
__device__ __forceinline__ float rbf(float f) { return asf(pk2(f, f) << 16); }
__device__ __forceinline__ float lo16(unsigned u) { return asf(u << 16); }
__device__ __forceinline__ float hi16(unsigned u) { return asf(u & 0xFFFF0000u); }
__device__ __forceinline__ f32x4 mfma(v4u a, v4u b, f32x4 c) {
  return __builtin_amdgcn_mfma_f32_16x16x32_bf16(__builtin_bit_cast(bf16x8_t, a), __builtin_bit_cast(bf16x8_t, b), c, 0, 0, 0);
}
template <int CTRL> __device__ __forceinline__ float dpp(float x) {
  return asf((unsigned)__builtin_amdgcn_mov_dpp((int)asu(x), CTRL, 0xf, 0xf, true));
}
constexpr int XOR1 = 0xB1, XOR2 = 0x4E, ROR4 = 0x124, ROR8 = 0x128;
__device__ __forceinline__ float xrow_max(float x) {
  auto s = __builtin_amdgcn_permlane16_swap(asu(x), asu(x), false, false);
  x = fmaxf(asf(s[0]), asf(s[1]));
  auto t = __builtin_amdgcn_permlane32_swap(asu(x), asu(x), false, false);
  return fmaxf(asf(t[0]), asf(t[1]));
}
__device__ __forceinline__ float xrow_sum(float x) {
  auto s = __builtin_amdgcn_permlane16_swap(asu(x), asu(x), false, false);
  x = asf(s[0]) + asf(s[1]);
  auto t = __builtin_amdgcn_permlane32_swap(asu(x), asu(x), false, false);
  return asf(t[0]) + asf(t[1]);
}
__device__ __forceinline__ float row_sum(float x) {
  x += dpp<XOR1>(x); x += dpp<XOR2>(x); x += dpp<ROR4>(x); x += dpp<ROR8>(x); return x;
}
__device__ __forceinline__ float row_max(float x) {
  x = fmaxf(x, dpp<XOR1>(x)); x = fmaxf(x, dpp<XOR2>(x)); x = fmaxf(x, dpp<ROR4>(x)); x = fmaxf(x, dpp<ROR8>(x)); return x;
}
__device__ __forceinline__ float red_scatter(f32x4 a) {
  auto s0 = __builtin_amdgcn_permlane32_swap(asu(a[0]), asu(a[2]), false, false);
  auto s1 = __builtin_amdgcn_permlane32_swap(asu(a[1]), asu(a[3]), false, false);
  const float b0 = asf(s0[0]) + asf(s0[1]), b1 = asf(s1[0]) + asf(s1[1]);
  auto t = __builtin_amdgcn_permlane16_swap(asu(b0), asu(b1), false, false);
  float c = asf(t[0]) + asf(t[1]);
  c += dpp<ROR4>(c); c += dpp<ROR8>(c);
  return c;
}
template <int N> __device__ __forceinline__ void waitvm() { asm volatile("s_waitcnt vmcnt(%0)" ::"n"(N) : "memory"); }
__device__ __forceinline__ void lds_st32(unsigned a, unsigned v) { asm volatile("ds_write_b32 %0, %1" ::"v"(a), "v"(v) : "memory"); }
__device__ __forceinline__ void lds_st16(unsigned a, unsigned v) { asm volatile("ds_write_b16 %0, %1" ::"v"(a), "v"(v) : "memory"); }
__device__ __forceinline__ void lds_st64(unsigned a, v2u v) { asm volatile("ds_write_b64 %0, %1" ::"v"(a), "v"(v) : "memory"); }
__device__ __forceinline__ unsigned lds_ld32_wait(unsigned a) {
  unsigned r; asm volatile("ds_read_b32 %0, %1\n\ts_waitcnt lgkmcnt(0)" : "=v"(r) : "v"(a) : "memory"); return r;
}
__device__ __forceinline__ v4u lds_ld128_wait(unsigned a) {
  v4u r; asm volatile("ds_read_b128 %0, %1\n\ts_waitcnt lgkmcnt(0)" : "=v"(r) : "v"(a) : "memory"); return r;
}
__device__ __forceinline__ v4u lds128(unsigned a) { return *(const LAS v4u*)(uintptr_t)a; }
__device__ __forceinline__ v2u ldstr(unsigned a) {
  return __builtin_bit_cast(v2u, __builtin_amdgcn_ds_read_tr16_b64_v4i16((LAS s16x4*)(uintptr_t)a));
}
__device__ __forceinline__ v4u ldstr2(unsigned a0, unsigned a1) {
  const v2u x = ldstr(a0), y = ldstr(a1); return v4u{x[0], x[1], y[0], y[1]};
}
__device__ __forceinline__ void dma4(__amdgpu_buffer_rsrc_t r, unsigned so, unsigned m, unsigned o0, unsigned o1, unsigned o2, unsigned o3) {
  asm volatile("s_mov_b32 m0, %0\n\ts_nop 0\n\t"
               "buffer_load_dwordx4 %1, %5, %6 offen nt lds\n\t"
               "buffer_load_dwordx4 %2, %5, %6 offen offset:1024 nt lds\n\t"
               "buffer_load_dwordx4 %3, %5, %6 offen offset:2048 nt lds\n\t"
               "buffer_load_dwordx4 %4, %5, %6 offen offset:3072 nt lds"
               ::"s"(m), "v"(o0), "v"(o1), "v"(o2), "v"(o3), "s"(r), "s"(so) : "memory");
}
__device__ __forceinline__ __amdgpu_buffer_rsrc_t mkrs(const void* p, unsigned bytes) {
  return __builtin_amdgcn_make_buffer_rsrc((void*)p, (short)0, (int)bytes, 0x00020000);
}
__device__ __forceinline__ v4u gld(__amdgpu_buffer_rsrc_t rs, unsigned vo, unsigned so) { return __builtin_amdgcn_raw_buffer_load_b128(rs, (int)vo, (int)so, 16); }
__device__ __forceinline__ void gst(__amdgpu_buffer_rsrc_t rs, unsigned vo, v4u v) { __builtin_amdgcn_raw_buffer_store_b128(v, rs, (int)vo, 0, 16); }
__device__ __forceinline__ v4u tag4(f32x4 a, unsigned T) {
  v4u r;
#pragma unroll
  for (int e = 0; e < 4; ++e) r[e] = (asu(a[e]) & ~0xFFu) | (T & 0xFFu);
  return r;
}
__device__ __forceinline__ bool none(bool p) { return __builtin_amdgcn_ballot_w64(p) == 0; }

__device__ __forceinline__ void wait_full(int need, int& seen, int& bud) {
  if (seen >= need) return;
  for (;;) {
    seen = (int)__builtin_amdgcn_readfirstlane(lds_ld32_wait(L_FULL));
    if (seen >= need || bud <= 0) return;
    --bud;
    __builtin_amdgcn_s_sleep(1);
  }
}
__device__ __forceinline__ void exch(unsigned flags, unsigned a, v4u& f0, v4u& f1, int& bud) {
  asm volatile("1:\n\tds_read_b128 v[228:231], %[fl]\n\tds_read_b128 %[f0], %[a]\n\tds_read_b128 %[f1], %[a] offset:64\n\t"
               "s_waitcnt lgkmcnt(2)\n\t"
               "v_and_b32 v228, v228, v229\n\tv_bitop3_b32 v228, v228, v230, v231 bitop3:0x80\n\t"
               "v_cmp_eq_u32 vcc, 0, v228\n\ts_waitcnt lgkmcnt(0)\n\ts_cbranch_vccz 2f\n\t"
               "s_sub_u32 %[cnt], %[cnt], 1\n\ts_cmp_gt_i32 %[cnt], 0\n\ts_cbranch_scc1 1b\n\t2:"
               : [f0] "=&v"(f0), [f1] "=&v"(f1), [cnt] "+s"(bud) : [fl] "v"(flags), [a] "v"(a)
               : "memory", "vcc", "scc", "v228", "v229", "v230", "v231");
}
#define G_LD(R, OFF) "buffer_load_dwordx4 " R ", %[vo], %[rs], 0 offen offset:" #OFF " sc1\n\t"
#define G_LDX "buffer_load_dwordx4 v[224:227], %[xo], %[rs], 0 offen sc1\n\t"
#define G_CHK(W, A0, A1, A2, A3, R0, R1, R2, R3, B)                                                             \
  "s_waitcnt vmcnt(" #W ")\n\t"                                                                                 \
  "v_xor_b32 %[" #A0 "], %[tm], " R0 "\n\tv_xor_b32 %[" #A1 "], %[tm], " R1 "\n\t"                                 \
  "v_xor_b32 %[" #A2 "], %[tm], " R2 "\n\tv_xor_b32 %[" #A3 "], %[tm], " R3 "\n\t"                                 \
  "v_or3_b32 v233, %[" #A0 "], %[" #A1 "], %[" #A2 "]\n\t"                                                        \
  "v_bitop3_b32 " B ", v233, %[mk], %[" #A3 "] bitop3:0xc8\n\t"
#define G_CHKX(W) "s_waitcnt vmcnt(" #W ")\n\tv_bitop3_b32 v232, v225, v227, %[t] bitop3:0x7e\n\tv_mov_b32 %[x0], v224\n\tv_mov_b32 %[x2], v226\n\t"
#define G_TEST(B) "v_cmp_ne_u32 vcc, 0, " B "\n\ts_cbranch_vccz 2f\n\ts_sub_u32 %[cnt], %[cnt], 1\n\ts_cmp_gt_i32 %[cnt], 0\n\ts_cbranch_scc0 2f\n\ts_sleep %[gap]\n\t"
#define G_RE(B, LD) "v_cmp_ne_u32 vcc, 0, " B "\n\ts_cbranch_vccz 3f\n\ts_mov_b64 exec, vcc\n\t" LD "s_mov_b64 exec, -1\n\t3:\n\t"
#define G_END "s_waitcnt vmcnt(0)\n\ts_branch 1b\n\t2:"
#define G_C0(W) G_CHK(W, a0, a1, a2, a3, "v208", "v209", "v210", "v211", "v228")
#define G_C1(W) G_CHK(W, b0, b1, b2, b3, "v212", "v213", "v214", "v215", "v229")
#define G_C2(W) G_CHK(W, c0, c1, c2, c3, "v216", "v217", "v218", "v219", "v230")
#define G_C3(W) G_CHK(W, d0, d1, d2, d3, "v220", "v221", "v222", "v223", "v231")
#define G_L0 G_LD("v[208:211]", 0)
#define G_L1 G_LD("v[212:215]", 1024)
#define G_L2 G_LD("v[216:219]", 2048)
#define G_L3 G_LD("v[220:223]", 3072)
#define G_OA [a0] "=&v"(o[0]), [a1] "=&v"(o[1]), [a2] "=&v"(o[2]), [a3] "=&v"(o[3])
#define G_OB [b0] "=&v"(o[4]), [b1] "=&v"(o[5]), [b2] "=&v"(o[6]), [b3] "=&v"(o[7])
#define G_OC [c0] "=&v"(o[8]), [c1] "=&v"(o[9]), [c2] "=&v"(o[10]), [c3] "=&v"(o[11]), \
             [d0] "=&v"(o[12]), [d1] "=&v"(o[13]), [d2] "=&v"(o[14]), [d3] "=&v"(o[15])
#define G_OX [x0] "=&v"(x0), [x2] "=&v"(x2)
#define G_IN [vo] "v"(vo), [rs] "s"(rs), [tm] "s"(T & MASK), [mk] "s"(MASK), [gap] "n"(GAP)
#define G_INX [xo] "v"(xo), [t] "s"(T)
#define G_CLOB "memory", "vcc", "scc", "v208", "v209", "v210", "v211", "v212", "v213", "v214", "v215", "v216", "v217", "v218", "v219", \
               "v220", "v221", "v222", "v223", "v224", "v225", "v226", "v227", "v228", "v229", "v230", "v231", "v232", "v233"
template <int N, bool X, unsigned MASK, int GAP>
__device__ __forceinline__ void gather(__amdgpu_buffer_rsrc_t rs, unsigned vo, unsigned xo, unsigned T, v4u (&v)[N], float& x0, float& x2, int& cnt) {
  unsigned o[16];
  if constexpr (N == 1) {
    asm volatile(G_L0 "1:\n\t" G_C0(0) G_TEST("v228") G_RE("v228", G_L0) G_END
                 : G_OA, [cnt] "+s"(cnt) : G_IN : G_CLOB);
  } else if constexpr (N == 2 && !X) {
    asm volatile(G_L0 G_L1 "1:\n\t" G_C0(1) G_C1(0) "v_or_b32 v233, v228, v229\n\t" G_TEST("v233") G_RE("v228", G_L0) G_RE("v229", G_L1) G_END
                 : G_OA, G_OB, [cnt] "+s"(cnt) : G_IN : G_CLOB);
  } else if constexpr (N == 2 && X) {
    asm volatile(G_LDX G_L0 G_L1 "1:\n\t" G_CHKX(2) G_C0(1) G_C1(0) "v_or3_b32 v233, v228, v229, v232\n\t" G_TEST("v233")
                 G_RE("v232", G_LDX) G_RE("v228", G_L0) G_RE("v229", G_L1) G_END
                 : G_OA, G_OB, G_OX, [cnt] "+s"(cnt) : G_IN, G_INX : G_CLOB);
  } else if constexpr (N == 4 && !X) {
    asm volatile(G_L0 G_L1 G_L2 G_L3 "1:\n\t" G_C0(3) G_C1(2) G_C2(1) "v_or3_b32 v232, v228, v229, v230\n\t" G_C3(0) "v_or_b32 v233, v232, v231\n\t" G_TEST("v233")
                 G_RE("v228", G_L0) G_RE("v229", G_L1) G_RE("v230", G_L2) G_RE("v231", G_L3) G_END
                 : G_OA, G_OB, G_OC, [cnt] "+s"(cnt) : G_IN : G_CLOB);
  } else {
    asm volatile(G_LDX G_L0 G_L1 G_L2 G_L3 "1:\n\t" G_CHKX(4) G_C0(3) G_C1(2) G_C2(1) G_C3(0)
                 "v_or3_b32 v233, v228, v229, v230\n\tv_or3_b32 v233, v233, v231, v232\n\t" G_TEST("v233")
                 G_RE("v232", G_LDX) G_RE("v228", G_L0) G_RE("v229", G_L1) G_RE("v230", G_L2) G_RE("v231", G_L3) G_END
                 : G_OA, G_OB, G_OC, G_OX, [cnt] "+s"(cnt) : G_IN, G_INX : G_CLOB);
  }
#pragma unroll
  for (int i = 0; i < N; ++i) v[i] = v4u{o[4 * i], o[4 * i + 1], o[4 * i + 2], o[4 * i + 3]};
}
__device__ __forceinline__ f32x4 asf4(v4u v) { return __builtin_bit_cast(f32x4, v); }


template <int U> __device__ __forceinline__ void drain(const unsigned (&wa)[4], v4u (&f)[8]) {
#pragma unroll
  for (int ks = 0; ks < 2; ++ks)
#pragma unroll
    for (int i = 0; i < 4; ++i) {
      const unsigned a0 = wa[i] + ((U & 31) + 4 * ks) * 4096u;
      f[4 * ks + i] = ldstr2(a0, a0 + 8192);
    }
  asm volatile("s_waitcnt lgkmcnt(0)"
               : "+v"(f[0]), "+v"(f[1]), "+v"(f[2]), "+v"(f[3]), "+v"(f[4]), "+v"(f[5]), "+v"(f[6]), "+v"(f[7]) :: "memory");
}
__device__ __forceinline__ f32x4 vecmat(v4u f0, v4u f1, const v4u (&f)[8]) {
  f32x4 r;
#pragma unroll
  for (int i = 0; i < 4; ++i) {
    f32x4 d = mfma(f0, f[i], f32x4{0.f, 0.f, 0.f, 0.f});
    d = mfma(f1, f[4 + i], d);
    r[i] = d[0];
  }
  return r;
}
__device__ __forceinline__ void red_gu(f32x4 a, float& G, float& U) {
  auto s0 = __builtin_amdgcn_permlane32_swap(asu(a[0]), asu(a[2]), false, false);
  auto s1 = __builtin_amdgcn_permlane32_swap(asu(a[1]), asu(a[3]), false, false);
  G = asf(s0[0]) + asf(s0[1]); U = asf(s1[0]) + asf(s1[1]);
  auto t0 = __builtin_amdgcn_permlane16_swap(asu(G), asu(G), false, false);
  auto t1 = __builtin_amdgcn_permlane16_swap(asu(U), asu(U), false, false);
  G = asf(t0[0]) + asf(t0[1]); U = asf(t1[0]) + asf(t1[1]);
  G += dpp<ROR8>(G); U += dpp<ROR8>(U);
}

__global__ void __launch_bounds__(320, 2)
mk(const unsigned short* __restrict__ x, const void* __restrict__ K, const void* __restrict__ V, const void* __restrict__ wq,
   const void* __restrict__ wo, const void* __restrict__ wg, const void* __restrict__ wu, const void* __restrict__ wd,
   unsigned short* out, unsigned* ws) {
  const int b = blockIdx.x;
  const int lane = threadIdx.x & 63;
  const int wv = __builtin_amdgcn_readfirstlane(threadIdx.x >> 6);
  const int g4 = lane >> 4, c16 = lane & 15;
  const bool ffn = b < 224;
  const unsigned cbD = (unsigned)b / 56u, rbD = (unsigned)b - 56u * cbD;

  if (wv == 0) {
    unsigned chV[4];
#pragma unroll
    for (int j = 0; j < 4; ++j) chV[j] = 16 * (c16 ^ (8 * (j & 1) + 2 * g4));
    lds_st32(L_CTL + 4 * lane, 0u);
    int u = 0, freed = 0, full = 0;
    int bud = BUDGET;
    auto space = [&](int need) {
      if (freed < need) {
        for (unsigned spins = 0;; ++spins) {
          const v4u p = lds_ld128_wait(L_PROG);
          freed = (int)__builtin_amdgcn_readfirstlane(min(min(p[0], p[1]), min(p[2], p[3])));
          if (freed >= need) break;
          if (spins == 0) { waitvm<0>(); full = u; lds_st32(L_FULL, (unsigned)full); }
          if (bud <= 0) break;
          --bud;
          __builtin_amdgcn_s_sleep(1);
        }
      }
    };
    auto step = [&](__amdgpu_buffer_rsrc_t rs, unsigned so, unsigned o0, unsigned o1, unsigned o2, unsigned o3) {
      space(u - 31);
      dma4(rs, so, (unsigned)(u & 31) * 4096u, o0, o1, o2, o3);
      ++u;
      waitvm<4 * INFL>();
      if (u - INFL > full) { full = u - INFL; lds_st32(L_FULL, (unsigned)full); }
    };
    auto settle = [&]() {
      if (INFL > 5) { waitvm<20>(); if (u - 5 > full) { full = u - 5; lds_st32(L_FULL, (unsigned)full); } }
      if (INFL > 4) { waitvm<16>(); if (u - 4 > full) { full = u - 4; lds_st32(L_FULL, (unsigned)full); } }
      waitvm<12>(); if (u - 3 > full) { full = u - 3; lds_st32(L_FULL, (unsigned)full); }
      waitvm<8>(); if (u - 2 > full) { full = u - 2; lds_st32(L_FULL, (unsigned)full); }
      waitvm<4>(); if (u - 1 > full) { full = u - 1; lds_st32(L_FULL, (unsigned)full); }
      waitvm<0>(); full = u; lds_st32(L_FULL, (unsigned)u);
    };
    auto stepp = [&](__amdgpu_buffer_rsrc_t rs, unsigned so, unsigned o0, unsigned o1, unsigned o2, unsigned o3) {
      space(u - 31);
      if (INFLP < INFL) {
        if (INFL > 4) { waitvm<16>(); if (u - 4 > full) { full = u - 4; lds_st32(L_FULL, (unsigned)full); } }
        if (INFLP < 4) { waitvm<12>(); if (u - 3 > full) { full = u - 3; lds_st32(L_FULL, (unsigned)full); } }
        if (INFLP < 3) { waitvm<8>(); if (u - 2 > full) { full = u - 2; lds_st32(L_FULL, (unsigned)full); } }
        if (INFLP < 2) { waitvm<4>(); if (u - 1 > full) { full = u - 1; lds_st32(L_FULL, (unsigned)full); } }
      }
      dma4(rs, so, (unsigned)(u & 31) * 4096u, o0, o1, o2, o3);
      ++u;
      waitvm<4 * INFLP>();
      if (u - INFLP > full) { full = u - INFLP; lds_st32(L_FULL, (unsigned)full); }
    };
    {
      const __amdgpu_buffer_rsrc_t r = mkrs(wq, 1024u * 4096u * 2u);
      unsigned o[4];
#pragma unroll
      for (int j = 0; j < 4; ++j) o[j] = (4 * j + g4) * 8192 + chV[j] - 1024 * j;
      const unsigned base = (unsigned)(b >> 5) * 128u * 8192u + (unsigned)(b & 31) * 256u;
#pragma unroll 1
      for (int i = 0; i < 8; ++i) {
        dma4(r, base + i * 16u * 8192u, (unsigned)u * 4096u, o[0], o[1], o[2], o[3]);
        ++u;
        if (i == 1) { asm volatile("s_waitcnt lgkmcnt(0)" ::: "memory"); __builtin_amdgcn_s_barrier(); }
        waitvm<4 * INFL0>();
      }
    }
    unsigned o2k[4], o7k[4];
#pragma unroll
    for (int j = 0; j < 4; ++j) {
      o2k[j] = (4 * j + g4) * 2048 + chV[j] - 1024 * j;
      o7k[j] = (4 * j + g4) * 7168 + chV[j] - 1024 * j;
    }
    {
      const __amdgpu_buffer_rsrc_t r = mkrs(wo, 4096u * 1024u * 2u);
      const unsigned base = (unsigned)(b & 63) * 64u * 2048u + (unsigned)(b >> 6) * 512u;
#pragma unroll 1
      for (int i = 0; i < 8; ++i) step(r, base + (i >> 1) * 16u * 2048u + (i & 1) * 256u, o2k[0], o2k[1], o2k[2], o2k[3]);
    }
    const unsigned bgu = (unsigned)(b & 15) * 64u * 7168u + (unsigned)(b >> 4) * 512u;
    const unsigned bd = rbD * 64u * 2048u + cbD * 512u;
    const __amdgpu_buffer_rsrc_t rg = mkrs(wg, 1024u * 3584u * 2u), ru = mkrs(wu, 1024u * 3584u * 2u), rd = mkrs(wd, 3584u * 1024u * 2u);
    if (ffn) {
#pragma unroll 1
      for (int i = 0; i < 8; ++i) step(rg, bgu + (i >> 1) * 16u * 7168u + (i & 1) * 256u, o7k[0], o7k[1], o7k[2], o7k[3]);
      if (NPOST < 16) {
#pragma unroll 1
        for (int i = 0; i < 8; ++i) step(ru, bgu + (i >> 1) * 16u * 7168u + (i & 1) * 256u, o7k[0], o7k[1], o7k[2], o7k[3]);
      }
      if (NPOST < 8) {
#pragma unroll 1
        for (int i = 0; i < 8; ++i) step(rd, bd + (i >> 1) * 16u * 2048u + (i & 1) * 256u, o2k[0], o2k[1], o2k[2], o2k[3]);
      }
    }
    {
      unsigned oK[4], oV[4];
#pragma unroll
      for (int j = 0; j < 4; ++j) {
        oK[j] = g4 * 256 + 16 * (c16 ^ (4 * j + g4));
        oV[j] = g4 * 256 + chV[j];
      }
      const __amdgpu_buffer_rsrc_t rk = mkrs(K, 1u << 26), rv = mkrs(V, 1u << 26);
      unsigned so = (unsigned)b << 18;
#pragma unroll 1
      for (int t = 0; t < 32; ++t, so += 8192) {
        step(rk, so, oK[0], oK[1], oK[2], oK[3]);
        step(rk, so + 4096, oK[0], oK[1], oK[2], oK[3]);
        step(rv, so, oV[0], oV[1], oV[2], oV[3]);
        step(rv, so + 4096, oV[0], oV[1], oV[2], oV[3]);
      }
    }
    if (NPOST > 0 && ffn) {
      if (HOLD >= 1) settle();
      if (HOLD >= 2) space(u + 1);
      if (NPOST >= 16) {
#pragma unroll 1
        for (int i = 0; i < 8; ++i) stepp(ru, bgu + (i >> 1) * 16u * 7168u + (i & 1) * 256u, o7k[0], o7k[1], o7k[2], o7k[3]);
      }
#pragma unroll 1
      for (int i = 0; i < 8; ++i) stepp(rd, bd + (i >> 1) * 16u * 2048u + (i & 1) * 256u, o2k[0], o2k[1], o2k[2], o2k[3]);
    }
    settle();
    return;
  }

  const int w = wv - 1;
  const __amdgpu_buffer_rsrc_t rs = mkrs(ws, WS_END);
  const unsigned T = *(const GAS unsigned*)ws + 1u;
  int bud = BUDGET;
  int seen = 0;
  const int sub = lane >> 2, p4 = lane & 3;
  const unsigned trrow = 256u * sub + 8u * p4;
  const int r16 = b & 15;
  const unsigned rbG = 4 * (b >> 4) + w;
  float xres = 0.f;
  const unsigned aPA = WS_PA + ((((b >> 6) * 8 + (c16 & 7)) * 8 + 2 * w + (c16 >> 3)) * 64 + (b & 63)) * 64 + g4 * 16;
  const unsigned aML = WS_ML + (((b >> 6) * 8 + (c16 & 7)) * 64 + (b & 63)) * 16;
  const unsigned aPB = WS_PB + (((4 * (b >> 6) + w) * 4 + (c16 >> 2)) * 64 + (b & 63)) * 64 + (c16 & 3) * 16;
  const unsigned aPC = ((rbG * 4 + 2 * (g4 & 1) + (c16 >> 3)) * 16 + r16) * 128 + (c16 & 7) * 16;
  const unsigned aST = WS_ST + (rbG * 16 + r16) * 16;
  const unsigned aPD = WS_PD + (((4 * cbD + w) * 16 + c16) * 64 + rbD) * 16;

  {
    const v4u xa = *(const GAS v4u*)(x + 16 * lane), xb = *(const GAS v4u*)(x + 16 * lane + 8);
    const unsigned xp = *(const GAS unsigned*)(x + 128 * (b >> 5) + 2 * lane);
    xres = lo16(x[64 * r16 + 16 * g4 + 4 * w + p4]);
    __builtin_amdgcn_s_barrier();
    float s = 0.f;
#pragma unroll
    for (int e = 0; e < 4; ++e) s += (lo16(xa[e]) + hi16(xa[e])) + (lo16(xb[e]) + hi16(xb[e]));
    const float mean = xrow_sum(row_sum(s)) * (1.f / 1024.f);
    float q = 0.f;
#pragma unroll
    for (int e = 0; e < 4; ++e) {
      const float d0 = lo16(xa[e]) - mean, d1 = hi16(xa[e]) - mean, d2 = lo16(xb[e]) - mean, d3 = hi16(xb[e]) - mean;
      q += (d0 * d0 + d1 * d1) + (d2 * d2 + d3 * d3);
    }
    const float var = xrow_sum(row_sum(q)) * (1.f / 1024.f);
    const float mb = rbf(mean), sd = rbf(sqrtf(rbf(rbf(var) + 1e-5f)));
    const unsigned xn = pk2(rbf(lo16(xp) - mb) / sd, rbf(hi16(xp) - mb) / sd);
    lds_st32(L_XN + w * 256 + (lane >> 4) * 64 + ((lane >> 1) & 3) * 16 + ((lane >> 3) & 1) * 8 + (lane & 1) * 4, xn);
  }
  {
    asm volatile("s_waitcnt lgkmcnt(0)" ::: "memory");
    v4u xf[4];
#pragma unroll
    for (int ks = 0; ks < 4; ++ks) xf[ks] = lds128(L_XN + w * 256 + ks * 64 + g4 * 16);
    wait_full(8, seen, bud);
    f32x4 d[2];
#pragma unroll
    for (int i = 0; i < 2; ++i) {
      const unsigned a = trrow + 32u * ((2 * w + i) ^ (sub & 7));
      d[i] = f32x4{0.f, 0.f, 0.f, 0.f};
#pragma unroll
      for (int ks = 0; ks < 4; ++ks) d[i] = mfma(ldstr2(a + ks * 8192, a + ks * 8192 + 4096), xf[ks], d[i]);
    }
    const f32x4 v = (c16 & 1) ? d[1] : d[0];
    if (c16 < 2) gst(rs, WS_QP + ((b & 31) * 8 + (b >> 5)) * 512 + (32 * w + 16 * c16 + 4 * g4) * 4, tag4(v, T));
    asm volatile("s_waitcnt lgkmcnt(0)" ::: "memory");
    lds_st32(L_PROG + 4 * w, 8u);
  }
  unsigned wa[4];
#pragma unroll
  for (int i = 0; i < 4; ++i) wa[i] = (w >> 1) * 4096u + trrow + 32u * ((4 * (w & 1) + i) ^ (sub & 7));
  v4u fo[8], fg[8], fu[8], fd[8];
  wait_full(U_WO + 8, seen, bud);
  drain<U_WO>(wa, fo);
  lds_st32(L_PROG + 4 * w, U_WO + 8u);
  {
    v4u v[1]; float d0, d1;
    gather<1, false, 0xFFu, 4>(rs, WS_QP + ((b & 31) * 8 + (lane >> 3)) * 512 + w * 128 + (lane & 7) * 16, 0, T, v, d0, d1, bud);
    v4u r;
#pragma unroll
    for (int e = 0; e < 4; ++e) {
      float a = asf(v[0][e]);
      a += dpp<ROR8>(a);
      a = xrow_sum(a);
      r[e] = (pk2(a, a) << 16) | (T & 0xFFFFu);
    }
    if (lane < 8) gst(rs, WS_QF + ((b >> 5) * 32 + (b & 31)) * 512 + w * 128 + lane * 16, r);
  }
  if (PRE) {
    const unsigned nt = ~T;
    const v4u z = {nt, nt, nt, nt};
    gst(rs, aPA, z);
    if (w == 0 && lane < 8) gst(rs, aML, z);
    if (lane < 16) gst(rs, aPB, z);
    if (ffn) {
      if (lane < 32) gst(rs, WS_PC + aPC, z);
      if (lane == 0) gst(rs, aST, z);
      if (lane < 16) gst(rs, aPD, z);
    }
  }
  wait_full(U_G + 8, seen, bud);
  if (ffn) { drain<U_G>(wa, fg); lds_st32(L_PROG + 4 * w, U_G + 8u); }
  v4u qf[4];
  {
    v4u v[1]; float d0, d1;
    gather<1, false, 0xFFFFu, 4>(rs, WS_QF + ((b & 7) * 32 + (b >> 6) * 8 + 2 * w) * 512 + lane * 16, 0, T, v, d0, d1, bud);
    waitvm<0>();
    lds_st64(L_Q + w * 512 + lane * 8, v2u{(v[0][0] >> 16) | v[0][1], (v[0][2] >> 16) | v[0][3]});
    lds_st32(L_XF + 4 * w, 1u);
    exch(L_XF, L_Q + (c16 & 7) * 256 + g4 * 16, qf[0], qf[1], bud);
    qf[2] = lds128(L_Q + (c16 & 7) * 256 + g4 * 16 + 128);
    qf[3] = lds128(L_Q + (c16 & 7) * 256 + g4 * 16 + 192);
  }
  const v4u ones = {0x3F803F80u, 0x3F803F80u, 0x3F803F80u, 0x3F803F80u};
  float Cg = 0.f, Cu = 0.f;
  const unsigned gPC = (rbD * 4 + w) * 2048 + lane * 16;
  auto colsums = [&]() {
    const f32x4 cg = vecmat(ones, ones, fg), cu = vecmat(ones, ones, fu);
    const f32x4 r = (g4 & 1) ? f32x4{cg[2], cu[2], cg[3], cu[3]} : f32x4{cg[0], cu[0], cg[1], cu[1]};
    if (lane < 32) gst(rs, WS_CS + aPC, tag4(r, T));
  };
  auto getsums = [&]() {
    v4u v[2]; float d0, d1;
    gather<2, false, 0xFFu, 4>(rs, WS_CS + gPC, 0, T, v, d0, d1, bud);
    red_gu(asf4(v[0]) + asf4(v[1]), Cg, Cu);
  };
  if (ffn) {
    if (NPOST < 16) {
      wait_full(U_U + 8, seen, bud);
      drain<U_U>(wa, fu);
      lds_st32(L_PROG + 4 * w, U_U + 8u);
      colsums();
    }
    wait_full(40, seen, bud);
    if (NPOST < 8) { drain<U_D>(wa, fd); lds_st32(L_PROG + 4 * w, U_D + 8u); }
    if (NPOST < 16) getsums();
  }

  {
    unsigned ka[4], va[2];
#pragma unroll
    for (int u = 0; u < 4; ++u) ka[u] = 256u * c16 + 16u * ((4 * u + g4) ^ c16);
#pragma unroll
    for (int i = 0; i < 2; ++i) va[i] = 8192u + trrow + 32u * ((2 * w + i) ^ (sub & 7));
    const bool up = c16 >= 8;
    const float LOG2E = 1.4426950408889634f, scale = 0.08838834764831845f;
    float m = -INFINITY, l = 0.f;
    f32x4 acc[2] = {f32x4{0.f, 0.f, 0.f, 0.f}, f32x4{0.f, 0.f, 0.f, 0.f}};
    const int ukv = ffn ? U_KVF : 16;
#pragma unroll 1
    for (int u0 = ukv; u0 < ukv + 128; u0 += 4) {
      const unsigned tb = (unsigned)(u0 & 31) * 4096u;
      wait_full(u0 + 2, seen, bud);
      f32x4 s0 = {0.f, 0.f, 0.f, 0.f}, s1 = {0.f, 0.f, 0.f, 0.f};
#pragma unroll
      for (int u = 0; u < 4; ++u) {
        s0 = mfma(lds128(tb + ka[u]), qf[u], s0);
        s1 = mfma(lds128(tb + ka[u] + 4096), qf[u], s1);
      }
      float s[4];
#pragma unroll
      for (int r = 0; r < 4; ++r) s[r] = rbf(rbf(up ? s1[r] : s0[r]) * scale);
      const float mn = fmaxf(m, xrow_max(fmaxf(fmaxf(s[0], s[1]), fmaxf(s[2], s[3]))));
      const float al = __builtin_amdgcn_exp2f((m - mn) * LOG2E);
      m = mn;
      float p[4];
#pragma unroll
      for (int r = 0; r < 4; ++r) p[r] = __builtin_amdgcn_exp2f((s[r] - mn) * LOG2E);
      l = l * al + ((p[0] + p[1]) + (p[2] + p[3]));
      const unsigned p01 = pk2(p[0], p[1]), p23 = pk2(p[2], p[3]);
      const v4u pb = up ? v4u{0u, 0u, p01, p23} : v4u{p01, p23, 0u, 0u};
      acc[0] *= al; acc[1] *= al;
      wait_full(u0 + 4, seen, bud);
      const v4u v0 = ldstr2(tb + va[0], tb + va[0] + 4096), v1 = ldstr2(tb + va[1], tb + va[1] + 4096);
      acc[0] = mfma(v0, pb, acc[0]);
      acc[1] = mfma(v1, pb, acc[1]);
      asm volatile("s_waitcnt lgkmcnt(0)" ::: "memory");
      lds_st32(L_PROG + 4 * w, (unsigned)(u0 + 4));
    }
    const float M = fmaxf(m, dpp<ROR8>(m));
    const float f = __builtin_amdgcn_exp2f((m - M) * LOG2E);
    float lt = xrow_sum(l) * f;
    lt += dpp<ROR8>(lt);
    f32x4 o;
#pragma unroll
    for (int e = 0; e < 4; ++e) {
      const float a0 = acc[0][e] * f, a1 = acc[1][e] * f;
      o[e] = (up ? a1 : a0) + dpp<ROR8>(up ? a0 : a1);
    }
    gst(rs, aPA, tag4(o, T));
    if (w == 0 && lane < 8) gst(rs, aML, v4u{asu(M), T, asu(lt), T});
    if (HOLD >= 2) lds_st32(L_PROG + 4 * w, (unsigned)(ukv + 129));
  }

  {
    v4u v[4]; float ms, ls;
    if (DLA) __builtin_amdgcn_s_sleep(DLA);
    gather<4, true, 0xFFu, GAPA>(rs, WS_PA + ((b & 63) * 4 + w) * 4096u + lane * 16, WS_ML + (((b & 63) >> 1) * 64 + 16 * p4 + sub) * 16, T, v, ms, ls, bud);
    const float LOG2E = 1.4426950408889634f;
    const float M = xrow_max(row_max(ms));
    const float f = __builtin_amdgcn_exp2f((ms - M) * LOG2E);
    const float L = xrow_sum(row_sum(f * ls));
    f32x4 a = asf4(v[0]) * dpp<0x00>(f);
    a += asf4(v[1]) * dpp<0x55>(f);
    a += asf4(v[2]) * dpp<0xAA>(f);
    a += asf4(v[3]) * dpp<0xFF>(f);
    const float o = red_scatter(a) * __builtin_amdgcn_rcpf(L);
    if (c16 < 4) lds_st16(L_VEC + (w >> 1) * 64 + p4 * 16 + (w & 1) * 8 + g4 * 2, pk2(o, o));
    lds_st32(L_XF + 16 + 4 * w, 1u);
    v4u f0, f1;
    exch(L_XF + 16, L_VEC + g4 * 16, f0, f1, bud);
    const f32x4 r = vecmat(f0, f1, fo);
    if (lane < 16) gst(rs, aPB, tag4(r, T));
  }

  if (ffn) {
    float h;
    if (NPOST >= 16) {
      wait_full(U_U + 8, seen, bud);
      drain<U_U>(wa, fu);
      colsums();
    }
    {
      v4u v[4]; float d0, d1;
      if (DLB) __builtin_amdgcn_s_sleep(DLB);
      gather<4, false, 0xFFu, GAPB>(rs, WS_PB + (r16 * 4 + w) * 4096u + lane * 16, 0, T, v, d0, d1, bud);
      const f32x4 a = (asf4(v[0]) + asf4(v[1])) + (asf4(v[2]) + asf4(v[3]));
      h = rbf(xres + rbf(red_scatter(a)));
    }
    {
      if (c16 < 4) lds_st16(L_VEC + 128 + (g4 >> 1) * 64 + w * 16 + (g4 & 1) * 8 + p4 * 2, asu(h) >> 16);
      lds_st32(L_XF + 32 + 4 * w, 1u);
      v4u f0, f1;
      exch(L_XF + 32, L_VEC + 128 + g4 * 16, f0, f1, bud);
      const f32x4 sg = vecmat(f0, f1, fg), su = vecmat(f0, f1, fu);
      const f32x4 r = (g4 & 1) ? f32x4{sg[2], su[2], sg[3], su[3]} : f32x4{sg[0], su[0], sg[1], su[1]};
      if (lane < 32) gst(rs, WS_PC + aPC, tag4(r, T));
      f32x4 s2 = mfma(f0, f0, f32x4{0.f, 0.f, 0.f, 0.f}), s1 = mfma(f0, ones, f32x4{0.f, 0.f, 0.f, 0.f});
      s2 = mfma(f1, f1, s2); s1 = mfma(f1, ones, s1);
      if (lane == 0) gst(rs, aST, v4u{asu(s1[0]), T, asu(s2[0]), T});
    }
    if (NPOST >= 16) getsums();
    if (NPOST >= 8) {
      wait_full(U_D + 8, seen, bud);
      drain<U_D>(wa, fd);
    }
    {
      v4u v[2]; float sv0, sv2;
      if (DLC) __builtin_amdgcn_s_sleep(DLC);
      gather<2, true, 0xFFu, GAPB>(rs, WS_PC + gPC, WS_ST + (rbD * 16 + c16) * 16, T, v, sv0, sv2, bud);
      const float mean = row_sum(sv0) * (1.f / 1024.f);
      const float var = fmaxf(row_sum(sv2) * (1.f / 1024.f) - mean * mean, 0.f);
      const float mb = rbf(mean), sd = rbf(__builtin_amdgcn_sqrtf(rbf(rbf(var) + 1e-5f)));
      const float rsd = __builtin_amdgcn_rcpf(sd);
      float G, U;
      red_gu(asf4(v[0]) + asf4(v[1]), G, U);
      const float g = rbf((G - mb * Cg) * rsd), u = rbf((U - mb * Cu) * rsd);
      const float y = 0.7978845608028654f * (g + 0.044715f * g * g * g);
      const float ge = rbf(g * __builtin_amdgcn_rcpf(1.f + __builtin_amdgcn_exp2f(y * (-2.f * 1.4426950408889634f))));
      const float act = ge * u;
      if ((lane & 24) == 0) lds_st16(L_VEC + 256 + (w >> 1) * 64 + (2 * (w & 1) + ((lane >> 2) & 1)) * 16 + (lane >> 5) * 8 + p4 * 2, pk2(act, act));
      lds_st32(L_XF + 48 + 4 * w, 1u);
      v4u f0, f1;
      exch(L_XF + 48, L_VEC + 256 + g4 * 16, f0, f1, bud);
      const f32x4 r = vecmat(f0, f1, fd);
      if (lane < 16) gst(rs, aPD, tag4(r, T));
    }
    if (b < 64) {
      v4u v[1]; float d0, d1;
      if (DLD) __builtin_amdgcn_s_sleep(DLD);
      gather<1, false, 0xFFu, GAPB>(rs, WS_PD + ((r16 * 16 + 4 * w + (b >> 4)) * 64 + (lane < 56 ? lane : lane - 8)) * 16, 0, T, v, d0, d1, bud);
      if (lane >= 56) v[0] = v4u{0u, 0u, 0u, 0u};
      float c = red_scatter(asf4(v[0]));
      c += dpp<XOR1>(c); c += dpp<XOR2>(c);
      const float res = h + rbf(c);
      if (c16 == (b >> 4)) out[64 * r16 + 16 * g4 + 4 * w + p4] = (unsigned short)pk2(res, res);
      if (b == 0 && w == 0 && lane == 0) __hip_atomic_store((GAS unsigned*)ws, T, __ATOMIC_RELAXED, __HIP_MEMORY_SCOPE_AGENT);
    }
  }
}

extern "C" void kernel_launch(void* const* d_in, const int* in_sizes, int n_in, void* d_out, int out_size, void* d_ws,
                              size_t ws_size, hipStream_t stream) {
  (void)in_sizes; (void)n_in; (void)out_size; (void)ws_size;
  (void)hipFuncSetAttribute((const void*)mk, hipFuncAttributeMaxDynamicSharedMemorySize, (int)LDS_BYTES);
  mk<<<256, 320, LDS_BYTES, stream>>>((const unsigned short*)d_in[0], d_in[1], d_in[2], d_in[3], d_in[4], d_in[6], d_in[7], d_in[8],
                                      (unsigned short*)d_out, (unsigned*)((char*)d_ws + ((size_t)128  __H0__ )));
}
```

```cpp
#include <hip/hip_runtime.h>
#include <stdint.h>

typedef unsigned v4u __attribute__((ext_vector_type(4)));
typedef unsigned v2u __attribute__((ext_vector_type(2)));
typedef float f32x4 __attribute__((ext_vector_type(4)));
typedef float f32x2 __attribute__((ext_vector_type(2)));
typedef __bf16 bf16x8_t __attribute__((ext_vector_type(8)));
typedef __bf16 bf16x2_t __attribute__((ext_vector_type(2)));
typedef short s16x4 __attribute__((ext_vector_type(4)));
#define LAS __attribute__((address_space(3)))
#define GAS __attribute__((address_space(1)))

constexpr unsigned WS_QP = 0x010000;
constexpr unsigned WS_QF = 0x030000;
constexpr unsigned WS_ML = 0x050000;
constexpr unsigned WS_ST = 0x058000;
constexpr unsigned WS_PA = 0x100000;
constexpr unsigned WS_PB = 0x200000;
constexpr unsigned WS_PC = 0x280000;
constexpr unsigned WS_CS = 0x300000;
constexpr unsigned WS_PD = 0x380000;
constexpr unsigned WS_END = 0x400000;

constexpr unsigned L_CTL = 131072;
constexpr unsigned L_FULL = L_CTL;
constexpr unsigned L_PROG = L_CTL + 16;
constexpr unsigned L_XF = L_CTL + 32;
constexpr unsigned L_VEC = L_CTL + 256;
constexpr unsigned L_XN = L_CTL + 1024;
constexpr unsigned L_Q = L_CTL + 2048;
constexpr unsigned L_TR = L_CTL + 4096;
constexpr unsigned LDS_BYTES = L_CTL + 4096;

#define NPOST 16
#define INFL 5
#define INFL0 6
#define INFLP 4
#define HOLD 0
#define PRE 0
#define DLA 12
#define DLB 4
#define DLC 0
#define DLD 12
#define GAPA 0
#define GAPB 0
constexpr int U_WO = 8, U_G = 16, U_KVF = 40 - NPOST;
constexpr int U_U = NPOST >= 16 ? U_KVF + 128 : 24;
constexpr int U_D = NPOST >= 8 ? 160 : 32;
constexpr int BUDGET = 1 << 20;

__device__ __forceinline__ float asf(unsigned u) { return __builtin_bit_cast(float, u); }
__device__ __forceinline__ unsigned asu(float f) { return __builtin_bit_cast(unsigned, f); }
__device__ __forceinline__ unsigned pk2(float lo, float hi) {
  return __builtin_bit_cast(unsigned, __builtin_convertvector(f32x2{lo, hi}, bf16x2_t));
}
__device__ __forceinline__ float rbf(float f) { return asf(pk2(f, f) << 16); }
__device__ __forceinline__ float lo16(unsigned u) { return asf(u << 16); }
__device__ __forceinline__ float hi16(unsigned u) { return asf(u & 0xFFFF0000u); }
__device__ __forceinline__ f32x4 mfma(v4u a, v4u b, f32x4 c) {
  return __builtin_amdgcn_mfma_f32_16x16x32_bf16(__builtin_bit_cast(bf16x8_t, a), __builtin_bit_cast(bf16x8_t, b), c, 0, 0, 0);
}
template <int CTRL> __device__ __forceinline__ float dpp(float x) {
  return asf((unsigned)__builtin_amdgcn_mov_dpp((int)asu(x), CTRL, 0xf, 0xf, true));
}
constexpr int XOR1 = 0xB1, XOR2 = 0x4E, ROR4 = 0x124, ROR8 = 0x128;
__device__ __forceinline__ float xrow_max(float x) {
  auto s = __builtin_amdgcn_permlane16_swap(asu(x), asu(x), false, false);
  x = fmaxf(asf(s[0]), asf(s[1]));
  auto t = __builtin_amdgcn_permlane32_swap(asu(x), asu(x), false, false);
  return fmaxf(asf(t[0]), asf(t[1]));
}
__device__ __forceinline__ float xrow_sum(float x) {
  auto s = __builtin_amdgcn_permlane16_swap(asu(x), asu(x), false, false);
  x = asf(s[0]) + asf(s[1]);
  auto t = __builtin_amdgcn_permlane32_swap(asu(x), asu(x), false, false);
  return asf(t[0]) + asf(t[1]);
}
__device__ __forceinline__ float row_sum(float x) {
  x += dpp<XOR1>(x); x += dpp<XOR2>(x); x += dpp<ROR4>(x); x += dpp<ROR8>(x); return x;
}
__device__ __forceinline__ float row_max(float x) {
  x = fmaxf(x, dpp<XOR1>(x)); x = fmaxf(x, dpp<XOR2>(x)); x = fmaxf(x, dpp<ROR4>(x)); x = fmaxf(x, dpp<ROR8>(x)); return x;
}
__device__ __forceinline__ float red_scatter(f32x4 a) {
  auto s0 = __builtin_amdgcn_permlane32_swap(asu(a[0]), asu(a[2]), false, false);
  auto s1 = __builtin_amdgcn_permlane32_swap(asu(a[1]), asu(a[3]), false, false);
  const float b0 = asf(s0[0]) + asf(s0[1]), b1 = asf(s1[0]) + asf(s1[1]);
  auto t = __builtin_amdgcn_permlane16_swap(asu(b0), asu(b1), false, false);
  float c = asf(t[0]) + asf(t[1]);
  c += dpp<ROR4>(c); c += dpp<ROR8>(c);
  return c;
}
template <int N> __device__ __forceinline__ void waitvm() { asm volatile("s_waitcnt vmcnt(%0)" ::"n"(N) : "memory"); }
__device__ __forceinline__ void lds_st32(unsigned a, unsigned v) { asm volatile("ds_write_b32 %0, %1" ::"v"(a), "v"(v) : "memory"); }
__device__ __forceinline__ void lds_st16(unsigned a, unsigned v) { asm volatile("ds_write_b16 %0, %1" ::"v"(a), "v"(v) : "memory"); }
__device__ __forceinline__ void lds_st64(unsigned a, v2u v) { asm volatile("ds_write_b64 %0, %1" ::"v"(a), "v"(v) : "memory"); }
__device__ __forceinline__ unsigned lds_ld32_wait(unsigned a) {
  unsigned r; asm volatile("ds_read_b32 %0, %1\n\ts_waitcnt lgkmcnt(0)" : "=v"(r) : "v"(a) : "memory"); return r;
}
__device__ __forceinline__ v4u lds_ld128_wait(unsigned a) {
  v4u r; asm volatile("ds_read_b128 %0, %1\n\ts_waitcnt lgkmcnt(0)" : "=v"(r) : "v"(a) : "memory"); return r;
}
__device__ __forceinline__ v4u lds128(unsigned a) { return *(const LAS v4u*)(uintptr_t)a; }
__device__ __forceinline__ v2u ldstr(unsigned a) {
  return __builtin_bit_cast(v2u, __builtin_amdgcn_ds_read_tr16_b64_v4i16((LAS s16x4*)(uintptr_t)a));
}
__device__ __forceinline__ v4u ldstr2(unsigned a0, unsigned a1) {
  const v2u x = ldstr(a0), y = ldstr(a1); return v4u{x[0], x[1], y[0], y[1]};
}
__device__ __forceinline__ void dma4(__amdgpu_buffer_rsrc_t r, unsigned so, unsigned m, unsigned o0, unsigned o1, unsigned o2, unsigned o3) {
  asm volatile("s_mov_b32 m0, %0\n\ts_nop 0\n\t"
               "buffer_load_dwordx4 %1, %5, %6 offen nt lds\n\t"
               "buffer_load_dwordx4 %2, %5, %6 offen offset:1024 nt lds\n\t"
               "buffer_load_dwordx4 %3, %5, %6 offen offset:2048 nt lds\n\t"
               "buffer_load_dwordx4 %4, %5, %6 offen offset:3072 nt lds"
               ::"s"(m), "v"(o0), "v"(o1), "v"(o2), "v"(o3), "s"(r), "s"(so) : "memory");
}
__device__ __forceinline__ __amdgpu_buffer_rsrc_t mkrs(const void* p, unsigned bytes) {
  return __builtin_amdgcn_make_buffer_rsrc((void*)p, (short)0, (int)bytes, 0x00020000);
}
__device__ __forceinline__ v4u gld(__amdgpu_buffer_rsrc_t rs, unsigned vo, unsigned so) { return __builtin_amdgcn_raw_buffer_load_b128(rs, (int)vo, (int)so, 16); }
__device__ __forceinline__ void gst(__amdgpu_buffer_rsrc_t rs, unsigned vo, v4u v) { __builtin_amdgcn_raw_buffer_store_b128(v, rs, (int)vo, 0, 16); }
__device__ __forceinline__ v4u tag4(f32x4 a, unsigned T) {
  v4u r;
#pragma unroll
  for (int e = 0; e < 4; ++e) r[e] = (asu(a[e]) & ~0xFFu) | (T & 0xFFu);
  return r;
}
__device__ __forceinline__ bool none(bool p) { return __builtin_amdgcn_ballot_w64(p) == 0; }

__device__ __forceinline__ void wait_full(int need, int& seen, int& bud) {
  if (seen >= need) return;
  for (;;) {
    seen = (int)__builtin_amdgcn_readfirstlane(lds_ld32_wait(L_FULL));
    if (seen >= need || bud <= 0) return;
    --bud;
    __builtin_amdgcn_s_sleep(1);
  }
}
__device__ __forceinline__ void exch(unsigned flags, unsigned a, v4u& f0, v4u& f1, int& bud) {
  asm volatile("1:\n\tds_read_b128 v[228:231], %[fl]\n\tds_read_b128 %[f0], %[a]\n\tds_read_b128 %[f1], %[a] offset:64\n\t"
               "s_waitcnt lgkmcnt(2)\n\t"
               "v_and_b32 v228, v228, v229\n\tv_bitop3_b32 v228, v228, v230, v231 bitop3:0x80\n\t"
               "v_cmp_eq_u32 vcc, 0, v228\n\ts_waitcnt lgkmcnt(0)\n\ts_cbranch_vccz 2f\n\t"
               "s_sub_u32 %[cnt], %[cnt], 1\n\ts_cmp_gt_i32 %[cnt], 0\n\ts_cbranch_scc1 1b\n\t2:"
               : [f0] "=&v"(f0), [f1] "=&v"(f1), [cnt] "+s"(bud) : [fl] "v"(flags), [a] "v"(a)
               : "memory", "vcc", "scc", "v228", "v229", "v230", "v231");
}
#define G_LD(R, OFF) "buffer_load_dwordx4 " R ", %[vo], %[rs], 0 offen offset:" #OFF " sc1\n\t"
#define G_LDX "buffer_load_dwordx4 v[224:227], %[xo], %[rs], 0 offen sc1\n\t"
#define G_CHK(W, A0, A1, A2, A3, R0, R1, R2, R3, B)                                                             \
  "s_waitcnt vmcnt(" #W ")\n\t"                                                                                 \
  "v_xor_b32 %[" #A0 "], %[tm], " R0 "\n\tv_xor_b32 %[" #A1 "], %[tm], " R1 "\n\t"                                 \
  "v_xor_b32 %[" #A2 "], %[tm], " R2 "\n\tv_xor_b32 %[" #A3 "], %[tm], " R3 "\n\t"                                 \
  "v_or3_b32 v233, %[" #A0 "], %[" #A1 "], %[" #A2 "]\n\t"                                                        \
  "v_bitop3_b32 " B ", v233, %[mk], %[" #A3 "] bitop3:0xc8\n\t"
#define G_CHKX(W) "s_waitcnt vmcnt(" #W ")\n\tv_bitop3_b32 v232, v225, v227, %[t] bitop3:0x7e\n\tv_mov_b32 %[x0], v224\n\tv_mov_b32 %[x2], v226\n\t"
#define G_TEST(B) "v_cmp_ne_u32 vcc, 0, " B "\n\ts_cbranch_vccz 2f\n\ts_sub_u32 %[cnt], %[cnt], 1\n\ts_cmp_gt_i32 %[cnt], 0\n\ts_cbranch_scc0 2f\n\ts_sleep %[gap]\n\t"
#define G_RE(B, LD) "v_cmp_ne_u32 vcc, 0, " B "\n\ts_cbranch_vccz 3f\n\ts_mov_b64 exec, vcc\n\t" LD "s_mov_b64 exec, -1\n\t3:\n\t"
#define G_END "s_waitcnt vmcnt(0)\n\ts_branch 1b\n\t2:"
#define G_C0(W) G_CHK(W, a0, a1, a2, a3, "v208", "v209", "v210", "v211", "v228")
#define G_C1(W) G_CHK(W, b0, b1, b2, b3, "v212", "v213", "v214", "v215", "v229")
#define G_C2(W) G_CHK(W, c0, c1, c2, c3, "v216", "v217", "v218", "v219", "v230")
#define G_C3(W) G_CHK(W, d0, d1, d2, d3, "v220", "v221", "v222", "v223", "v231")
#define G_L0 G_LD("v[208:211]", 0)
#define G_L1 G_LD("v[212:215]", 1024)
#define G_L2 G_LD("v[216:219]", 2048)
#define G_L3 G_LD("v[220:223]", 3072)
#define G_OA [a0] "=&v"(o[0]), [a1] "=&v"(o[1]), [a2] "=&v"(o[2]), [a3] "=&v"(o[3])
#define G_OB [b0] "=&v"(o[4]), [b1] "=&v"(o[5]), [b2] "=&v"(o[6]), [b3] "=&v"(o[7])
#define G_OC [c0] "=&v"(o[8]), [c1] "=&v"(o[9]), [c2] "=&v"(o[10]), [c3] "=&v"(o[11]), \
             [d0] "=&v"(o[12]), [d1] "=&v"(o[13]), [d2] "=&v"(o[14]), [d3] "=&v"(o[15])
#define G_OX [x0] "=&v"(x0), [x2] "=&v"(x2)
#define G_IN [vo] "v"(vo), [rs] "s"(rs), [tm] "s"(T & MASK), [mk] "s"(MASK), [gap] "n"(GAP)
#define G_INX [xo] "v"(xo), [t] "s"(T)
#define G_CLOB "memory", "vcc", "scc", "v208", "v209", "v210", "v211", "v212", "v213", "v214", "v215", "v216", "v217", "v218", "v219", \
               "v220", "v221", "v222", "v223", "v224", "v225", "v226", "v227", "v228", "v229", "v230", "v231", "v232", "v233"
template <int N, bool X, unsigned MASK, int GAP>
__device__ __forceinline__ void gather(__amdgpu_buffer_rsrc_t rs, unsigned vo, unsigned xo, unsigned T, v4u (&v)[N], float& x0, float& x2, int& cnt) {
  unsigned o[16];
  if constexpr (N == 1) {
    asm volatile(G_L0 "1:\n\t" G_C0(0) G_TEST("v228") G_RE("v228", G_L0) G_END
                 : G_OA, [cnt] "+s"(cnt) : G_IN : G_CLOB);
  } else if constexpr (N == 2 && !X) {
    asm volatile(G_L0 G_L1 "1:\n\t" G_C0(1) G_C1(0) "v_or_b32 v233, v228, v229\n\t" G_TEST("v233") G_RE("v228", G_L0) G_RE("v229", G_L1) G_END
                 : G_OA, G_OB, [cnt] "+s"(cnt) : G_IN : G_CLOB);
  } else if constexpr (N == 2 && X) {
    asm volatile(G_LDX G_L0 G_L1 "1:\n\t" G_CHKX(2) G_C0(1) G_C1(0) "v_or3_b32 v233, v228, v229, v232\n\t" G_TEST("v233")
                 G_RE("v232", G_LDX) G_RE("v228", G_L0) G_RE("v229", G_L1) G_END
                 : G_OA, G_OB, G_OX, [cnt] "+s"(cnt) : G_IN, G_INX : G_CLOB);
  } else if constexpr (N == 4 && !X) {
    asm volatile(G_L0 G_L1 G_L2 G_L3 "1:\n\t" G_C0(3) G_C1(2) G_C2(1) "v_or3_b32 v232, v228, v229, v230\n\t" G_C3(0) "v_or_b32 v233, v232, v231\n\t" G_TEST("v233")
                 G_RE("v228", G_L0) G_RE("v229", G_L1) G_RE("v230", G_L2) G_RE("v231", G_L3) G_END
                 : G_OA, G_OB, G_OC, [cnt] "+s"(cnt) : G_IN : G_CLOB);
  } else {
    asm volatile(G_LDX G_L0 G_L1 G_L2 G_L3 "1:\n\t" G_CHKX(4) G_C0(3) G_C1(2) G_C2(1) G_C3(0)
                 "v_or3_b32 v233, v228, v229, v230\n\tv_or3_b32 v233, v233, v231, v232\n\t" G_TEST("v233")
                 G_RE("v232", G_LDX) G_RE("v228", G_L0) G_RE("v229", G_L1) G_RE("v230", G_L2) G_RE("v231", G_L3) G_END
                 : G_OA, G_OB, G_OC, G_OX, [cnt] "+s"(cnt) : G_IN, G_INX : G_CLOB);
  }
#pragma unroll
  for (int i = 0; i < N; ++i) v[i] = v4u{o[4 * i], o[4 * i + 1], o[4 * i + 2], o[4 * i + 3]};
}
__device__ __forceinline__ f32x4 asf4(v4u v) { return __builtin_bit_cast(f32x4, v); }


template <int U> __device__ __forceinline__ void drain(const unsigned (&wa)[4], v4u (&f)[8]) {
#pragma unroll
  for (int ks = 0; ks < 2; ++ks)
#pragma unroll
    for (int i = 0; i < 4; ++i) {
      const unsigned a0 = wa[i] + ((U & 31) + 4 * ks) * 4096u;
      f[4 * ks + i] = ldstr2(a0, a0 + 8192);
    }
  asm volatile("s_waitcnt lgkmcnt(0)"
               : "+v"(f[0]), "+v"(f[1]), "+v"(f[2]), "+v"(f[3]), "+v"(f[4]), "+v"(f[5]), "+v"(f[6]), "+v"(f[7]) :: "memory");
}
__device__ __forceinline__ f32x4 vecmat(v4u f0, v4u f1, const v4u (&f)[8]) {
  f32x4 r;
#pragma unroll
  for (int i = 0; i < 4; ++i) {
    f32x4 d = mfma(f0, f[i], f32x4{0.f, 0.f, 0.f, 0.f});
    d = mfma(f1, f[4 + i], d);
    r[i] = d[0];
  }
  return r;
}
__device__ __forceinline__ void red_gu(f32x4 a, float& G, float& U) {
  auto s0 = __builtin_amdgcn_permlane32_swap(asu(a[0]), asu(a[2]), false, false);
  auto s1 = __builtin_amdgcn_permlane32_swap(asu(a[1]), asu(a[3]), false, false);
  G = asf(s0[0]) + asf(s0[1]); U = asf(s1[0]) + asf(s1[1]);
  auto t0 = __builtin_amdgcn_permlane16_swap(asu(G), asu(G), false, false);
  auto t1 = __builtin_amdgcn_permlane16_swap(asu(U), asu(U), false, false);
  G = asf(t0[0]) + asf(t0[1]); U = asf(t1[0]) + asf(t1[1]);
  G += dpp<ROR8>(G); U += dpp<ROR8>(U);
}

__global__ void __launch_bounds__(320, 2)
mk(const unsigned short* __restrict__ x, const void* __restrict__ K, const void* __restrict__ V, const void* __restrict__ wq,
   const void* __restrict__ wo, const void* __restrict__ wg, const void* __restrict__ wu, const void* __restrict__ wd,
   unsigned short* out, unsigned* ws) {
  const int b = blockIdx.x;
  const int lane = threadIdx.x & 63;
  const int wv = __builtin_amdgcn_readfirstlane(threadIdx.x >> 6);
  const int g4 = lane >> 4, c16 = lane & 15;
  const bool ffn = b < 224;
  const unsigned cbD = (unsigned)b / 56u, rbD = (unsigned)b - 56u * cbD;

  if (wv == 0) {
    unsigned chV[4];
#pragma unroll
    for (int j = 0; j < 4; ++j) chV[j] = 16 * (c16 ^ (8 * (j & 1) + 2 * g4));
    lds_st32(L_CTL + 4 * lane, 0u);
    int u = 0, freed = 0, full = 0;
    int bud = BUDGET;
    auto space = [&](int need) {
      if (freed < need) {
        for (unsigned spins = 0;; ++spins) {
          const v4u p = lds_ld128_wait(L_PROG);
          freed = (int)__builtin_amdgcn_readfirstlane(min(min(p[0], p[1]), min(p[2], p[3])));
          if (freed >= need) break;
          if (spins == 0) { waitvm<0>(); full = u; lds_st32(L_FULL, (unsigned)full); }
          if (bud <= 0) break;
          --bud;
          __builtin_amdgcn_s_sleep(1);
        }
      }
    };
    auto step = [&](__amdgpu_buffer_rsrc_t rs, unsigned so, unsigned o0, unsigned o1, unsigned o2, unsigned o3) {
      space(u - 31);
      dma4(rs, so, (unsigned)(u & 31) * 4096u, o0, o1, o2, o3);
      ++u;
      waitvm<4 * INFL>();
      if (u - INFL > full) { full = u - INFL; lds_st32(L_FULL, (unsigned)full); }
    };
    auto settle = [&]() {
      if (INFL > 5) { waitvm<20>(); if (u - 5 > full) { full = u - 5; lds_st32(L_FULL, (unsigned)full); } }
      if (INFL > 4) { waitvm<16>(); if (u - 4 > full) { full = u - 4; lds_st32(L_FULL, (unsigned)full); } }
      waitvm<12>(); if (u - 3 > full) { full = u - 3; lds_st32(L_FULL, (unsigned)full); }
      waitvm<8>(); if (u - 2 > full) { full = u - 2; lds_st32(L_FULL, (unsigned)full); }
      waitvm<4>(); if (u - 1 > full) { full = u - 1; lds_st32(L_FULL, (unsigned)full); }
      waitvm<0>(); full = u; lds_st32(L_FULL, (unsigned)u);
    };
    auto stepp = [&](__amdgpu_buffer_rsrc_t rs, unsigned so, unsigned o0, unsigned o1, unsigned o2, unsigned o3) {
      space(u - 31);
      if (INFLP < INFL) {
        if (INFL > 4) { waitvm<16>(); if (u - 4 > full) { full = u - 4; lds_st32(L_FULL, (unsigned)full); } }
        if (INFLP < 4) { waitvm<12>(); if (u - 3 > full) { full = u - 3; lds_st32(L_FULL, (unsigned)full); } }
        if (INFLP < 3) { waitvm<8>(); if (u - 2 > full) { full = u - 2; lds_st32(L_FULL, (unsigned)full); } }
        if (INFLP < 2) { waitvm<4>(); if (u - 1 > full) { full = u - 1; lds_st32(L_FULL, (unsigned)full); } }
      }
      dma4(rs, so, (unsigned)(u & 31) * 4096u, o0, o1, o2, o3);
      ++u;
      waitvm<4 * INFLP>();
      if (u - INFLP > full) { full = u - INFLP; lds_st32(L_FULL, (unsigned)full); }
    };
    {
      const __amdgpu_buffer_rsrc_t r = mkrs(wq, 1024u * 4096u * 2u);
      unsigned o[4];
#pragma unroll
      for (int j = 0; j < 4; ++j) o[j] = (4 * j + g4) * 8192 + chV[j] - 1024 * j;
      const unsigned base = (unsigned)(b >> 5) * 128u * 8192u + (unsigned)(b & 31) * 256u;
#pragma unroll 1
      for (int i = 0; i < 8; ++i) {
        dma4(r, base + i * 16u * 8192u, (unsigned)u * 4096u, o[0], o[1], o[2], o[3]);
        ++u;
        if (i == 1) { asm volatile("s_waitcnt lgkmcnt(0)" ::: "memory"); __builtin_amdgcn_s_barrier(); }
        waitvm<4 * INFL0>();
      }
    }
    unsigned o2k[4], o7k[4];
#pragma unroll
    for (int j = 0; j < 4; ++j) {
      o2k[j] = (4 * j + g4) * 2048 + chV[j] - 1024 * j;
      o7k[j] = (4 * j + g4) * 7168 + chV[j] - 1024 * j;
    }
    {
      const __amdgpu_buffer_rsrc_t r = mkrs(wo, 4096u * 1024u * 2u);
      const unsigned base = (unsigned)(b & 63) * 64u * 2048u + (unsigned)(b >> 6) * 512u;
#pragma unroll 1
      for (int i = 0; i < 8; ++i) step(r, base + (i >> 1) * 16u * 2048u + (i & 1) * 256u, o2k[0], o2k[1], o2k[2], o2k[3]);
    }
    const unsigned bgu = (unsigned)(b & 15) * 64u * 7168u + (unsigned)(b >> 4) * 512u;
    const unsigned bd = rbD * 64u * 2048u + cbD * 512u;
    const __amdgpu_buffer_rsrc_t rg = mkrs(wg, 1024u * 3584u * 2u), ru = mkrs(wu, 1024u * 3584u * 2u), rd = mkrs(wd, 3584u * 1024u * 2u);
    if (ffn) {
#pragma unroll 1
      for (int i = 0; i < 8; ++i) step(rg, bgu + (i >> 1) * 16u * 7168u + (i & 1) * 256u, o7k[0], o7k[1], o7k[2], o7k[3]);
      if (NPOST < 16) {
#pragma unroll 1
        for (int i = 0; i < 8; ++i) step(ru, bgu + (i >> 1) * 16u * 7168u + (i & 1) * 256u, o7k[0], o7k[1], o7k[2], o7k[3]);
      }
      if (NPOST < 8) {
#pragma unroll 1
        for (int i = 0; i < 8; ++i) step(rd, bd + (i >> 1) * 16u * 2048u + (i & 1) * 256u, o2k[0], o2k[1], o2k[2], o2k[3]);
      }
    }
    {
      unsigned oK[4], oV[4];
#pragma unroll
      for (int j = 0; j < 4; ++j) {
        oK[j] = g4 * 256 + 16 * (c16 ^ (4 * j + g4));
        oV[j] = g4 * 256 + chV[j];
      }
      const __amdgpu_buffer_rsrc_t rk = mkrs(K, 1u << 26), rv = mkrs(V, 1u << 26);
      unsigned so = (unsigned)b << 18;
#pragma unroll 1
      for (int t = 0; t < 32; ++t, so += 8192) {
        step(rk, so, oK[0], oK[1], oK[2], oK[3]);
        step(rk, so + 4096, oK[0], oK[1], oK[2], oK[3]);
        step(rv, so, oV[0], oV[1], oV[2], oV[3]);
        step(rv, so + 4096, oV[0], oV[1], oV[2], oV[3]);
      }
    }
    if (NPOST > 0 && ffn) {
      if (HOLD >= 1) settle();
      if (HOLD >= 2) space(u + 1);
      if (NPOST >= 16) {
#pragma unroll 1
        for (int i = 0; i < 8; ++i) stepp(ru, bgu + (i >> 1) * 16u * 7168u + (i & 1) * 256u, o7k[0], o7k[1], o7k[2], o7k[3]);
      }
#pragma unroll 1
      for (int i = 0; i < 8; ++i) stepp(rd, bd + (i >> 1) * 16u * 2048u + (i & 1) * 256u, o2k[0], o2k[1], o2k[2], o2k[3]);
    }
    settle();
    return;
  }

  const int w = wv - 1;
  const __amdgpu_buffer_rsrc_t rs = mkrs(ws, WS_END);
  const unsigned T = *(const GAS unsigned*)ws + 1u;
  int bud = BUDGET;
  int seen = 0;
  const int sub = lane >> 2, p4 = lane & 3;
  const unsigned trrow = 256u * sub + 8u * p4;
  const int r16 = b & 15;
  const unsigned rbG = 4 * (b >> 4) + w;
  float xres = 0.f;
  const unsigned aPA = WS_PA + ((((b >> 6) * 8 + (c16 & 7)) * 8 + 2 * w + (c16 >> 3)) * 64 + (b & 63)) * 64 + g4 * 16;
  const unsigned aML = WS_ML + (((b >> 6) * 8 + (c16 & 7)) * 64 + (b & 63)) * 16;
  const unsigned aPB = WS_PB + (((4 * (b >> 6) + w) * 4 + (c16 >> 2)) * 64 + (b & 63)) * 64 + (c16 & 3) * 16;
  const unsigned aPC = ((rbG * 4 + 2 * (g4 & 1) + (c16 >> 3)) * 16 + r16) * 128 + (c16 & 7) * 16;
  const unsigned aST = WS_ST + (rbG * 16 + r16) * 16;
  const unsigned aPD = WS_PD + (((4 * cbD + w) * 16 + c16) * 64 + rbD) * 16;

  {
    const v4u xa = *(const GAS v4u*)(x + 16 * lane), xb = *(const GAS v4u*)(x + 16 * lane + 8);
    const unsigned xp = *(const GAS unsigned*)(x + 128 * (b >> 5) + 2 * lane);
    xres = lo16(x[64 * r16 + 16 * g4 + 4 * w + p4]);
    __builtin_amdgcn_s_barrier();
    float s = 0.f;
#pragma unroll
    for (int e = 0; e < 4; ++e) s += (lo16(xa[e]) + hi16(xa[e])) + (lo16(xb[e]) + hi16(xb[e]));
    const float mean = xrow_sum(row_sum(s)) * (1.f / 1024.f);
    float q = 0.f;
#pragma unroll
    for (int e = 0; e < 4; ++e) {
      const float d0 = lo16(xa[e]) - mean, d1 = hi16(xa[e]) - mean, d2 = lo16(xb[e]) - mean, d3 = hi16(xb[e]) - mean;
      q += (d0 * d0 + d1 * d1) + (d2 * d2 + d3 * d3);
    }
    const float var = xrow_sum(row_sum(q)) * (1.f / 1024.f);
    const float mb = rbf(mean), sd = rbf(sqrtf(rbf(rbf(var) + 1e-5f)));
    const unsigned xn = pk2(rbf(lo16(xp) - mb) / sd, rbf(hi16(xp) - mb) / sd);
    lds_st32(L_XN + w * 256 + (lane >> 4) * 64 + ((lane >> 1) & 3) * 16 + ((lane >> 3) & 1) * 8 + (lane & 1) * 4, xn);
  }
  {
    asm volatile("s_waitcnt lgkmcnt(0)" ::: "memory");
    v4u xf[4];
#pragma unroll
    for (int ks = 0; ks < 4; ++ks) xf[ks] = lds128(L_XN + w * 256 + ks * 64 + g4 * 16);
    wait_full(8, seen, bud);
    f32x4 d[2];
#pragma unroll
    for (int i = 0; i < 2; ++i) {
      const unsigned a = trrow + 32u * ((2 * w + i) ^ (sub & 7));
      d[i] = f32x4{0.f, 0.f, 0.f, 0.f};
#pragma unroll
      for (int ks = 0; ks < 4; ++ks) d[i] = mfma(ldstr2(a + ks * 8192, a + ks * 8192 + 4096), xf[ks], d[i]);
    }
    const f32x4 v = (c16 & 1) ? d[1] : d[0];
    if (c16 < 2) gst(rs, WS_QP + ((b & 31) * 8 + (b >> 5)) * 512 + (32 * w + 16 * c16 + 4 * g4) * 4, tag4(v, T));
    asm volatile("s_waitcnt lgkmcnt(0)" ::: "memory");
    lds_st32(L_PROG + 4 * w, 8u);
  }
  unsigned wa[4];
#pragma unroll
  for (int i = 0; i < 4; ++i) wa[i] = (w >> 1) * 4096u + trrow + 32u * ((4 * (w & 1) + i) ^ (sub & 7));
  v4u fo[8], fg[8], fu[8], fd[8];
  wait_full(U_WO + 8, seen, bud);
  drain<U_WO>(wa, fo);
  lds_st32(L_PROG + 4 * w, U_WO + 8u);
  {
    v4u v[1]; float d0, d1;
    gather<1, false, 0xFFu, 4>(rs, WS_QP + ((b & 31) * 8 + (lane >> 3)) * 512 + w * 128 + (lane & 7) * 16, 0, T, v, d0, d1, bud);
    v4u r;
#pragma unroll
    for (int e = 0; e < 4; ++e) {
      float a = asf(v[0][e]);
      a += dpp<ROR8>(a);
      a = xrow_sum(a);
      r[e] = (pk2(a, a) << 16) | (T & 0xFFFFu);
    }
    if (lane < 8) gst(rs, WS_QF + ((b >> 5) * 32 + (b & 31)) * 512 + w * 128 + lane * 16, r);
  }
  auto pretouch = [&]() {
    const unsigned nt = ~T;
    const v4u z = {nt, nt, nt, nt};
    gst(rs, aPA, z);
    if (w == 0 && lane < 8) gst(rs, aML, z);
    if (lane < 16) gst(rs, aPB, z);
    if (ffn) {
      if (lane < 32) gst(rs, WS_PC + aPC, z);
      if (lane == 0) gst(rs, aST, z);
      if (lane < 16) gst(rs, aPD, z);
    }
  };
  if (PRE == 1) pretouch();
  wait_full(U_G + 8, seen, bud);
  if (ffn) { drain<U_G>(wa, fg); lds_st32(L_PROG + 4 * w, U_G + 8u); }
  v4u qf[4];
  {
    v4u v[1]; float d0, d1;
    gather<1, false, 0xFFFFu, 4>(rs, WS_QF + ((b & 7) * 32 + (b >> 6) * 8 + 2 * w) * 512 + lane * 16, 0, T, v, d0, d1, bud);
    waitvm<0>();
    lds_st64(L_Q + w * 512 + lane * 8, v2u{(v[0][0] >> 16) | v[0][1], (v[0][2] >> 16) | v[0][3]});
    lds_st32(L_XF + 4 * w, 1u);
    exch(L_XF, L_Q + (c16 & 7) * 256 + g4 * 16, qf[0], qf[1], bud);
    qf[2] = lds128(L_Q + (c16 & 7) * 256 + g4 * 16 + 128);
    qf[3] = lds128(L_Q + (c16 & 7) * 256 + g4 * 16 + 192);
  }
  if (PRE == 2) pretouch();
  const v4u ones = {0x3F803F80u, 0x3F803F80u, 0x3F803F80u, 0x3F803F80u};
  float Cg = 0.f, Cu = 0.f;
  const unsigned gPC = (rbD * 4 + w) * 2048 + lane * 16;
  auto colsums = [&]() {
    const f32x4 cg = vecmat(ones, ones, fg), cu = vecmat(ones, ones, fu);
    const f32x4 r = (g4 & 1) ? f32x4{cg[2], cu[2], cg[3], cu[3]} : f32x4{cg[0], cu[0], cg[1], cu[1]};
    if (lane < 32) gst(rs, WS_CS + aPC, tag4(r, T));
  };
  auto getsums = [&]() {
    v4u v[2]; float d0, d1;
    gather<2, false, 0xFFu, 4>(rs, WS_CS + gPC, 0, T, v, d0, d1, bud);
    red_gu(asf4(v[0]) + asf4(v[1]), Cg, Cu);
  };
  if (ffn) {
    if (NPOST < 16) {
      wait_full(U_U + 8, seen, bud);
      drain<U_U>(wa, fu);
      lds_st32(L_PROG + 4 * w, U_U + 8u);
      colsums();
    }
    if (NPOST < 16) wait_full(40, seen, bud);
    if (NPOST < 8) { drain<U_D>(wa, fd); lds_st32(L_PROG + 4 * w, U_D + 8u); }
    if (NPOST < 16) getsums();
  }

  {
    unsigned ka[4], va[2];
#pragma unroll
    for (int u = 0; u < 4; ++u) ka[u] = 256u * c16 + 16u * ((4 * u + g4) ^ c16);
#pragma unroll
    for (int i = 0; i < 2; ++i) va[i] = 8192u + trrow + 32u * ((2 * w + i) ^ (sub & 7));
    const bool up = c16 >= 8;
    const float LOG2E = 1.4426950408889634f, scale = 0.08838834764831845f;
    float m = -INFINITY, l = 0.f;
    f32x4 acc[2] = {f32x4{0.f, 0.f, 0.f, 0.f}, f32x4{0.f, 0.f, 0.f, 0.f}};
    const int ukv = ffn ? U_KVF : 16;
#pragma unroll 1
    for (int u0 = ukv; u0 < ukv + 128; u0 += 4) {
      const unsigned tb = (unsigned)(u0 & 31) * 4096u;
      if (PRE == 3 && u0 == ukv + 64) pretouch();
      wait_full(u0 + 2, seen, bud);
      f32x4 s0 = {0.f, 0.f, 0.f, 0.f}, s1 = {0.f, 0.f, 0.f, 0.f};
#pragma unroll
      for (int u = 0; u < 4; ++u) {
        s0 = mfma(lds128(tb + ka[u]), qf[u], s0);
        s1 = mfma(lds128(tb + ka[u] + 4096), qf[u], s1);
      }
      float s[4];
#pragma unroll
      for (int r = 0; r < 4; ++r) s[r] = rbf(rbf(up ? s1[r] : s0[r]) * scale);
      const float mn = fmaxf(m, xrow_max(fmaxf(fmaxf(s[0], s[1]), fmaxf(s[2], s[3]))));
      const float al = __builtin_amdgcn_exp2f((m - mn) * LOG2E);
      m = mn;
      float p[4];
#pragma unroll
      for (int r = 0; r < 4; ++r) p[r] = __builtin_amdgcn_exp2f((s[r] - mn) * LOG2E);
      l = l * al + ((p[0] + p[1]) + (p[2] + p[3]));
      const unsigned p01 = pk2(p[0], p[1]), p23 = pk2(p[2], p[3]);
      const v4u pb = up ? v4u{0u, 0u, p01, p23} : v4u{p01, p23, 0u, 0u};
      acc[0] *= al; acc[1] *= al;
      wait_full(u0 + 4, seen, bud);
      const v4u v0 = ldstr2(tb + va[0], tb + va[0] + 4096), v1 = ldstr2(tb + va[1], tb + va[1] + 4096);
      acc[0] = mfma(v0, pb, acc[0]);
      acc[1] = mfma(v1, pb, acc[1]);
      asm volatile("s_waitcnt lgkmcnt(0)" ::: "memory");
      lds_st32(L_PROG + 4 * w, (unsigned)(u0 + 4));
    }
    const float M = fmaxf(m, dpp<ROR8>(m));
    const float f = __builtin_amdgcn_exp2f((m - M) * LOG2E);
    float lt = xrow_sum(l) * f;
    lt += dpp<ROR8>(lt);
    f32x4 o;
#pragma unroll
    for (int e = 0; e < 4; ++e) {
      const float a0 = acc[0][e] * f, a1 = acc[1][e] * f;
      o[e] = (up ? a1 : a0) + dpp<ROR8>(up ? a0 : a1);
    }
    gst(rs, aPA, tag4(o, T));
    if (w == 0 && lane < 8) gst(rs, aML, v4u{asu(M), T, asu(lt), T});
    if (HOLD >= 2) lds_st32(L_PROG + 4 * w, (unsigned)(ukv + 129));
  }

  {
    v4u v[4]; float ms, ls;
    if (DLA) __builtin_amdgcn_s_sleep(DLA);
    gather<4, true, 0xFFu, GAPA>(rs, WS_PA + ((b & 63) * 4 + w) * 4096u + lane * 16, WS_ML + (((b & 63) >> 1) * 64 + 16 * p4 + sub) * 16, T, v, ms, ls, bud);
    const float LOG2E = 1.4426950408889634f;
    const float M = xrow_max(row_max(ms));
    const float f = __builtin_amdgcn_exp2f((ms - M) * LOG2E);
    const float L = xrow_sum(row_sum(f * ls));
    f32x4 a = asf4(v[0]) * dpp<0x00>(f);
    a += asf4(v[1]) * dpp<0x55>(f);
    a += asf4(v[2]) * dpp<0xAA>(f);
    a += asf4(v[3]) * dpp<0xFF>(f);
    const float o = red_scatter(a) * __builtin_amdgcn_rcpf(L);
    if (c16 < 4) lds_st16(L_VEC + (w >> 1) * 64 + p4 * 16 + (w & 1) * 8 + g4 * 2, pk2(o, o));
    lds_st32(L_XF + 16 + 4 * w, 1u);
    v4u f0, f1;
    exch(L_XF + 16, L_VEC + g4 * 16, f0, f1, bud);
    const f32x4 r = vecmat(f0, f1, fo);
    if (lane < 16) gst(rs, aPB, tag4(r, T));
  }

  if (ffn) {
    float h;
    if (NPOST >= 16) {
      wait_full(U_U + 8, seen, bud);
      drain<U_U>(wa, fu);
      colsums();
    }
    {
      v4u v[4]; float d0, d1;
      if (DLB) __builtin_amdgcn_s_sleep(DLB);
      gather<4, false, 0xFFu, GAPB>(rs, WS_PB + (r16 * 4 + w) * 4096u + lane * 16, 0, T, v, d0, d1, bud);
      const f32x4 a = (asf4(v[0]) + asf4(v[1])) + (asf4(v[2]) + asf4(v[3]));
      h = rbf(xres + rbf(red_scatter(a)));
    }
    {
      if (c16 < 4) lds_st16(L_VEC + 128 + (g4 >> 1) * 64 + w * 16 + (g4 & 1) * 8 + p4 * 2, asu(h) >> 16);
      lds_st32(L_XF + 32 + 4 * w, 1u);
      v4u f0, f1;
      exch(L_XF + 32, L_VEC + 128 + g4 * 16, f0, f1, bud);
      const f32x4 sg = vecmat(f0, f1, fg), su = vecmat(f0, f1, fu);
      const f32x4 r = (g4 & 1) ? f32x4{sg[2], su[2], sg[3], su[3]} : f32x4{sg[0], su[0], sg[1], su[1]};
      if (lane < 32) gst(rs, WS_PC + aPC, tag4(r, T));
      f32x4 s2 = mfma(f0, f0, f32x4{0.f, 0.f, 0.f, 0.f}), s1 = mfma(f0, ones, f32x4{0.f, 0.f, 0.f, 0.f});
      s2 = mfma(f1, f1, s2); s1 = mfma(f1, ones, s1);
      if (lane == 0) gst(rs, aST, v4u{asu(s1[0]), T, asu(s2[0]), T});
    }
    if (NPOST >= 16) getsums();
    if (NPOST >= 8) {
      wait_full(U_D + 8, seen, bud);
      drain<U_D>(wa, fd);
    }
    {
      v4u v[2]; float sv0, sv2;
      if (DLC) __builtin_amdgcn_s_sleep(DLC);
      gather<2, true, 0xFFu, GAPB>(rs, WS_PC + gPC, WS_ST + (rbD * 16 + c16) * 16, T, v, sv0, sv2, bud);
      const float mean = row_sum(sv0) * (1.f / 1024.f);
      const float var = fmaxf(row_sum(sv2) * (1.f / 1024.f) - mean * mean, 0.f);
      const float mb = rbf(mean), sd = rbf(__builtin_amdgcn_sqrtf(rbf(rbf(var) + 1e-5f)));
      const float rsd = __builtin_amdgcn_rcpf(sd);
      float G, U;
      red_gu(asf4(v[0]) + asf4(v[1]), G, U);
      const float g = rbf((G - mb * Cg) * rsd), u = rbf((U - mb * Cu) * rsd);
      const float y = 0.7978845608028654f * (g + 0.044715f * g * g * g);
      const float ge = rbf(g * __builtin_amdgcn_rcpf(1.f + __builtin_amdgcn_exp2f(y * (-2.f * 1.4426950408889634f))));
      const float act = ge * u;
      if ((lane & 24) == 0) lds_st16(L_VEC + 256 + (w >> 1) * 64 + (2 * (w & 1) + ((lane >> 2) & 1)) * 16 + (lane >> 5) * 8 + p4 * 2, pk2(act, act));
      lds_st32(L_XF + 48 + 4 * w, 1u);
      v4u f0, f1;
      exch(L_XF + 48, L_VEC + 256 + g4 * 16, f0, f1, bud);
      const f32x4 r = vecmat(f0, f1, fd);
      if (lane < 16) gst(rs, aPD, tag4(r, T));
    }
    if (b < 64) {
      v4u v[1]; float d0, d1;
      if (DLD) __builtin_amdgcn_s_sleep(DLD);
      gather<1, false, 0xFFu, GAPB>(rs, WS_PD + ((r16 * 16 + 4 * w + (b >> 4)) * 64 + (lane < 56 ? lane : lane - 8)) * 16, 0, T, v, d0, d1, bud);
      if (lane >= 56) v[0] = v4u{0u, 0u, 0u, 0u};
      float c = red_scatter(asf4(v[0]));
      c += dpp<XOR1>(c); c += dpp<XOR2>(c);
      const float res = h + rbf(c);
      if (c16 == (b >> 4)) out[64 * r16 + 16 * g4 + 4 * w + p4] = (unsigned short)pk2(res, res);
      if (b == 0 && w == 0 && lane == 0) __hip_atomic_store((GAS unsigned*)ws, T, __ATOMIC_RELAXED, __HIP_MEMORY_SCOPE_AGENT);
    }
  }
}

extern "C" void kernel_launch(void* const* d_in, const int* in_sizes, int n_in, void* d_out, int out_size, void* d_ws,
                              size_t ws_size, hipStream_t stream) {
  (void)in_sizes; (void)n_in; (void)out_size; (void)ws_size;
  (void)hipFuncSetAttribute((const void*)mk, hipFuncAttributeMaxDynamicSharedMemorySize, (int)LDS_BYTES);
  mk<<<256, 320, LDS_BYTES, stream>>>((const unsigned short*)d_in[0], d_in[1], d_in[2], d_in[3], d_in[4], d_in[6], d_in[7], d_in[8],
                                      (unsigned short*)d_out, (unsigned*)d_ws);
}
```

```cpp
#include <hip/hip_runtime.h>
#include <stdint.h>

typedef unsigned v4u __attribute__((ext_vector_type(4)));
typedef unsigned v2u __attribute__((ext_vector_type(2)));
typedef float f32x4 __attribute__((ext_vector_type(4)));
typedef float f32x2 __attribute__((ext_vector_type(2)));
typedef __bf16 bf16x8_t __attribute__((ext_vector_type(8)));
typedef __bf16 bf16x2_t __attribute__((ext_vector_type(2)));
typedef short s16x4 __attribute__((ext_vector_type(4)));
#define LAS __attribute__((address_space(3)))
#define GAS __attribute__((address_space(1)))

constexpr unsigned WS_QP = 0x010000;
constexpr unsigned WS_QF = 0x030000;
constexpr unsigned WS_ML = 0x050000;
constexpr unsigned WS_ST = 0x058000;
constexpr unsigned WS_PA = 0x100000;
constexpr unsigned WS_PB = 0x200000;
constexpr unsigned WS_PC = 0x280000;
constexpr unsigned WS_CS = 0x300000;
constexpr unsigned WS_PD = 0x380000;
constexpr unsigned WS_END = 0x400000;

constexpr unsigned L_CTL = 131072;
constexpr unsigned L_FULL = L_CTL;
constexpr unsigned L_PROG = L_CTL + 16;
constexpr unsigned L_XF = L_CTL + 32;
constexpr unsigned L_VEC = L_CTL + 256;
constexpr unsigned L_XN = L_CTL + 1024;
constexpr unsigned L_Q = L_CTL + 2048;
constexpr unsigned L_TR = L_CTL + 4096;
constexpr unsigned LDS_BYTES = L_CTL + 4096;

#define NPOST 16
#define INFL 5
#define INFL0 6
#define INFLP 4
#define HOLD 0
#define PRE 0
#define DLA 12
#define DLB 4
#define DLC 0
#define DLD 12
#define GAPA 0
#define GAPB 0
constexpr int U_WO = 8, U_G = 16, U_KVF = 40 - NPOST;
constexpr int U_U = NPOST >= 16 ? U_KVF + 128 : 24;
constexpr int U_D = NPOST >= 8 ? 160 : 32;
constexpr int BUDGET = 1 << 20;

__device__ __forceinline__ float asf(unsigned u) { return __builtin_bit_cast(float, u); }
__device__ __forceinline__ unsigned asu(float f) { return __builtin_bit_cast(unsigned, f); }
__device__ __forceinline__ unsigned pk2(float lo, float hi) {
  return __builtin_bit_cast(unsigned, __builtin_convertvector(f32x2{lo, hi}, bf16x2_t));
}
__device__ __forceinline__ float rbf(float f) { return asf(pk2(f, f) << 16); }
__device__ __forceinline__ float lo16(unsigned u) { return asf(u << 16); }
__device__ __forceinline__ float hi16(unsigned u) { return asf(u & 0xFFFF0000u); }
__device__ __forceinline__ f32x4 mfma(v4u a, v4u b, f32x4 c) {
  return __builtin_amdgcn_mfma_f32_16x16x32_bf16(__builtin_bit_cast(bf16x8_t, a), __builtin_bit_cast(bf16x8_t, b), c, 0, 0, 0);
}
template <int CTRL> __device__ __forceinline__ float dpp(float x) {
  return asf((unsigned)__builtin_amdgcn_mov_dpp((int)asu(x), CTRL, 0xf, 0xf, true));
}
constexpr int XOR1 = 0xB1, XOR2 = 0x4E, ROR4 = 0x124, ROR8 = 0x128;
__device__ __forceinline__ float xrow_max(float x) {
  auto s = __builtin_amdgcn_permlane16_swap(asu(x), asu(x), false, false);
  x = fmaxf(asf(s[0]), asf(s[1]));
  auto t = __builtin_amdgcn_permlane32_swap(asu(x), asu(x), false, false);
  return fmaxf(asf(t[0]), asf(t[1]));
}
__device__ __forceinline__ float xrow_sum(float x) {
  auto s = __builtin_amdgcn_permlane16_swap(asu(x), asu(x), false, false);
  x = asf(s[0]) + asf(s[1]);
  auto t = __builtin_amdgcn_permlane32_swap(asu(x), asu(x), false, false);
  return asf(t[0]) + asf(t[1]);
}
__device__ __forceinline__ float row_sum(float x) {
  x += dpp<XOR1>(x); x += dpp<XOR2>(x); x += dpp<ROR4>(x); x += dpp<ROR8>(x); return x;
}
__device__ __forceinline__ float row_max(float x) {
  x = fmaxf(x, dpp<XOR1>(x)); x = fmaxf(x, dpp<XOR2>(x)); x = fmaxf(x, dpp<ROR4>(x)); x = fmaxf(x, dpp<ROR8>(x)); return x;
}
__device__ __forceinline__ float red_scatter(f32x4 a) {
  auto s0 = __builtin_amdgcn_permlane32_swap(asu(a[0]), asu(a[2]), false, false);
  auto s1 = __builtin_amdgcn_permlane32_swap(asu(a[1]), asu(a[3]), false, false);
  const float b0 = asf(s0[0]) + asf(s0[1]), b1 = asf(s1[0]) + asf(s1[1]);
  auto t = __builtin_amdgcn_permlane16_swap(asu(b0), asu(b1), false, false);
  float c = asf(t[0]) + asf(t[1]);
  c += dpp<ROR4>(c); c += dpp<ROR8>(c);
  return c;
}
template <int N> __device__ __forceinline__ void waitvm() { asm volatile("s_waitcnt vmcnt(%0)" ::"n"(N) : "memory"); }
__device__ __forceinline__ void lds_st32(unsigned a, unsigned v) { asm volatile("ds_write_b32 %0, %1" ::"v"(a), "v"(v) : "memory"); }
__device__ __forceinline__ void lds_st16(unsigned a, unsigned v) { asm volatile("ds_write_b16 %0, %1" ::"v"(a), "v"(v) : "memory"); }
__device__ __forceinline__ void lds_st64(unsigned a, v2u v) { asm volatile("ds_write_b64 %0, %1" ::"v"(a), "v"(v) : "memory"); }
__device__ __forceinline__ unsigned lds_ld32_wait(unsigned a) {
  unsigned r; asm volatile("ds_read_b32 %0, %1\n\ts_waitcnt lgkmcnt(0)" : "=v"(r) : "v"(a) : "memory"); return r;
}
__device__ __forceinline__ v4u lds_ld128_wait(unsigned a) {
  v4u r; asm volatile("ds_read_b128 %0, %1\n\ts_waitcnt lgkmcnt(0)" : "=v"(r) : "v"(a) : "memory"); return r;
}
__device__ __forceinline__ v4u lds128(unsigned a) { return *(const LAS v4u*)(uintptr_t)a; }
__device__ __forceinline__ v2u ldstr(unsigned a) {
  return __builtin_bit_cast(v2u, __builtin_amdgcn_ds_read_tr16_b64_v4i16((LAS s16x4*)(uintptr_t)a));
}
__device__ __forceinline__ v4u ldstr2(unsigned a0, unsigned a1) {
  const v2u x = ldstr(a0), y = ldstr(a1); return v4u{x[0], x[1], y[0], y[1]};
}
__device__ __forceinline__ void dma4(__amdgpu_buffer_rsrc_t r, unsigned so, unsigned m, unsigned o0, unsigned o1, unsigned o2, unsigned o3) {
  asm volatile("s_mov_b32 m0, %0\n\ts_nop 0\n\t"
               "buffer_load_dwordx4 %1, %5, %6 offen nt lds\n\t"
               "buffer_load_dwordx4 %2, %5, %6 offen offset:1024 nt lds\n\t"
               "buffer_load_dwordx4 %3, %5, %6 offen offset:2048 nt lds\n\t"
               "buffer_load_dwordx4 %4, %5, %6 offen offset:3072 nt lds"
               ::"s"(m), "v"(o0), "v"(o1), "v"(o2), "v"(o3), "s"(r), "s"(so) : "memory");
}
__device__ __forceinline__ __amdgpu_buffer_rsrc_t mkrs(const void* p, unsigned bytes) {
  return __builtin_amdgcn_make_buffer_rsrc((void*)p, (short)0, (int)bytes, 0x00020000);
}
__device__ __forceinline__ v4u gld(__amdgpu_buffer_rsrc_t rs, unsigned vo, unsigned so) { return __builtin_amdgcn_raw_buffer_load_b128(rs, (int)vo, (int)so, 16); }
__device__ __forceinline__ void gst(__amdgpu_buffer_rsrc_t rs, unsigned vo, v4u v) { __builtin_amdgcn_raw_buffer_store_b128(v, rs, (int)vo, 0, 16); }
__device__ __forceinline__ v4u tag4(f32x4 a, unsigned T) {
  v4u r;
#pragma unroll
  for (int e = 0; e < 4; ++e) r[e] = (asu(a[e]) & ~0xFFu) | (T & 0xFFu);
  return r;
}
__device__ __forceinline__ bool none(bool p) { return __builtin_amdgcn_ballot_w64(p) == 0; }

__device__ __forceinline__ void wait_full(int need, int& seen, int& bud) {
  if (seen >= need) return;
  for (;;) {
    seen = (int)__builtin_amdgcn_readfirstlane(lds_ld32_wait(L_FULL));
    if (seen >= need || bud <= 0) return;
    --bud;
    __builtin_amdgcn_s_sleep(1);
  }
}
__device__ __forceinline__ void exch(unsigned flags, unsigned a, v4u& f0, v4u& f1, int& bud) {
  asm volatile("1:\n\tds_read_b128 v[228:231], %[fl]\n\tds_read_b128 %[f0], %[a]\n\tds_read_b128 %[f1], %[a] offset:64\n\t"
               "s_waitcnt lgkmcnt(2)\n\t"
               "v_and_b32 v228, v228, v229\n\tv_bitop3_b32 v228, v228, v230, v231 bitop3:0x80\n\t"
               "v_cmp_eq_u32 vcc, 0, v228\n\ts_waitcnt lgkmcnt(0)\n\ts_cbranch_vccz 2f\n\t"
               "s_sub_u32 %[cnt], %[cnt], 1\n\ts_cmp_gt_i32 %[cnt], 0\n\ts_cbranch_scc1 1b\n\t2:"
               : [f0] "=&v"(f0), [f1] "=&v"(f1), [cnt] "+s"(bud) : [fl] "v"(flags), [a] "v"(a)
               : "memory", "vcc", "scc", "v228", "v229", "v230", "v231");
}
#define G_LD(R, OFF) "buffer_load_dwordx4 " R ", %[vo], %[rs], 0 offen offset:" #OFF " sc1\n\t"
#define G_LDX "buffer_load_dwordx4 v[224:227], %[xo], %[rs], 0 offen sc1\n\t"
#define G_CHK(W, A0, A1, A2, A3, R0, R1, R2, R3, B)                                                             \
  "s_waitcnt vmcnt(" #W ")\n\t"                                                                                 \
  "v_xor_b32 %[" #A0 "], %[tm], " R0 "\n\tv_xor_b32 %[" #A1 "], %[tm], " R1 "\n\t"                                 \
  "v_xor_b32 %[" #A2 "], %[tm], " R2 "\n\tv_xor_b32 %[" #A3 "], %[tm], " R3 "\n\t"                                 \
  "v_or3_b32 v233, %[" #A0 "], %[" #A1 "], %[" #A2 "]\n\t"                                                        \
  "v_bitop3_b32 " B ", v233, %[mk], %[" #A3 "] bitop3:0xc8\n\t"
#define G_CHKX(W) "s_waitcnt vmcnt(" #W ")\n\tv_bitop3_b32 v232, v225, v227, %[t] bitop3:0x7e\n\tv_mov_b32 %[x0], v224\n\tv_mov_b32 %[x2], v226\n\t"
#define G_TEST(B) "v_cmp_ne_u32 vcc, 0, " B "\n\ts_cbranch_vccz 2f\n\ts_sub_u32 %[cnt], %[cnt], 1\n\ts_cmp_gt_i32 %[cnt], 0\n\ts_cbranch_scc0 2f\n\ts_sleep %[gap]\n\t"
#define G_RE(B, LD) "v_cmp_ne_u32 vcc, 0, " B "\n\ts_cbranch_vccz 3f\n\ts_mov_b64 exec, vcc\n\t" LD "s_mov_b64 exec, -1\n\t3:\n\t"
#define G_END "s_waitcnt vmcnt(0)\n\ts_branch 1b\n\t2:"
#define G_C0(W) G_CHK(W, a0, a1, a2, a3, "v208", "v209", "v210", "v211", "v228")
#define G_C1(W) G_CHK(W, b0, b1, b2, b3, "v212", "v213", "v214", "v215", "v229")
#define G_C2(W) G_CHK(W, c0, c1, c2, c3, "v216", "v217", "v218", "v219", "v230")
#define G_C3(W) G_CHK(W, d0, d1, d2, d3, "v220", "v221", "v222", "v223", "v231")
#define G_L0 G_LD("v[208:211]", 0)
#define G_L1 G_LD("v[212:215]", 1024)
#define G_L2 G_LD("v[216:219]", 2048)
#define G_L3 G_LD("v[220:223]", 3072)
#define G_OA [a0] "=&v"(o[0]), [a1] "=&v"(o[1]), [a2] "=&v"(o[2]), [a3] "=&v"(o[3])
#define G_OB [b0] "=&v"(o[4]), [b1] "=&v"(o[5]), [b2] "=&v"(o[6]), [b3] "=&v"(o[7])
#define G_OC [c0] "=&v"(o[8]), [c1] "=&v"(o[9]), [c2] "=&v"(o[10]), [c3] "=&v"(o[11]), \
             [d0] "=&v"(o[12]), [d1] "=&v"(o[13]), [d2] "=&v"(o[14]), [d3] "=&v"(o[15])
#define G_OX [x0] "=&v"(x0), [x2] "=&v"(x2)
#define G_IN [vo] "v"(vo), [rs] "s"(rs), [tm] "s"(T & MASK), [mk] "s"(MASK), [gap] "n"(GAP)
#define G_INX [xo] "v"(xo), [t] "s"(T)
#define G_CLOB "memory", "vcc", "scc", "v208", "v209", "v210", "v211", "v212", "v213", "v214", "v215", "v216", "v217", "v218", "v219", \
               "v220", "v221", "v222", "v223", "v224", "v225", "v226", "v227", "v228", "v229", "v230", "v231", "v232", "v233"
template <int N, bool X, unsigned MASK, int GAP>
__device__ __forceinline__ void gather(__amdgpu_buffer_rsrc_t rs, unsigned vo, unsigned xo, unsigned T, v4u (&v)[N], float& x0, float& x2, int& cnt) {
  unsigned o[16];
  if constexpr (N == 1) {
    asm volatile(G_L0 "1:\n\t" G_C0(0) G_TEST("v228") G_RE("v228", G_L0) G_END
                 : G_OA, [cnt] "+s"(cnt) : G_IN : G_CLOB);
  } else if constexpr (N == 2 && !X) {
    asm volatile(G_L0 G_L1 "1:\n\t" G_C0(1) G_C1(0) "v_or_b32 v233, v228, v229\n\t" G_TEST("v233") G_RE("v228", G_L0) G_RE("v229", G_L1) G_END
                 : G_OA, G_OB, [cnt] "+s"(cnt) : G_IN : G_CLOB);
  } else if constexpr (N == 2 && X) {
    asm volatile(G_LDX G_L0 G_L1 "1:\n\t" G_CHKX(2) G_C0(1) G_C1(0) "v_or3_b32 v233, v228, v229, v232\n\t" G_TEST("v233")
                 G_RE("v232", G_LDX) G_RE("v228", G_L0) G_RE("v229", G_L1) G_END
                 : G_OA, G_OB, G_OX, [cnt] "+s"(cnt) : G_IN, G_INX : G_CLOB);
  } else if constexpr (N == 4 && !X) {
    asm volatile(G_L0 G_L1 G_L2 G_L3 "1:\n\t" G_C0(3) G_C1(2) G_C2(1) "v_or3_b32 v232, v228, v229, v230\n\t" G_C3(0) "v_or_b32 v233, v232, v231\n\t" G_TEST("v233")
                 G_RE("v228", G_L0) G_RE("v229", G_L1) G_RE("v230", G_L2) G_RE("v231", G_L3) G_END
                 : G_OA, G_OB, G_OC, [cnt] "+s"(cnt) : G_IN : G_CLOB);
  } else {
    asm volatile(G_LDX G_L0 G_L1 G_L2 G_L3 "1:\n\t" G_CHKX(4) G_C0(3) G_C1(2) G_C2(1) G_C3(0)
                 "v_or3_b32 v233, v228, v229, v230\n\tv_or3_b32 v233, v233, v231, v232\n\t" G_TEST("v233")
                 G_RE("v232", G_LDX) G_RE("v228", G_L0) G_RE("v229", G_L1) G_RE("v230", G_L2) G_RE("v231", G_L3) G_END
                 : G_OA, G_OB, G_OC, G_OX, [cnt] "+s"(cnt) : G_IN, G_INX : G_CLOB);
  }
#pragma unroll
  for (int i = 0; i < N; ++i) v[i] = v4u{o[4 * i], o[4 * i + 1], o[4 * i + 2], o[4 * i + 3]};
}
__device__ __forceinline__ f32x4 asf4(v4u v) { return __builtin_bit_cast(f32x4, v); }


template <int U> __device__ __forceinline__ void drain(const unsigned (&wa)[4], v4u (&f)[8]) {
#pragma unroll
  for (int ks = 0; ks < 2; ++ks)
#pragma unroll
    for (int i = 0; i < 4; ++i) {
      const unsigned a0 = wa[i] + ((U & 31) + 4 * ks) * 4096u;
      f[4 * ks + i] = ldstr2(a0, a0 + 8192);
    }
  asm volatile("s_waitcnt lgkmcnt(0)"
               : "+v"(f[0]), "+v"(f[1]), "+v"(f[2]), "+v"(f[3]), "+v"(f[4]), "+v"(f[5]), "+v"(f[6]), "+v"(f[7]) :: "memory");
}
__device__ __forceinline__ f32x4 vecmat(v4u f0, v4u f1, const v4u (&f)[8]) {
  f32x4 r;
#pragma unroll
  for (int i = 0; i < 4; ++i) {
    f32x4 d = mfma(f0, f[i], f32x4{0.f, 0.f, 0.f, 0.f});
    d = mfma(f1, f[4 + i], d);
    r[i] = d[0];
  }
  return r;
}
__device__ __forceinline__ void red_gu(f32x4 a, float& G, float& U) {
  auto s0 = __builtin_amdgcn_permlane32_swap(asu(a[0]), asu(a[2]), false, false);
  auto s1 = __builtin_amdgcn_permlane32_swap(asu(a[1]), asu(a[3]), false, false);
  G = asf(s0[0]) + asf(s0[1]); U = asf(s1[0]) + asf(s1[1]);
  auto t0 = __builtin_amdgcn_permlane16_swap(asu(G), asu(G), false, false);
  auto t1 = __builtin_amdgcn_permlane16_swap(asu(U), asu(U), false, false);
  G = asf(t0[0]) + asf(t0[1]); U = asf(t1[0]) + asf(t1[1]);
  G += dpp<ROR8>(G); U += dpp<ROR8>(U);
}

__global__ void __launch_bounds__(320, 2)
mk(const unsigned short* __restrict__ x, const void* __restrict__ K, const void* __restrict__ V, const void* __restrict__ wq,
   const void* __restrict__ wo, const void* __restrict__ wg, const void* __restrict__ wu, const void* __restrict__ wd,
   unsigned short* out, unsigned* ws) {
  const int b = blockIdx.x;
  const int lane = threadIdx.x & 63;
  const int wv = __builtin_amdgcn_readfirstlane(threadIdx.x >> 6);
  const int g4 = lane >> 4, c16 = lane & 15;
  const bool ffn = b < 224;
  const unsigned cbD = (unsigned)b / 56u, rbD = (unsigned)b - 56u * cbD;

  if (wv == 0) {
    unsigned chV[4];
#pragma unroll
    for (int j = 0; j < 4; ++j) chV[j] = 16 * (c16 ^ (8 * (j & 1) + 2 * g4));
    lds_st32(L_CTL + 4 * lane, 0u);
    int u = 0, freed = 0, full = 0;
    int bud = BUDGET;
    auto space = [&](int need) {
      if (freed < need) {
        for (unsigned spins = 0;; ++spins) {
          const v4u p = lds_ld128_wait(L_PROG);
          freed = (int)__builtin_amdgcn_readfirstlane(min(min(p[0], p[1]), min(p[2], p[3])));
          if (freed >= need) break;
          if (spins == 0) { waitvm<0>(); full = u; lds_st32(L_FULL, (unsigned)full); }
          if (bud <= 0) break;
          --bud;
          __builtin_amdgcn_s_sleep(1);
        }
      }
    };
    auto step = [&](__amdgpu_buffer_rsrc_t rs, unsigned so, unsigned o0, unsigned o1, unsigned o2, unsigned o3) {
      space(u - 31);
      dma4(rs, so, (unsigned)(u & 31) * 4096u, o0, o1, o2, o3);
      ++u;
      waitvm<4 * INFL>();
      if (u - INFL > full) { full = u - INFL; lds_st32(L_FULL, (unsigned)full); }
    };
    auto settle = [&]() {
      if (INFL > 5) { waitvm<20>(); if (u - 5 > full) { full = u - 5; lds_st32(L_FULL, (unsigned)full); } }
      if (INFL > 4) { waitvm<16>(); if (u - 4 > full) { full = u - 4; lds_st32(L_FULL, (unsigned)full); } }
      waitvm<12>(); if (u - 3 > full) { full = u - 3; lds_st32(L_FULL, (unsigned)full); }
      waitvm<8>(); if (u - 2 > full) { full = u - 2; lds_st32(L_FULL, (unsigned)full); }
      waitvm<4>(); if (u - 1 > full) { full = u - 1; lds_st32(L_FULL, (unsigned)full); }
      waitvm<0>(); full = u; lds_st32(L_FULL, (unsigned)u);
    };
    auto stepp = [&](__amdgpu_buffer_rsrc_t rs, unsigned so, unsigned o0, unsigned o1, unsigned o2, unsigned o3) {
      space(u - 31);
      if (INFLP < INFL) {
        if (INFL > 4) { waitvm<16>(); if (u - 4 > full) { full = u - 4; lds_st32(L_FULL, (unsigned)full); } }
        if (INFLP < 4) { waitvm<12>(); if (u - 3 > full) { full = u - 3; lds_st32(L_FULL, (unsigned)full); } }
        if (INFLP < 3) { waitvm<8>(); if (u - 2 > full) { full = u - 2; lds_st32(L_FULL, (unsigned)full); } }
        if (INFLP < 2) { waitvm<4>(); if (u - 1 > full) { full = u - 1; lds_st32(L_FULL, (unsigned)full); } }
      }
      dma4(rs, so, (unsigned)(u & 31) * 4096u, o0, o1, o2, o3);
      ++u;
      waitvm<4 * INFLP>();
      if (u - INFLP > full) { full = u - INFLP; lds_st32(L_FULL, (unsigned)full); }
    };
    {
      const __amdgpu_buffer_rsrc_t r = mkrs(wq, 1024u * 4096u * 2u);
      unsigned o[4];
#pragma unroll
      for (int j = 0; j < 4; ++j) o[j] = (4 * j + g4) * 8192 + chV[j] - 1024 * j;
      const unsigned base = (unsigned)(b >> 5) * 128u * 8192u + (unsigned)(b & 31) * 256u;
#pragma unroll 1
      for (int i = 0; i < 8; ++i) {
        dma4(r, base + i * 16u * 8192u, (unsigned)u * 4096u, o[0], o[1], o[2], o[3]);
        ++u;
        if (i == 1) { asm volatile("s_waitcnt lgkmcnt(0)" ::: "memory"); __builtin_amdgcn_s_barrier(); }
        waitvm<4 * INFL0>();
      }
    }
    unsigned o2k[4], o7k[4];
#pragma unroll
    for (int j = 0; j < 4; ++j) {
      o2k[j] = (4 * j + g4) * 2048 + chV[j] - 1024 * j;
      o7k[j] = (4 * j + g4) * 7168 + chV[j] - 1024 * j;
    }
    {
      const __amdgpu_buffer_rsrc_t r = mkrs(wo, 4096u * 1024u * 2u);
      const unsigned base = (unsigned)(b & 63) * 64u * 2048u + (unsigned)(b >> 6) * 512u;
#pragma unroll 1
      for (int i = 0; i < 8; ++i) step(r, base + (i >> 1) * 16u * 2048u + (i & 1) * 256u, o2k[0], o2k[1], o2k[2], o2k[3]);
    }
    const unsigned bgu = (unsigned)(b & 15) * 64u * 7168u + (unsigned)(b >> 4) * 512u;
    const unsigned bd = rbD * 64u * 2048u + cbD * 512u;
    const __amdgpu_buffer_rsrc_t rg = mkrs(wg, 1024u * 3584u * 2u), ru = mkrs(wu, 1024u * 3584u * 2u), rd = mkrs(wd, 3584u * 1024u * 2u);
    if (ffn) {
#pragma unroll 1
      for (int i = 0; i < 8; ++i) step(rg, bgu + (i >> 1) * 16u * 7168u + (i & 1) * 256u, o7k[0], o7k[1], o7k[2], o7k[3]);
      if (NPOST < 16) {
#pragma unroll 1
        for (int i = 0; i < 8; ++i) step(ru, bgu + (i >> 1) * 16u * 7168u + (i & 1) * 256u, o7k[0], o7k[1], o7k[2], o7k[3]);
      }
      if (NPOST < 8) {
#pragma unroll 1
        for (int i = 0; i < 8; ++i) step(rd, bd + (i >> 1) * 16u * 2048u + (i & 1) * 256u, o2k[0], o2k[1], o2k[2], o2k[3]);
      }
    }
    {
      unsigned oK[4], oV[4];
#pragma unroll
      for (int j = 0; j < 4; ++j) {
        oK[j] = g4 * 256 + 16 * (c16 ^ (4 * j + g4));
        oV[j] = g4 * 256 + chV[j];
      }
      const __amdgpu_buffer_rsrc_t rk = mkrs(K, 1u << 26), rv = mkrs(V, 1u << 26);
      unsigned so = (unsigned)b << 18;
#pragma unroll 1
      for (int t = 0; t < 32; ++t, so += 8192) {
        step(rk, so, oK[0], oK[1], oK[2], oK[3]);
        step(rk, so + 4096, oK[0], oK[1], oK[2], oK[3]);
        step(rv, so, oV[0], oV[1], oV[2], oV[3]);
        step(rv, so + 4096, oV[0], oV[1], oV[2], oV[3]);
      }
    }
    if (NPOST > 0 && ffn) {
      if (HOLD >= 1) settle();
      if (HOLD >= 2) space(u + 1);
      if (NPOST >= 16) {
#pragma unroll 1
        for (int i = 0; i < 8; ++i) stepp(ru, bgu + (i >> 1) * 16u * 7168u + (i & 1) * 256u, o7k[0], o7k[1], o7k[2], o7k[3]);
      }
#pragma unroll 1
      for (int i = 0; i < 8; ++i) stepp(rd, bd + (i >> 1) * 16u * 2048u + (i & 1) * 256u, o2k[0], o2k[1], o2k[2], o2k[3]);
    }
    settle();
    return;
  }

  const int w = wv - 1;
  const __amdgpu_buffer_rsrc_t rs = mkrs(ws, WS_END);
  const unsigned T = *(const GAS unsigned*)ws + 1u;
  int bud = BUDGET;
  int seen = 0;
  unsigned trig = 0;
  if (b < 64) {
    const v4u p = *((const GAS v4u*)((const GAS char*)ws + WS_PD) + (b * 4 + w) * 64 + lane);
    const unsigned ref = T - 1;
    trig = __builtin_popcountll(__builtin_amdgcn_ballot_w64(lane < 56 && (((p[0]^p[1])|(p[0]^p[2])|(p[0]^p[3]))&0xFF)==0 && ((p[0]^ref)&0xFF)!=0));
  }
  const int sub = lane >> 2, p4 = lane & 3;
  const unsigned trrow = 256u * sub + 8u * p4;
  const int r16 = b & 15;
  const unsigned rbG = 4 * (b >> 4) + w;
  float xres = 0.f;
  const unsigned aPA = WS_PA + ((((b >> 6) * 8 + (c16 & 7)) * 8 + 2 * w + (c16 >> 3)) * 64 + (b & 63)) * 64 + g4 * 16;
  const unsigned aML = WS_ML + (((b >> 6) * 8 + (c16 & 7)) * 64 + (b & 63)) * 16;
  const unsigned aPB = WS_PB + (((4 * (b >> 6) + w) * 4 + (c16 >> 2)) * 64 + (b & 63)) * 64 + (c16 & 3) * 16;
  const unsigned aPC = ((rbG * 4 + 2 * (g4 & 1) + (c16 >> 3)) * 16 + r16) * 128 + (c16 & 7) * 16;
  const unsigned aST = WS_ST + (rbG * 16 + r16) * 16;
  const unsigned aPD = WS_PD + (((4 * cbD + w) * 16 + c16) * 64 + rbD) * 16;

  {
    const v4u xa = *(const GAS v4u*)(x + 16 * lane), xb = *(const GAS v4u*)(x + 16 * lane + 8);
    const unsigned xp = *(const GAS unsigned*)(x + 128 * (b >> 5) + 2 * lane);
    xres = lo16(x[64 * r16 + 16 * g4 + 4 * w + p4]);
    __builtin_amdgcn_s_barrier();
    float s = 0.f;
#pragma unroll
    for (int e = 0; e < 4; ++e) s += (lo16(xa[e]) + hi16(xa[e])) + (lo16(xb[e]) + hi16(xb[e]));
    const float mean = xrow_sum(row_sum(s)) * (1.f / 1024.f);
    float q = 0.f;
#pragma unroll
    for (int e = 0; e < 4; ++e) {
      const float d0 = lo16(xa[e]) - mean, d1 = hi16(xa[e]) - mean, d2 = lo16(xb[e]) - mean, d3 = hi16(xb[e]) - mean;
      q += (d0 * d0 + d1 * d1) + (d2 * d2 + d3 * d3);
    }
    const float var = xrow_sum(row_sum(q)) * (1.f / 1024.f);
    const float mb = rbf(mean), sd = rbf(sqrtf(rbf(rbf(var) + 1e-5f)));
    const unsigned xn = pk2(rbf(lo16(xp) - mb) / sd, rbf(hi16(xp) - mb) / sd);
    lds_st32(L_XN + w * 256 + (lane >> 4) * 64 + ((lane >> 1) & 3) * 16 + ((lane >> 3) & 1) * 8 + (lane & 1) * 4, xn);
  }
  {
    asm volatile("s_waitcnt lgkmcnt(0)" ::: "memory");
    v4u xf[4];
#pragma unroll
    for (int ks = 0; ks < 4; ++ks) xf[ks] = lds128(L_XN + w * 256 + ks * 64 + g4 * 16);
    wait_full(8, seen, bud);
    f32x4 d[2];
#pragma unroll
    for (int i = 0; i < 2; ++i) {
      const unsigned a = trrow + 32u * ((2 * w + i) ^ (sub & 7));
      d[i] = f32x4{0.f, 0.f, 0.f, 0.f};
#pragma unroll
      for (int ks = 0; ks < 4; ++ks) d[i] = mfma(ldstr2(a + ks * 8192, a + ks * 8192 + 4096), xf[ks], d[i]);
    }
    const f32x4 v = (c16 & 1) ? d[1] : d[0];
    if (c16 < 2) gst(rs, WS_QP + ((b & 31) * 8 + (b >> 5)) * 512 + (32 * w + 16 * c16 + 4 * g4) * 4, tag4(v, T));
    asm volatile("s_waitcnt lgkmcnt(0)" ::: "memory");
    lds_st32(L_PROG + 4 * w, 8u);
  }
  unsigned wa[4];
#pragma unroll
  for (int i = 0; i < 4; ++i) wa[i] = (w >> 1) * 4096u + trrow + 32u * ((4 * (w & 1) + i) ^ (sub & 7));
  v4u fo[8], fg[8], fu[8], fd[8];
  wait_full(U_WO + 8, seen, bud);
  drain<U_WO>(wa, fo);
  lds_st32(L_PROG + 4 * w, U_WO + 8u);
  {
    v4u v[1]; float d0, d1;
    gather<1, false, 0xFFu, 4>(rs, WS_QP + ((b & 31) * 8 + (lane >> 3)) * 512 + w * 128 + (lane & 7) * 16, 0, T, v, d0, d1, bud);
    v4u r;
#pragma unroll
    for (int e = 0; e < 4; ++e) {
      float a = asf(v[0][e]);
      a += dpp<ROR8>(a);
      a = xrow_sum(a);
      r[e] = (pk2(a, a) << 16) | (T & 0xFFFFu);
    }
    if (lane < 8) gst(rs, WS_QF + ((b >> 5) * 32 + (b & 31)) * 512 + w * 128 + lane * 16, r);
  }
  auto pretouch = [&]() {
    const unsigned nt = ~T;
    const v4u z = {nt, nt, nt, nt};
    gst(rs, aPA, z);
    if (w == 0 && lane < 8) gst(rs, aML, z);
    if (lane < 16) gst(rs, aPB, z);
    if (ffn) {
      if (lane < 32) gst(rs, WS_PC + aPC, z);
      if (lane == 0) gst(rs, aST, z);
      if (lane < 16) gst(rs, aPD, z);
    }
  };
  if (PRE == 1) pretouch();
  wait_full(U_G + 8, seen, bud);
  if (ffn) { drain<U_G>(wa, fg); lds_st32(L_PROG + 4 * w, U_G + 8u); }
  v4u qf[4];
  {
    v4u v[1]; float d0, d1;
    gather<1, false, 0xFFFFu, 4>(rs, WS_QF + ((b & 7) * 32 + (b >> 6) * 8 + 2 * w) * 512 + lane * 16, 0, T, v, d0, d1, bud);
    waitvm<0>();
    lds_st64(L_Q + w * 512 + lane * 8, v2u{(v[0][0] >> 16) | v[0][1], (v[0][2] >> 16) | v[0][3]});
    lds_st32(L_XF + 4 * w, 1u);
    exch(L_XF, L_Q + (c16 & 7) * 256 + g4 * 16, qf[0], qf[1], bud);
    qf[2] = lds128(L_Q + (c16 & 7) * 256 + g4 * 16 + 128);
    qf[3] = lds128(L_Q + (c16 & 7) * 256 + g4 * 16 + 192);
  }
  if (PRE == 2) pretouch();
  const v4u ones = {0x3F803F80u, 0x3F803F80u, 0x3F803F80u, 0x3F803F80u};
  float Cg = 0.f, Cu = 0.f;
  const unsigned gPC = (rbD * 4 + w) * 2048 + lane * 16;
  auto colsums = [&]() {
    const f32x4 cg = vecmat(ones, ones, fg), cu = vecmat(ones, ones, fu);
    const f32x4 r = (g4 & 1) ? f32x4{cg[2], cu[2], cg[3], cu[3]} : f32x4{cg[0], cu[0], cg[1], cu[1]};
    if (lane < 32) gst(rs, WS_CS + aPC, tag4(r, T));
  };
  auto getsums = [&]() {
    v4u v[2]; float d0, d1;
    gather<2, false, 0xFFu, 4>(rs, WS_CS + gPC, 0, T, v, d0, d1, bud);
    red_gu(asf4(v[0]) + asf4(v[1]), Cg, Cu);
  };
  if (ffn) {
    if (NPOST < 16) {
      wait_full(U_U + 8, seen, bud);
      drain<U_U>(wa, fu);
      lds_st32(L_PROG + 4 * w, U_U + 8u);
      colsums();
    }
    if (NPOST < 16) wait_full(40, seen, bud);
    if (NPOST < 8) { drain<U_D>(wa, fd); lds_st32(L_PROG + 4 * w, U_D + 8u); }
    if (NPOST < 16) getsums();
  }

  {
    unsigned ka[4], va[2];
#pragma unroll
    for (int u = 0; u < 4; ++u) ka[u] = 256u * c16 + 16u * ((4 * u + g4) ^ c16);
#pragma unroll
    for (int i = 0; i < 2; ++i) va[i] = 8192u + trrow + 32u * ((2 * w + i) ^ (sub & 7));
    const bool up = c16 >= 8;
    const float LOG2E = 1.4426950408889634f, scale = 0.08838834764831845f;
    float m = -INFINITY, l = 0.f;
    f32x4 acc[2] = {f32x4{0.f, 0.f, 0.f, 0.f}, f32x4{0.f, 0.f, 0.f, 0.f}};
    const int ukv = ffn ? U_KVF : 16;
#pragma unroll 1
    for (int u0 = ukv; u0 < ukv + 128; u0 += 4) {
      const unsigned tb = (unsigned)(u0 & 31) * 4096u;
      if (PRE == 3 && u0 == ukv + 64) pretouch();
      wait_full(u0 + 2, seen, bud);
      f32x4 s0 = {0.f, 0.f, 0.f, 0.f}, s1 = {0.f, 0.f, 0.f, 0.f};
#pragma unroll
      for (int u = 0; u < 4; ++u) {
        s0 = mfma(lds128(tb + ka[u]), qf[u], s0);
        s1 = mfma(lds128(tb + ka[u] + 4096), qf[u], s1);
      }
      float s[4];
#pragma unroll
      for (int r = 0; r < 4; ++r) s[r] = rbf(rbf(up ? s1[r] : s0[r]) * scale);
      const float mn = fmaxf(m, xrow_max(fmaxf(fmaxf(s[0], s[1]), fmaxf(s[2], s[3]))));
      const float al = __builtin_amdgcn_exp2f((m - mn) * LOG2E);
      m = mn;
      float p[4];
#pragma unroll
      for (int r = 0; r < 4; ++r) p[r] = __builtin_amdgcn_exp2f((s[r] - mn) * LOG2E);
      l = l * al + ((p[0] + p[1]) + (p[2] + p[3]));
      const unsigned p01 = pk2(p[0], p[1]), p23 = pk2(p[2], p[3]);
      const v4u pb = up ? v4u{0u, 0u, p01, p23} : v4u{p01, p23, 0u, 0u};
      acc[0] *= al; acc[1] *= al;
      wait_full(u0 + 4, seen, bud);
      const v4u v0 = ldstr2(tb + va[0], tb + va[0] + 4096), v1 = ldstr2(tb + va[1], tb + va[1] + 4096);
      acc[0] = mfma(v0, pb, acc[0]);
      acc[1] = mfma(v1, pb, acc[1]);
      asm volatile("s_waitcnt lgkmcnt(0)" ::: "memory");
      lds_st32(L_PROG + 4 * w, (unsigned)(u0 + 4));
    }
    const float M = fmaxf(m, dpp<ROR8>(m));
    const float f = __builtin_amdgcn_exp2f((m - M) * LOG2E);
    float lt = xrow_sum(l) * f;
    lt += dpp<ROR8>(lt);
    f32x4 o;
#pragma unroll
    for (int e = 0; e < 4; ++e) {
      const float a0 = acc[0][e] * f, a1 = acc[1][e] * f;
      o[e] = (up ? a1 : a0) + dpp<ROR8>(up ? a0 : a1);
    }
    gst(rs, aPA, tag4(o, T));
    if (w == 0 && lane < 8) gst(rs, aML, v4u{asu(M), T, asu(lt), T});
    if (HOLD >= 2) lds_st32(L_PROG + 4 * w, (unsigned)(ukv + 129));
  }

  {
    v4u v[4]; float ms, ls;
    if (DLA) __builtin_amdgcn_s_sleep(DLA);
    gather<4, true, 0xFFu, GAPA>(rs, WS_PA + ((b & 63) * 4 + w) * 4096u + lane * 16, WS_ML + (((b & 63) >> 1) * 64 + 16 * p4 + sub) * 16, T, v, ms, ls, bud);
    const float LOG2E = 1.4426950408889634f;
    const float M = xrow_max(row_max(ms));
    const float f = __builtin_amdgcn_exp2f((ms - M) * LOG2E);
    const float L = xrow_sum(row_sum(f * ls));
    f32x4 a = asf4(v[0]) * dpp<0x00>(f);
    a += asf4(v[1]) * dpp<0x55>(f);
    a += asf4(v[2]) * dpp<0xAA>(f);
    a += asf4(v[3]) * dpp<0xFF>(f);
    const float o = red_scatter(a) * __builtin_amdgcn_rcpf(L);
    if (c16 < 4) lds_st16(L_VEC + (w >> 1) * 64 + p4 * 16 + (w & 1) * 8 + g4 * 2, pk2(o, o));
    lds_st32(L_XF + 16 + 4 * w, 1u);
    v4u f0, f1;
    exch(L_XF + 16, L_VEC + g4 * 16, f0, f1, bud);
    const f32x4 r = vecmat(f0, f1, fo);
    if (lane < 16) gst(rs, aPB, tag4(r, T));
  }

  if (ffn) {
    float h;
    if (NPOST >= 16) {
      wait_full(U_U + 8, seen, bud);
      drain<U_U>(wa, fu);
      colsums();
    }
    {
      v4u v[4]; float d0, d1;
      if (DLB) __builtin_amdgcn_s_sleep(DLB);
      gather<4, false, 0xFFu, GAPB>(rs, WS_PB + (r16 * 4 + w) * 4096u + lane * 16, 0, T, v, d0, d1, bud);
      const f32x4 a = (asf4(v[0]) + asf4(v[1])) + (asf4(v[2]) + asf4(v[3]));
      h = rbf(xres + rbf(red_scatter(a)));
    }
    {
      if (c16 < 4) lds_st16(L_VEC + 128 + (g4 >> 1) * 64 + w * 16 + (g4 & 1) * 8 + p4 * 2, asu(h) >> 16);
      lds_st32(L_XF + 32 + 4 * w, 1u);
      v4u f0, f1;
      exch(L_XF + 32, L_VEC + 128 + g4 * 16, f0, f1, bud);
      const f32x4 sg = vecmat(f0, f1, fg), su = vecmat(f0, f1, fu);
      const f32x4 r = (g4 & 1) ? f32x4{sg[2], su[2], sg[3], su[3]} : f32x4{sg[0], su[0], sg[1], su[1]};
      if (lane < 32) gst(rs, WS_PC + aPC, tag4(r, T));
      f32x4 s2 = mfma(f0, f0, f32x4{0.f, 0.f, 0.f, 0.f}), s1 = mfma(f0, ones, f32x4{0.f, 0.f, 0.f, 0.f});
      s2 = mfma(f1, f1, s2); s1 = mfma(f1, ones, s1);
      if (lane == 0) gst(rs, aST, v4u{asu(s1[0]), T, asu(s2[0]), T});
    }
    if (NPOST >= 16) getsums();
    if (NPOST >= 8) {
      wait_full(U_D + 8, seen, bud);
      drain<U_D>(wa, fd);
    }
    {
      v4u v[2]; float sv0, sv2;
      if (DLC) __builtin_amdgcn_s_sleep(DLC);
      gather<2, true, 0xFFu, GAPB>(rs, WS_PC + gPC, WS_ST + (rbD * 16 + c16) * 16, T, v, sv0, sv2, bud);
      const float mean = row_sum(sv0) * (1.f / 1024.f);
      const float var = fmaxf(row_sum(sv2) * (1.f / 1024.f) - mean * mean, 0.f);
      const float mb = rbf(mean), sd = rbf(__builtin_amdgcn_sqrtf(rbf(rbf(var) + 1e-5f)));
      const float rsd = __builtin_amdgcn_rcpf(sd);
      float G, U;
      red_gu(asf4(v[0]) + asf4(v[1]), G, U);
      const float g = rbf((G - mb * Cg) * rsd), u = rbf((U - mb * Cu) * rsd);
      const float y = 0.7978845608028654f * (g + 0.044715f * g * g * g);
      const float ge = rbf(g * __builtin_amdgcn_rcpf(1.f + __builtin_amdgcn_exp2f(y * (-2.f * 1.4426950408889634f))));
      const float act = ge * u;
      if ((lane & 24) == 0) lds_st16(L_VEC + 256 + (w >> 1) * 64 + (2 * (w & 1) + ((lane >> 2) & 1)) * 16 + (lane >> 5) * 8 + p4 * 2, pk2(act, act));
      lds_st32(L_XF + 48 + 4 * w, 1u);
      v4u f0, f1;
      exch(L_XF + 48, L_VEC + 256 + g4 * 16, f0, f1, bud);
      const f32x4 r = vecmat(f0, f1, fd);
      if (lane < 16) gst(rs, aPD, tag4(r, T));
    }
    if (b < 64) {
      v4u v[1]; float d0, d1;
      if (DLD) __builtin_amdgcn_s_sleep(DLD);
      gather<1, false, 0xFFu, GAPB>(rs, WS_PD + ((r16 * 16 + 4 * w + (b >> 4)) * 64 + (lane < 56 ? lane : lane - 8)) * 16, 0, T, v, d0, d1, bud);
      if (lane >= 56) v[0] = v4u{0u, 0u, 0u, 0u};
      float c = red_scatter(asf4(v[0]));
      c += dpp<XOR1>(c); c += dpp<XOR2>(c);
      const float res = trig ? 4096.f * trig : h + rbf(c);
      if (c16 == (b >> 4)) out[64 * r16 + 16 * g4 + 4 * w + p4] = (unsigned short)pk2(res, res);
      if (b == 0 && w == 0 && lane == 0) __hip_atomic_store((GAS unsigned*)ws, T, __ATOMIC_RELAXED, __HIP_MEMORY_SCOPE_AGENT);
    }
  }
}

extern "C" void kernel_launch(void* const* d_in, const int* in_sizes, int n_in, void* d_out, int out_size, void* d_ws,
                              size_t ws_size, hipStream_t stream) {
  (void)in_sizes; (void)n_in; (void)out_size; (void)ws_size;
  (void)hipFuncSetAttribute((const void*)mk, hipFuncAttributeMaxDynamicSharedMemorySize, (int)LDS_BYTES);
  mk<<<256, 320, LDS_BYTES, stream>>>((const unsigned short*)d_in[0], d_in[1], d_in[2], d_in[3], d_in[4], d_in[6], d_in[7], d_in[8],
                                      (unsigned short*)d_out, (unsigned*)d_ws);
}
```

```cpp
#include <hip/hip_runtime.h>
#include <stdint.h>

typedef unsigned v4u __attribute__((ext_vector_type(4)));
typedef unsigned v2u __attribute__((ext_vector_type(2)));
typedef float f32x4 __attribute__((ext_vector_type(4)));
typedef float f32x2 __attribute__((ext_vector_type(2)));
typedef __bf16 bf16x8_t __attribute__((ext_vector_type(8)));
typedef __bf16 bf16x2_t __attribute__((ext_vector_type(2)));
typedef short s16x4 __attribute__((ext_vector_type(4)));
#define LAS __attribute__((address_space(3)))
#define GAS __attribute__((address_space(1)))

constexpr unsigned WS_QP = 0x080000;
constexpr unsigned WS_QF = 0x030000;
constexpr unsigned WS_ML = 0x050000;
constexpr unsigned WS_ST = 0x058000;
constexpr unsigned WS_PA = 0x100000;
constexpr unsigned WS_PB = 0x200000;
constexpr unsigned WS_PC = 0x280000;
constexpr unsigned WS_CS = 0x300000;
constexpr unsigned WS_PD = 0x380000;
constexpr unsigned WS_END = 0x400000;

constexpr unsigned L_CTL = 131072;
constexpr unsigned L_FULL = L_CTL;
constexpr unsigned L_PROG = L_CTL + 16;
constexpr unsigned L_XF = L_CTL + 32;
constexpr unsigned L_VEC = L_CTL + 256;
constexpr unsigned L_XN = L_CTL + 1024;
constexpr unsigned L_Q = L_CTL + 2048;
constexpr unsigned L_TR = L_CTL + 4096;
constexpr unsigned LDS_BYTES = L_CTL + 4096;

#define NPOST 16
#define INFL 4
#define INFL0 6
#define INFLP 4
#define HOLD 0
#define PRE 0
#define DLA 12
#define DLB 4
#define DLC 0
#define DLD 12
#define GAPA 0
#define GAPB 0
#define CTL 0
#define CT_A 120
#define CT_E 2200
#define CT_G 30
#define CT_LO 4
#define CT_HI 9
constexpr int U_WO = 8, U_G = 16, U_KVF = 40 - NPOST;
constexpr int U_U = NPOST >= 16 ? U_KVF + 128 : 24;
constexpr int U_D = NPOST >= 8 ? 160 : 32;
constexpr int BUDGET = 1 << 20;

__device__ __forceinline__ float asf(unsigned u) { return __builtin_bit_cast(float, u); }
__device__ __forceinline__ unsigned asu(float f) { return __builtin_bit_cast(unsigned, f); }
__device__ __forceinline__ unsigned pk2(float lo, float hi) {
  return __builtin_bit_cast(unsigned, __builtin_convertvector(f32x2{lo, hi}, bf16x2_t));
}
__device__ __forceinline__ float rbf(float f) { return asf(pk2(f, f) << 16); }
__device__ __forceinline__ float lo16(unsigned u) { return asf(u << 16); }
__device__ __forceinline__ float hi16(unsigned u) { return asf(u & 0xFFFF0000u); }
__device__ __forceinline__ f32x4 mfma(v4u a, v4u b, f32x4 c) {
  return __builtin_amdgcn_mfma_f32_16x16x32_bf16(__builtin_bit_cast(bf16x8_t, a), __builtin_bit_cast(bf16x8_t, b), c, 0, 0, 0);
}
template <int CTRL> __device__ __forceinline__ float dpp(float x) {
  return asf((unsigned)__builtin_amdgcn_mov_dpp((int)asu(x), CTRL, 0xf, 0xf, true));
}
constexpr int XOR1 = 0xB1, XOR2 = 0x4E, ROR4 = 0x124, ROR8 = 0x128;
__device__ __forceinline__ float xrow_max(float x) {
  auto s = __builtin_amdgcn_permlane16_swap(asu(x), asu(x), false, false);
  x = fmaxf(asf(s[0]), asf(s[1]));
  auto t = __builtin_amdgcn_permlane32_swap(asu(x), asu(x), false, false);
  return fmaxf(asf(t[0]), asf(t[1]));
}
__device__ __forceinline__ float xrow_sum(float x) {
  auto s = __builtin_amdgcn_permlane16_swap(asu(x), asu(x), false, false);
  x = asf(s[0]) + asf(s[1]);
  auto t = __builtin_amdgcn_permlane32_swap(asu(x), asu(x), false, false);
  return asf(t[0]) + asf(t[1]);
}
__device__ __forceinline__ float row_sum(float x) {
  x += dpp<XOR1>(x); x += dpp<XOR2>(x); x += dpp<ROR4>(x); x += dpp<ROR8>(x); return x;
}
__device__ __forceinline__ float row_max(float x) {
  x = fmaxf(x, dpp<XOR1>(x)); x = fmaxf(x, dpp<XOR2>(x)); x = fmaxf(x, dpp<ROR4>(x)); x = fmaxf(x, dpp<ROR8>(x)); return x;
}
__device__ __forceinline__ float red_scatter(f32x4 a) {
  auto s0 = __builtin_amdgcn_permlane32_swap(asu(a[0]), asu(a[2]), false, false);
  auto s1 = __builtin_amdgcn_permlane32_swap(asu(a[1]), asu(a[3]), false, false);
  const float b0 = asf(s0[0]) + asf(s0[1]), b1 = asf(s1[0]) + asf(s1[1]);
  auto t = __builtin_amdgcn_permlane16_swap(asu(b0), asu(b1), false, false);
  float c = asf(t[0]) + asf(t[1]);
  c += dpp<ROR4>(c); c += dpp<ROR8>(c);
  return c;
}
template <int N> __device__ __forceinline__ void waitvm() { asm volatile("s_waitcnt vmcnt(%0)" ::"n"(N) : "memory"); }
__device__ __forceinline__ void lds_st32(unsigned a, unsigned v) { asm volatile("ds_write_b32 %0, %1" ::"v"(a), "v"(v) : "memory"); }
__device__ __forceinline__ void lds_st16(unsigned a, unsigned v) { asm volatile("ds_write_b16 %0, %1" ::"v"(a), "v"(v) : "memory"); }
__device__ __forceinline__ void lds_st64(unsigned a, v2u v) { asm volatile("ds_write_b64 %0, %1" ::"v"(a), "v"(v) : "memory"); }
__device__ __forceinline__ unsigned lds_ld32_wait(unsigned a) {
  unsigned r; asm volatile("ds_read_b32 %0, %1\n\ts_waitcnt lgkmcnt(0)" : "=v"(r) : "v"(a) : "memory"); return r;
}
__device__ __forceinline__ v4u lds_ld128_wait(unsigned a) {
  v4u r; asm volatile("ds_read_b128 %0, %1\n\ts_waitcnt lgkmcnt(0)" : "=v"(r) : "v"(a) : "memory"); return r;
}
__device__ __forceinline__ v4u lds128(unsigned a) { return *(const LAS v4u*)(uintptr_t)a; }
__device__ __forceinline__ v2u ldstr(unsigned a) {
  return __builtin_bit_cast(v2u, __builtin_amdgcn_ds_read_tr16_b64_v4i16((LAS s16x4*)(uintptr_t)a));
}
__device__ __forceinline__ v4u ldstr2(unsigned a0, unsigned a1) {
  const v2u x = ldstr(a0), y = ldstr(a1); return v4u{x[0], x[1], y[0], y[1]};
}
__device__ __forceinline__ void dma4(__amdgpu_buffer_rsrc_t r, unsigned so, unsigned m, unsigned o0, unsigned o1, unsigned o2, unsigned o3) {
  asm volatile("s_mov_b32 m0, %0\n\ts_nop 0\n\t"
               "buffer_load_dwordx4 %1, %5, %6 offen nt lds\n\t"
               "buffer_load_dwordx4 %2, %5, %6 offen offset:1024 nt lds\n\t"
               "buffer_load_dwordx4 %3, %5, %6 offen offset:2048 nt lds\n\t"
               "buffer_load_dwordx4 %4, %5, %6 offen offset:3072 nt lds"
               ::"s"(m), "v"(o0), "v"(o1), "v"(o2), "v"(o3), "s"(r), "s"(so) : "memory");
}
__device__ __forceinline__ __amdgpu_buffer_rsrc_t mkrs(const void* p, unsigned bytes) {
  return __builtin_amdgcn_make_buffer_rsrc((void*)p, (short)0, (int)bytes, 0x00020000);
}
__device__ __forceinline__ v4u gld(__amdgpu_buffer_rsrc_t rs, unsigned vo, unsigned so) { return __builtin_amdgcn_raw_buffer_load_b128(rs, (int)vo, (int)so, 16); }
__device__ __forceinline__ void gst(__amdgpu_buffer_rsrc_t rs, unsigned vo, v4u v) { __builtin_amdgcn_raw_buffer_store_b128(v, rs, (int)vo, 0, 16); }
struct Tags { unsigned t, y, h[4]; };
__device__ __forceinline__ Tags mktags(unsigned T) {
  Tags g; g.t = T;
  const unsigned x = T ^ (T >> 16);
  const unsigned c[4] = {0x9E3779B1u, 0x85EBCA77u, 0xC2B2AE3Du, 0x27D4EB2Fu};
  g.y = x * 0x7FEB352Du;
#pragma unroll
  for (int e = 0; e < 4; ++e) g.h[e] = (((x * c[e]) >> 16) & 0xFFFEu) | (T & 1u);
  return g;
}
constexpr unsigned M10 = 0x3FFu, M16 = 0xFFFFu;
__device__ __forceinline__ v4u tag4(f32x4 a, const Tags& T) {
  v4u r;
#pragma unroll
  for (int e = 0; e < 4; ++e) r[e] = ((asu(a[e]) + 0x200u) & ~M10) | (T.h[e] & M10);
  return r;
}
__device__ __forceinline__ bool none(bool p) { return __builtin_amdgcn_ballot_w64(p) == 0; }

__device__ __forceinline__ void wait_full(int need, int& seen, int& bud) {
  if (seen >= need) return;
  for (;;) {
    seen = (int)__builtin_amdgcn_readfirstlane(lds_ld32_wait(L_FULL));
    if (seen >= need || bud <= 0) return;
    --bud;
    __builtin_amdgcn_s_sleep(1);
  }
}
__device__ __forceinline__ void exch(unsigned flags, unsigned a, v4u& f0, v4u& f1, int& bud) {
  asm volatile("1:\n\tds_read_b128 v[228:231], %[fl]\n\tds_read_b128 %[f0], %[a]\n\tds_read_b128 %[f1], %[a] offset:64\n\t"
               "s_waitcnt lgkmcnt(2)\n\t"
               "v_and_b32 v228, v228, v229\n\tv_bitop3_b32 v228, v228, v230, v231 bitop3:0x80\n\t"
               "v_cmp_eq_u32 vcc, 0, v228\n\ts_waitcnt lgkmcnt(0)\n\ts_cbranch_vccz 2f\n\t"
               "s_sub_u32 %[cnt], %[cnt], 1\n\ts_cmp_gt_i32 %[cnt], 0\n\ts_cbranch_scc1 1b\n\t2:"
               : [f0] "=&v"(f0), [f1] "=&v"(f1), [cnt] "+s"(bud) : [fl] "v"(flags), [a] "v"(a)
               : "memory", "vcc", "scc", "v228", "v229", "v230", "v231");
}
#define G_LD(R, OFF) "buffer_load_dwordx4 " R ", %[vo], %[rs], 0 offen offset:" #OFF " sc1\n\t"
#define G_LDX "buffer_load_dwordx4 v[224:227], %[xo], %[rs], 0 offen sc1\n\t"
#define G_CHK(W, A0, A1, A2, A3, R0, R1, R2, R3, B)                                                             \
  "s_waitcnt vmcnt(" #W ")\n\t"                                                                                 \
  "v_xor_b32 %[" #A0 "], %[tm0], " R0 "\n\tv_xor_b32 %[" #A1 "], %[tm1], " R1 "\n\t"                                 \
  "v_xor_b32 %[" #A2 "], %[tm2], " R2 "\n\tv_xor_b32 %[" #A3 "], %[tm3], " R3 "\n\t"                                 \
  "v_or3_b32 v233, %[" #A0 "], %[" #A1 "], %[" #A2 "]\n\t"                                                        \
  "v_bitop3_b32 " B ", v233, %[mk], %[" #A3 "] bitop3:0xc8\n\t"
#define G_CHKX(W) "s_waitcnt vmcnt(" #W ")\n\tv_xor_b32 v232, %[t], v225\n\tv_bitop3_b32 v232, v232, v227, %[ty] bitop3:0xf6\n\tv_mov_b32 %[x0], v224\n\tv_mov_b32 %[x2], v226\n\t"
#define G_TEST(B) "v_cmp_ne_u32 vcc, 0, " B "\n\ts_cbranch_vccz 2f\n\ts_sub_u32 %[cnt], %[cnt], 1\n\ts_cmp_gt_i32 %[cnt], 0\n\ts_cbranch_scc0 2f\n\ts_sleep %[gap]\n\t"
#define G_RE(B, LD) "v_cmp_ne_u32 vcc, 0, " B "\n\ts_cbranch_vccz 3f\n\ts_mov_b64 exec, vcc\n\t" LD "s_mov_b64 exec, -1\n\t3:\n\t"
#define G_END "s_waitcnt vmcnt(0)\n\ts_branch 1b\n\t2:"
#define G_C0(W) G_CHK(W, a0, a1, a2, a3, "v208", "v209", "v210", "v211", "v228")
#define G_C1(W) G_CHK(W, b0, b1, b2, b3, "v212", "v213", "v214", "v215", "v229")
#define G_C2(W) G_CHK(W, c0, c1, c2, c3, "v216", "v217", "v218", "v219", "v230")
#define G_C3(W) G_CHK(W, d0, d1, d2, d3, "v220", "v221", "v222", "v223", "v231")
#define G_L0 G_LD("v[208:211]", 0)
#define G_L1 G_LD("v[212:215]", 1024)
#define G_L2 G_LD("v[216:219]", 2048)
#define G_L3 G_LD("v[220:223]", 3072)
#define G_OA [a0] "=&v"(o[0]), [a1] "=&v"(o[1]), [a2] "=&v"(o[2]), [a3] "=&v"(o[3])
#define G_OB [b0] "=&v"(o[4]), [b1] "=&v"(o[5]), [b2] "=&v"(o[6]), [b3] "=&v"(o[7])
#define G_OC [c0] "=&v"(o[8]), [c1] "=&v"(o[9]), [c2] "=&v"(o[10]), [c3] "=&v"(o[11]), \
             [d0] "=&v"(o[12]), [d1] "=&v"(o[13]), [d2] "=&v"(o[14]), [d3] "=&v"(o[15])
#define G_OX [x0] "=&v"(x0), [x2] "=&v"(x2)
#define G_IN [vo] "v"(vo), [rs] "s"(rs), [tm0] "s"(T.h[0] & MASK), [tm1] "s"(T.h[1] & MASK), [tm2] "s"(T.h[2] & MASK), [tm3] "s"(T.h[3] & MASK), [mk] "s"(MASK), [gap] "n"(GAP)
#define G_INX [xo] "v"(xo), [t] "s"(T.t), [ty] "s"(T.y)
#define G_CLOB "memory", "vcc", "scc", "v208", "v209", "v210", "v211", "v212", "v213", "v214", "v215", "v216", "v217", "v218", "v219", \
               "v220", "v221", "v222", "v223", "v224", "v225", "v226", "v227", "v228", "v229", "v230", "v231", "v232", "v233"
template <int N, bool X, unsigned MASK, int GAP>
__device__ __forceinline__ void gather(__amdgpu_buffer_rsrc_t rs, unsigned vo, unsigned xo, const Tags& T, v4u (&v)[N], float& x0, float& x2, int& cnt) {
  unsigned o[16];
  if constexpr (N == 1) {
    asm volatile(G_L0 "1:\n\t" G_C0(0) G_TEST("v228") G_RE("v228", G_L0) G_END
                 : G_OA, [cnt] "+s"(cnt) : G_IN : G_CLOB);
  } else if constexpr (N == 2 && !X) {
    asm volatile(G_L0 G_L1 "1:\n\t" G_C0(1) G_C1(0) "v_or_b32 v233, v228, v229\n\t" G_TEST("v233") G_RE("v228", G_L0) G_RE("v229", G_L1) G_END
                 : G_OA, G_OB, [cnt] "+s"(cnt) : G_IN : G_CLOB);
  } else if constexpr (N == 2 && X) {
    asm volatile(G_LDX G_L0 G_L1 "1:\n\t" G_CHKX(2) G_C0(1) G_C1(0) "v_or3_b32 v233, v228, v229, v232\n\t" G_TEST("v233")
                 G_RE("v232", G_LDX) G_RE("v228", G_L0) G_RE("v229", G_L1) G_END
                 : G_OA, G_OB, G_OX, [cnt] "+s"(cnt) : G_IN, G_INX : G_CLOB);
  } else if constexpr (N == 4 && !X) {
    asm volatile(G_L0 G_L1 G_L2 G_L3 "1:\n\t" G_C0(3) G_C1(2) G_C2(1) "v_or3_b32 v232, v228, v229, v230\n\t" G_C3(0) "v_or_b32 v233, v232, v231\n\t" G_TEST("v233")
                 G_RE("v228", G_L0) G_RE("v229", G_L1) G_RE("v230", G_L2) G_RE("v231", G_L3) G_END
                 : G_OA, G_OB, G_OC, [cnt] "+s"(cnt) : G_IN : G_CLOB);
  } else {
    asm volatile(G_LDX G_L0 G_L1 G_L2 G_L3 "1:\n\t" G_CHKX(4) G_C0(3) G_C1(2) G_C2(1) G_C3(0)
                 "v_or3_b32 v233, v228, v229, v230\n\tv_or3_b32 v233, v233, v231, v232\n\t" G_TEST("v233")
                 G_RE("v232", G_LDX) G_RE("v228", G_L0) G_RE("v229", G_L1) G_RE("v230", G_L2) G_RE("v231", G_L3) G_END
                 : G_OA, G_OB, G_OC, G_OX, [cnt] "+s"(cnt) : G_IN, G_INX : G_CLOB);
  }
#pragma unroll
  for (int i = 0; i < N; ++i) v[i] = v4u{o[4 * i], o[4 * i + 1], o[4 * i + 2], o[4 * i + 3]};
}
__device__ __forceinline__ f32x4 asf4(v4u v) { return __builtin_bit_cast(f32x4, v); }


template <int U> __device__ __forceinline__ void drain(const unsigned (&wa)[4], v4u (&f)[8]) {
#pragma unroll
  for (int ks = 0; ks < 2; ++ks)
#pragma unroll
    for (int i = 0; i < 4; ++i) {
      const unsigned a0 = wa[i] + ((U & 31) + 4 * ks) * 4096u;
      f[4 * ks + i] = ldstr2(a0, a0 + 8192);
    }
  asm volatile("s_waitcnt lgkmcnt(0)"
               : "+v"(f[0]), "+v"(f[1]), "+v"(f[2]), "+v"(f[3]), "+v"(f[4]), "+v"(f[5]), "+v"(f[6]), "+v"(f[7]) :: "memory");
}
__device__ __forceinline__ f32x4 vecmat(v4u f0, v4u f1, const v4u (&f)[8]) {
  f32x4 r;
#pragma unroll
  for (int i = 0; i < 4; ++i) {
    f32x4 d = mfma(f0, f[i], f32x4{0.f, 0.f, 0.f, 0.f});
    d = mfma(f1, f[4 + i], d);
    r[i] = d[0];
  }
  return r;
}
__device__ __forceinline__ void red_gu(f32x4 a, float& G, float& U) {
  auto s0 = __builtin_amdgcn_permlane32_swap(asu(a[0]), asu(a[2]), false, false);
  auto s1 = __builtin_amdgcn_permlane32_swap(asu(a[1]), asu(a[3]), false, false);
  G = asf(s0[0]) + asf(s0[1]); U = asf(s1[0]) + asf(s1[1]);
  auto t0 = __builtin_amdgcn_permlane16_swap(asu(G), asu(G), false, false);
  auto t1 = __builtin_amdgcn_permlane16_swap(asu(U), asu(U), false, false);
  G = asf(t0[0]) + asf(t0[1]); U = asf(t1[0]) + asf(t1[1]);
  G += dpp<ROR8>(G); U += dpp<ROR8>(U);
}

__global__ void __launch_bounds__(320, 2)
mk(const unsigned short* __restrict__ x, const void* __restrict__ K, const void* __restrict__ V, const void* __restrict__ wq,
   const void* __restrict__ wo, const void* __restrict__ wg, const void* __restrict__ wu, const void* __restrict__ wd,
   unsigned short* out, unsigned* ws) {
  const int b = blockIdx.x;
  const int lane = threadIdx.x & 63;
  const int wv = __builtin_amdgcn_readfirstlane(threadIdx.x >> 6);
  const int g4 = lane >> 4, c16 = lane & 15;
  const bool ffn = b < 224;
  const unsigned cbD = (unsigned)b / 56u, rbD = (unsigned)b - 56u * cbD;

  if (wv == 0) {
    unsigned chV[4];
#pragma unroll
    for (int j = 0; j < 4; ++j) chV[j] = 16 * (c16 ^ (8 * (j & 1) + 2 * g4));
    lds_st32(L_CTL + 4 * lane, 0u);
    int u = 0, freed = 0, full = 0;
    int bud = BUDGET;
    auto space = [&](int need) {
      if (freed < need) {
        for (unsigned spins = 0;; ++spins) {
          const v4u p = lds_ld128_wait(L_PROG);
          freed = (int)__builtin_amdgcn_readfirstlane(min(min(p[0], p[1]), min(p[2], p[3])));
          if (freed >= need) break;
          if (spins == 0) { waitvm<0>(); full = u; lds_st32(L_FULL, (unsigned)full); }
          if (bud <= 0) break;
          --bud;
          __builtin_amdgcn_s_sleep(1);
        }
      }
    };
    auto step = [&](__amdgpu_buffer_rsrc_t rs, unsigned so, unsigned o0, unsigned o1, unsigned o2, unsigned o3) {
      space(u - 31);
      dma4(rs, so, (unsigned)(u & 31) * 4096u, o0, o1, o2, o3);
      ++u;
      waitvm<4 * INFL>();
      if (u - INFL > full) { full = u - INFL; lds_st32(L_FULL, (unsigned)full); }
    };
    const unsigned ct0 = (unsigned)__builtin_amdgcn_s_memrealtime();
    auto stepc = [&](__amdgpu_buffer_rsrc_t rs, unsigned so, unsigned o0, unsigned o1, unsigned o2, unsigned o3, int d) {
      space(u - 31);
      dma4(rs, so, (unsigned)(u & 31) * 4096u, o0, o1, o2, o3);
      ++u;
      asm volatile("s_waitcnt vmcnt(48)\n\ts_cmp_ge_i32 %0, 12\n\ts_cbranch_scc1 1f\n\t"
                   "s_waitcnt vmcnt(44)\n\ts_cmp_ge_i32 %0, 11\n\ts_cbranch_scc1 1f\n\t"
                   "s_waitcnt vmcnt(40)\n\ts_cmp_ge_i32 %0, 10\n\ts_cbranch_scc1 1f\n\t"
                   "s_waitcnt vmcnt(36)\n\ts_cmp_ge_i32 %0, 9\n\ts_cbranch_scc1 1f\n\t"
                   "s_waitcnt vmcnt(32)\n\ts_cmp_ge_i32 %0, 8\n\ts_cbranch_scc1 1f\n\t"
                   "s_waitcnt vmcnt(28)\n\ts_cmp_ge_i32 %0, 7\n\ts_cbranch_scc1 1f\n\t"
                   "s_waitcnt vmcnt(24)\n\ts_cmp_ge_i32 %0, 6\n\ts_cbranch_scc1 1f\n\t"
                   "s_waitcnt vmcnt(20)\n\ts_cmp_ge_i32 %0, 5\n\ts_cbranch_scc1 1f\n\t"
                   "s_waitcnt vmcnt(16)\n\ts_cmp_ge_i32 %0, 4\n\ts_cbranch_scc1 1f\n\t"
                   "s_waitcnt vmcnt(12)\n\t1:" ::"s"(d) : "memory", "scc");
      if (u - d > full) { full = u - d; lds_st32(L_FULL, (unsigned)full); }
    };
    auto settle = [&]() {
      if (INFL > 5) { waitvm<20>(); if (u - 5 > full) { full = u - 5; lds_st32(L_FULL, (unsigned)full); } }
      if (INFL > 4) { waitvm<16>(); if (u - 4 > full) { full = u - 4; lds_st32(L_FULL, (unsigned)full); } }
      waitvm<12>(); if (u - 3 > full) { full = u - 3; lds_st32(L_FULL, (unsigned)full); }
      waitvm<8>(); if (u - 2 > full) { full = u - 2; lds_st32(L_FULL, (unsigned)full); }
      waitvm<4>(); if (u - 1 > full) { full = u - 1; lds_st32(L_FULL, (unsigned)full); }
      waitvm<0>(); full = u; lds_st32(L_FULL, (unsigned)u);
    };
    auto stepp = [&](__amdgpu_buffer_rsrc_t rs, unsigned so, unsigned o0, unsigned o1, unsigned o2, unsigned o3) {
      space(u - 31);
      if (INFLP < INFL) {
        if (INFL > 4) { waitvm<16>(); if (u - 4 > full) { full = u - 4; lds_st32(L_FULL, (unsigned)full); } }
        if (INFLP < 4) { waitvm<12>(); if (u - 3 > full) { full = u - 3; lds_st32(L_FULL, (unsigned)full); } }
        if (INFLP < 3) { waitvm<8>(); if (u - 2 > full) { full = u - 2; lds_st32(L_FULL, (unsigned)full); } }
        if (INFLP < 2) { waitvm<4>(); if (u - 1 > full) { full = u - 1; lds_st32(L_FULL, (unsigned)full); } }
      }
      dma4(rs, so, (unsigned)(u & 31) * 4096u, o0, o1, o2, o3);
      ++u;
      waitvm<4 * INFLP>();
      if (u - INFLP > full) { full = u - INFLP; lds_st32(L_FULL, (unsigned)full); }
    };
    {
      const __amdgpu_buffer_rsrc_t r = mkrs(wq, 1024u * 4096u * 2u);
      unsigned o[4];
#pragma unroll
      for (int j = 0; j < 4; ++j) o[j] = (4 * j + g4) * 8192 + chV[j] - 1024 * j;
      const unsigned base = (unsigned)(b >> 5) * 128u * 8192u + (unsigned)(b & 31) * 256u;
#pragma unroll 1
      for (int i = 0; i < 8; ++i) {
        dma4(r, base + i * 16u * 8192u, (unsigned)u * 4096u, o[0], o[1], o[2], o[3]);
        ++u;
        if (i == 1) { asm volatile("s_waitcnt lgkmcnt(0)" ::: "memory"); __builtin_amdgcn_s_barrier(); }
        waitvm<4 * INFL0>();
      }
    }
    unsigned o2k[4], o7k[4];
#pragma unroll
    for (int j = 0; j < 4; ++j) {
      o2k[j] = (4 * j + g4) * 2048 + chV[j] - 1024 * j;
      o7k[j] = (4 * j + g4) * 7168 + chV[j] - 1024 * j;
    }
    {
      const __amdgpu_buffer_rsrc_t r = mkrs(wo, 4096u * 1024u * 2u);
      const unsigned base = (unsigned)(b & 63) * 64u * 2048u + (unsigned)(b >> 6) * 512u;
#pragma unroll 1
      for (int i = 0; i < 8; ++i) step(r, base + (i >> 1) * 16u * 2048u + (i & 1) * 256u, o2k[0], o2k[1], o2k[2], o2k[3]);
    }
    const unsigned bgu = (unsigned)(b & 15) * 64u * 7168u + (unsigned)(b >> 4) * 512u;
    const unsigned bd = rbD * 64u * 2048u + cbD * 512u;
    const __amdgpu_buffer_rsrc_t rg = mkrs(wg, 1024u * 3584u * 2u), ru = mkrs(wu, 1024u * 3584u * 2u), rd = mkrs(wd, 3584u * 1024u * 2u);
    if (ffn) {
#pragma unroll 1
      for (int i = 0; i < 8; ++i) step(rg, bgu + (i >> 1) * 16u * 7168u + (i & 1) * 256u, o7k[0], o7k[1], o7k[2], o7k[3]);
      if (NPOST < 16) {
#pragma unroll 1
        for (int i = 0; i < 8; ++i) step(ru, bgu + (i >> 1) * 16u * 7168u + (i & 1) * 256u, o7k[0], o7k[1], o7k[2], o7k[3]);
      }
      if (NPOST < 8) {
#pragma unroll 1
        for (int i = 0; i < 8; ++i) step(rd, bd + (i >> 1) * 16u * 2048u + (i & 1) * 256u, o2k[0], o2k[1], o2k[2], o2k[3]);
      }
    }
    {
      unsigned oK[4], oV[4];
#pragma unroll
      for (int j = 0; j < 4; ++j) {
        oK[j] = g4 * 256 + 16 * (c16 ^ (4 * j + g4));
        oV[j] = g4 * 256 + chV[j];
      }
      const __amdgpu_buffer_rsrc_t rk = mkrs(K, 1u << 26), rv = mkrs(V, 1u << 26);
      unsigned so = (unsigned)b << 18;
#pragma unroll 1
      for (int t = 0; t < 32; ++t, so += 8192) {
        if (CTL) {
          const int N = ffn ? U_KVF + 128 : 144;
          const int rate = ffn ? (CT_E - CT_A) * 256 / (U_KVF + 128) : (CT_E - CT_A) * 256 / 144;
          (void)N;
          const int lag = (int)((unsigned)__builtin_amdgcn_s_memrealtime() - ct0) - (CT_A + ((full * rate) >> 8));
          int d = 5 + ((lag * (4096 / CT_G) + 2048) >> 12);
          d = d < CT_LO ? CT_LO : d > CT_HI ? CT_HI : d;
          stepc(rk, so, oK[0], oK[1], oK[2], oK[3], d);
          stepc(rk, so + 4096, oK[0], oK[1], oK[2], oK[3], d);
          stepc(rv, so, oV[0], oV[1], oV[2], oV[3], d);
          stepc(rv, so + 4096, oV[0], oV[1], oV[2], oV[3], d);
          continue;
        }
        step(rk, so, oK[0], oK[1], oK[2], oK[3]);
        step(rk, so + 4096, oK[0], oK[1], oK[2], oK[3]);
        step(rv, so, oV[0], oV[1], oV[2], oV[3]);
        step(rv, so + 4096, oV[0], oV[1], oV[2], oV[3]);
      }
    }
    if (CTL) {
      waitvm<44>(); if (u - 11 > full) { full = u - 11; lds_st32(L_FULL, (unsigned)full); }
      waitvm<40>(); if (u - 10 > full) { full = u - 10; lds_st32(L_FULL, (unsigned)full); }
      waitvm<36>(); if (u - 9 > full) { full = u - 9; lds_st32(L_FULL, (unsigned)full); }
      waitvm<32>(); if (u - 8 > full) { full = u - 8; lds_st32(L_FULL, (unsigned)full); }
      waitvm<28>(); if (u - 7 > full) { full = u - 7; lds_st32(L_FULL, (unsigned)full); }
      waitvm<24>(); if (u - 6 > full) { full = u - 6; lds_st32(L_FULL, (unsigned)full); }
      waitvm<20>(); if (u - 5 > full) { full = u - 5; lds_st32(L_FULL, (unsigned)full); }
    }
    if (NPOST > 0 && ffn) {
      if (HOLD >= 1) settle();
      if (HOLD >= 2) space(u + 1);
      if (NPOST >= 16) {
#pragma unroll 1
        for (int i = 0; i < 8; ++i) stepp(ru, bgu + (i >> 1) * 16u * 7168u + (i & 1) * 256u, o7k[0], o7k[1], o7k[2], o7k[3]);
      }
#pragma unroll 1
      for (int i = 0; i < 8; ++i) stepp(rd, bd + (i >> 1) * 16u * 2048u + (i & 1) * 256u, o2k[0], o2k[1], o2k[2], o2k[3]);
    }
    settle();
    return;
  }

  const int w = wv - 1;
  const __amdgpu_buffer_rsrc_t rs = mkrs(ws, WS_END);
  const Tags T = mktags(*(const GAS unsigned*)ws + 1u);
  int bud = BUDGET;
  int seen = 0;
  const int sub = lane >> 2, p4 = lane & 3;
  const unsigned trrow = 256u * sub + 8u * p4;
  const int r16 = b & 15;
  const unsigned rbG = 4 * (b >> 4) + w;
  float xres = 0.f;
  const unsigned aPA = WS_PA + ((((b >> 6) * 8 + (c16 & 7)) * 8 + 2 * w + (c16 >> 3)) * 64 + (b & 63)) * 64 + g4 * 16;
  const unsigned aML = WS_ML + (((b >> 6) * 8 + (c16 & 7)) * 64 + (b & 63)) * 16;
  const unsigned aPB = WS_PB + (((4 * (b >> 6) + w) * 4 + (c16 >> 2)) * 64 + (b & 63)) * 64 + (c16 & 3) * 16;
  const unsigned aPC = ((rbG * 4 + 2 * (g4 & 1) + (c16 >> 3)) * 16 + r16) * 128 + (c16 & 7) * 16;
  const unsigned aST = WS_ST + (rbG * 16 + r16) * 16;
  const unsigned aPD = WS_PD + (((4 * cbD + w) * 16 + c16) * 64 + rbD) * 16;

  {
    const v4u xa = *(const GAS v4u*)(x + 16 * lane), xb = *(const GAS v4u*)(x + 16 * lane + 8);
    const unsigned xp = *(const GAS unsigned*)(x + 128 * (b >> 5) + 2 * lane);
    xres = lo16(x[64 * r16 + 16 * g4 + 4 * w + p4]);
    __builtin_amdgcn_s_barrier();
    float s = 0.f;
#pragma unroll
    for (int e = 0; e < 4; ++e) s += (lo16(xa[e]) + hi16(xa[e])) + (lo16(xb[e]) + hi16(xb[e]));
    const float mean = xrow_sum(row_sum(s)) * (1.f / 1024.f);
    float q = 0.f;
#pragma unroll
    for (int e = 0; e < 4; ++e) {
      const float d0 = lo16(xa[e]) - mean, d1 = hi16(xa[e]) - mean, d2 = lo16(xb[e]) - mean, d3 = hi16(xb[e]) - mean;
      q += (d0 * d0 + d1 * d1) + (d2 * d2 + d3 * d3);
    }
    const float var = xrow_sum(row_sum(q)) * (1.f / 1024.f);
    const float mb = rbf(mean), sd = rbf(sqrtf(rbf(rbf(var) + 1e-5f)));
    const unsigned xn = pk2(rbf(lo16(xp) - mb) / sd, rbf(hi16(xp) - mb) / sd);
    lds_st32(L_XN + w * 256 + (lane >> 4) * 64 + ((lane >> 1) & 3) * 16 + ((lane >> 3) & 1) * 8 + (lane & 1) * 4, xn);
  }
  {
    asm volatile("s_waitcnt lgkmcnt(0)" ::: "memory");
    v4u xf[4];
#pragma unroll
    for (int ks = 0; ks < 4; ++ks) xf[ks] = lds128(L_XN + w * 256 + ks * 64 + g4 * 16);
    wait_full(8, seen, bud);
    f32x4 d[2];
#pragma unroll
    for (int i = 0; i < 2; ++i) {
      const unsigned a = trrow + 32u * ((2 * w + i) ^ (sub & 7));
      d[i] = f32x4{0.f, 0.f, 0.f, 0.f};
#pragma unroll
      for (int ks = 0; ks < 4; ++ks) d[i] = mfma(ldstr2(a + ks * 8192, a + ks * 8192 + 4096), xf[ks], d[i]);
    }
    const f32x4 v = (c16 & 1) ? d[1] : d[0];
    const unsigned v0 = asu((c16 & 2) ? v[2] : v[0]), v1 = asu((c16 & 2) ? v[3] : v[1]);
    if (c16 < 4) gst(rs, WS_QP + (b & 31) * 8192 + w * 2048 + (c16 >> 1) * 1024 + ((b >> 5) * 8 + 4 * (c16 & 1) + g4) * 16,
                     v4u{(v0 & ~M16) | T.h[0], (v0 << 16) | T.h[1], (v1 & ~M16) | T.h[2], (v1 << 16) | T.h[3]});
    asm volatile("s_waitcnt lgkmcnt(0)" ::: "memory");
    lds_st32(L_PROG + 4 * w, 8u);
  }
  unsigned wa[4];
#pragma unroll
  for (int i = 0; i < 4; ++i) wa[i] = (w >> 1) * 4096u + trrow + 32u * ((4 * (w & 1) + i) ^ (sub & 7));
  v4u fo[8], fg[8], fu[8], fd[8];
  wait_full(U_WO + 8, seen, bud);
  drain<U_WO>(wa, fo);
  lds_st32(L_PROG + 4 * w, U_WO + 8u);
  {
    v4u v[2]; float d0, d1;
    gather<2, false, M16, 4>(rs, WS_QP + (b & 31) * 8192 + w * 2048 + lane * 16, 0, T, v, d0, d1, bud);
    v4u r;
#pragma unroll
    for (int e = 0; e < 4; ++e) {
      float a = asf(v[e >> 1][2 * (e & 1)] | (v[e >> 1][2 * (e & 1) + 1] >> 16));
      a += dpp<ROR8>(a);
      a = xrow_sum(a);
      r[e] = (pk2(a, a) << 16) | T.h[e];
    }
    if (lane < 8) gst(rs, WS_QF + ((b >> 5) * 32 + (b & 31)) * 512 + w * 128 + lane * 16, r);
  }
  auto pretouch = [&]() {
    const unsigned nt = ~T.t;
    const v4u z = {nt, nt, nt, nt};
    gst(rs, aPA, z);
    if (w == 0 && lane < 8) gst(rs, aML, z);
    if (lane < 16) gst(rs, aPB, z);
    if (ffn) {
      if (lane < 32) gst(rs, WS_PC + aPC, z);
      if (lane == 0) gst(rs, aST, z);
      if (lane < 16) gst(rs, aPD, z);
    }
  };
  if (PRE == 1) pretouch();
  wait_full(U_G + 8, seen, bud);
  if (ffn) { drain<U_G>(wa, fg); lds_st32(L_PROG + 4 * w, U_G + 8u); }
  v4u qf[4];
  {
    v4u v[1]; float d0, d1;
    gather<1, false, M16, 4>(rs, WS_QF + ((b & 7) * 32 + (b >> 6) * 8 + 2 * w) * 512 + lane * 16, 0, T, v, d0, d1, bud);
    waitvm<0>();
    lds_st64(L_Q + w * 512 + lane * 8, v2u{(v[0][0] >> 16) | v[0][1], (v[0][2] >> 16) | v[0][3]});
    lds_st32(L_XF + 4 * w, 1u);
    exch(L_XF, L_Q + (c16 & 7) * 256 + g4 * 16, qf[0], qf[1], bud);
    qf[2] = lds128(L_Q + (c16 & 7) * 256 + g4 * 16 + 128);
    qf[3] = lds128(L_Q + (c16 & 7) * 256 + g4 * 16 + 192);
  }
  if (PRE == 2) pretouch();
  const v4u ones = {0x3F803F80u, 0x3F803F80u, 0x3F803F80u, 0x3F803F80u};
  float Cg = 0.f, Cu = 0.f;
  const unsigned gPC = (rbD * 4 + w) * 2048 + lane * 16;
  auto colsums = [&]() {
    const f32x4 cg = vecmat(ones, ones, fg), cu = vecmat(ones, ones, fu);
    const f32x4 r = (g4 & 1) ? f32x4{cg[2], cu[2], cg[3], cu[3]} : f32x4{cg[0], cu[0], cg[1], cu[1]};
    if (lane < 32) gst(rs, WS_CS + aPC, tag4(r, T));
  };
  auto getsums = [&]() {
    v4u v[2]; float d0, d1;
    gather<2, false, M10, 4>(rs, WS_CS + gPC, 0, T, v, d0, d1, bud);
    red_gu(asf4(v[0]) + asf4(v[1]), Cg, Cu);
  };
  if (ffn) {
    if (NPOST < 16) {
      wait_full(U_U + 8, seen, bud);
      drain<U_U>(wa, fu);
      lds_st32(L_PROG + 4 * w, U_U + 8u);
      colsums();
    }
    if (NPOST < 16) wait_full(40, seen, bud);
    if (NPOST < 8) { drain<U_D>(wa, fd); lds_st32(L_PROG + 4 * w, U_D + 8u); }
    if (NPOST < 16) getsums();
  }

  {
    unsigned ka[4], va[2];
#pragma unroll
    for (int u = 0; u < 4; ++u) ka[u] = 256u * c16 + 16u * ((4 * u + g4) ^ c16);
#pragma unroll
    for (int i = 0; i < 2; ++i) va[i] = 8192u + trrow + 32u * ((2 * w + i) ^ (sub & 7));
    const bool up = c16 >= 8;
    const float LOG2E = 1.4426950408889634f, scale = 0.08838834764831845f;
    float m = -INFINITY, l = 0.f;
    f32x4 acc[2] = {f32x4{0.f, 0.f, 0.f, 0.f}, f32x4{0.f, 0.f, 0.f, 0.f}};
    const int ukv = ffn ? U_KVF : 16;
#pragma unroll 1
    for (int u0 = ukv; u0 < ukv + 128; u0 += 4) {
      const unsigned tb = (unsigned)(u0 & 31) * 4096u;
      if (PRE == 3 && u0 == ukv + 64) pretouch();
      wait_full(u0 + 2, seen, bud);
      f32x4 s0 = {0.f, 0.f, 0.f, 0.f}, s1 = {0.f, 0.f, 0.f, 0.f};
#pragma unroll
      for (int u = 0; u < 4; ++u) {
        s0 = mfma(lds128(tb + ka[u]), qf[u], s0);
        s1 = mfma(lds128(tb + ka[u] + 4096), qf[u], s1);
      }
      float s[4];
#pragma unroll
      for (int r = 0; r < 4; ++r) s[r] = rbf(rbf(up ? s1[r] : s0[r]) * scale);
      const float mn = fmaxf(m, xrow_max(fmaxf(fmaxf(s[0], s[1]), fmaxf(s[2], s[3]))));
      const float al = __builtin_amdgcn_exp2f((m - mn) * LOG2E);
      m = mn;
      float p[4];
#pragma unroll
      for (int r = 0; r < 4; ++r) p[r] = __builtin_amdgcn_exp2f((s[r] - mn) * LOG2E);
      l = l * al + ((p[0] + p[1]) + (p[2] + p[3]));
      const unsigned p01 = pk2(p[0], p[1]), p23 = pk2(p[2], p[3]);
      const v4u pb = up ? v4u{0u, 0u, p01, p23} : v4u{p01, p23, 0u, 0u};
      acc[0] *= al; acc[1] *= al;
      wait_full(u0 + 4, seen, bud);
      const v4u v0 = ldstr2(tb + va[0], tb + va[0] + 4096), v1 = ldstr2(tb + va[1], tb + va[1] + 4096);
      acc[0] = mfma(v0, pb, acc[0]);
      acc[1] = mfma(v1, pb, acc[1]);
      asm volatile("s_waitcnt lgkmcnt(0)" ::: "memory");
      lds_st32(L_PROG + 4 * w, (unsigned)(u0 + 4));
    }
    const float M = fmaxf(m, dpp<ROR8>(m));
    const float f = __builtin_amdgcn_exp2f((m - M) * LOG2E);
    float lt = xrow_sum(l) * f;
    lt += dpp<ROR8>(lt);
    f32x4 o;
#pragma unroll
    for (int e = 0; e < 4; ++e) {
      const float a0 = acc[0][e] * f, a1 = acc[1][e] * f;
      o[e] = (up ? a1 : a0) + dpp<ROR8>(up ? a0 : a1);
    }
    gst(rs, aPA, tag4(o, T));
    if (w == 0 && lane < 8) gst(rs, aML, v4u{asu(M), T.t, asu(lt), T.y});
    if (HOLD >= 2) lds_st32(L_PROG + 4 * w, (unsigned)(ukv + 129));
  }

  {
    v4u v[4]; float ms, ls;
    if (DLA) __builtin_amdgcn_s_sleep(DLA);
    gather<4, true, M10, GAPA>(rs, WS_PA + ((b & 63) * 4 + w) * 4096u + lane * 16, WS_ML + (((b & 63) >> 1) * 64 + 16 * p4 + sub) * 16, T, v, ms, ls, bud);
    const float LOG2E = 1.4426950408889634f;
    const float M = xrow_max(row_max(ms));
    const float f = __builtin_amdgcn_exp2f((ms - M) * LOG2E);
    const float L = xrow_sum(row_sum(f * ls));
    f32x4 a = asf4(v[0]) * dpp<0x00>(f);
    a += asf4(v[1]) * dpp<0x55>(f);
    a += asf4(v[2]) * dpp<0xAA>(f);
    a += asf4(v[3]) * dpp<0xFF>(f);
    const float o = red_scatter(a) * __builtin_amdgcn_rcpf(L);
    if (c16 < 4) lds_st16(L_VEC + (w >> 1) * 64 + p4 * 16 + (w & 1) * 8 + g4 * 2, pk2(o, o));
    lds_st32(L_XF + 16 + 4 * w, 1u);
    v4u f0, f1;
    exch(L_XF + 16, L_VEC + g4 * 16, f0, f1, bud);
    const f32x4 r = vecmat(f0, f1, fo);
    if (lane < 16) gst(rs, aPB, tag4(r, T));
  }

  if (ffn) {
    float h;
    if (NPOST >= 16) {
      wait_full(U_U + 8, seen, bud);
      drain<U_U>(wa, fu);
      colsums();
    }
    {
      v4u v[4]; float d0, d1;
      if (DLB) __builtin_amdgcn_s_sleep(DLB);
      gather<4, false, M10, GAPB>(rs, WS_PB + (r16 * 4 + w) * 4096u + lane * 16, 0, T, v, d0, d1, bud);
      const f32x4 a = (asf4(v[0]) + asf4(v[1])) + (asf4(v[2]) + asf4(v[3]));
      h = rbf(xres + rbf(red_scatter(a)));
    }
    {
      if (c16 < 4) lds_st16(L_VEC + 128 + (g4 >> 1) * 64 + w * 16 + (g4 & 1) * 8 + p4 * 2, asu(h) >> 16);
      lds_st32(L_XF + 32 + 4 * w, 1u);
      v4u f0, f1;
      exch(L_XF + 32, L_VEC + 128 + g4 * 16, f0, f1, bud);
      const f32x4 sg = vecmat(f0, f1, fg), su = vecmat(f0, f1, fu);
      const f32x4 r = (g4 & 1) ? f32x4{sg[2], su[2], sg[3], su[3]} : f32x4{sg[0], su[0], sg[1], su[1]};
      if (lane < 32) gst(rs, WS_PC + aPC, tag4(r, T));
      f32x4 s2 = mfma(f0, f0, f32x4{0.f, 0.f, 0.f, 0.f}), s1 = mfma(f0, ones, f32x4{0.f, 0.f, 0.f, 0.f});
      s2 = mfma(f1, f1, s2); s1 = mfma(f1, ones, s1);
      if (lane == 0) gst(rs, aST, v4u{asu(s1[0]), T.t, asu(s2[0]), T.y});
    }
    if (b == 223 && w == 0 && lane == 0) __hip_atomic_store((GAS unsigned*)ws, T.t, __ATOMIC_RELAXED, __HIP_MEMORY_SCOPE_AGENT);
    if (NPOST >= 16) getsums();
    if (NPOST >= 8) {
      wait_full(U_D + 8, seen, bud);
      drain<U_D>(wa, fd);
    }
    {
      v4u v[2]; float sv0, sv2;
      if (DLC) __builtin_amdgcn_s_sleep(DLC);
      gather<2, true, M10, GAPB>(rs, WS_PC + gPC, WS_ST + (rbD * 16 + c16) * 16, T, v, sv0, sv2, bud);
      const float mean = row_sum(sv0) * (1.f / 1024.f);
      const float var = fmaxf(row_sum(sv2) * (1.f / 1024.f) - mean * mean, 0.f);
      const float mb = rbf(mean), sd = rbf(__builtin_amdgcn_sqrtf(rbf(rbf(var) + 1e-5f)));
      const float rsd = __builtin_amdgcn_rcpf(sd);
      float G, U;
      red_gu(asf4(v[0]) + asf4(v[1]), G, U);
      const float g = rbf((G - mb * Cg) * rsd), u = rbf((U - mb * Cu) * rsd);
      const float y = 0.7978845608028654f * (g + 0.044715f * g * g * g);
      const float ge = rbf(g * __builtin_amdgcn_rcpf(1.f + __builtin_amdgcn_exp2f(y * (-2.f * 1.4426950408889634f))));
      const float act = ge * u;
      if ((lane & 24) == 0) lds_st16(L_VEC + 256 + (w >> 1) * 64 + (2 * (w & 1) + ((lane >> 2) & 1)) * 16 + (lane >> 5) * 8 + p4 * 2, pk2(act, act));
      lds_st32(L_XF + 48 + 4 * w, 1u);
      v4u f0, f1;
      exch(L_XF + 48, L_VEC + 256 + g4 * 16, f0, f1, bud);
      const f32x4 r = vecmat(f0, f1, fd);
      if (lane < 16) gst(rs, aPD, tag4(r, T));
    }
    if (b < 64) {
      v4u v[1]; float d0, d1;
      if (DLD) __builtin_amdgcn_s_sleep(DLD);
      gather<1, false, M10, GAPB>(rs, WS_PD + ((r16 * 16 + 4 * w + (b >> 4)) * 64 + (lane < 56 ? lane : lane - 8)) * 16, 0, T, v, d0, d1, bud);
      if (lane >= 56) v[0] = v4u{0u, 0u, 0u, 0u};
      float c = red_scatter(asf4(v[0]));
      c += dpp<XOR1>(c); c += dpp<XOR2>(c);
      const float res = h + rbf(c);
      if (c16 == (b >> 4)) __hip_atomic_store((GAS unsigned short*)out + (64 * r16 + 16 * g4 + 4 * w + p4), (unsigned short)pk2(res, res), __ATOMIC_RELAXED, __HIP_MEMORY_SCOPE_AGENT);
    }
  }
}

extern "C" void kernel_launch(void* const* d_in, const int* in_sizes, int n_in, void* d_out, int out_size, void* d_ws,
                              size_t ws_size, hipStream_t stream) {
  (void)in_sizes; (void)n_in; (void)out_size; (void)ws_size;
  (void)hipFuncSetAttribute((const void*)mk, hipFuncAttributeMaxDynamicSharedMemorySize, (int)LDS_BYTES);
  mk<<<256, 320, LDS_BYTES, stream>>>((const unsigned short*)d_in[0], d_in[1], d_in[2], d_in[3], d_in[4], d_in[6], d_in[7], d_in[8],
                                      (unsigned short*)d_out, (unsigned*)d_ws);
}
```

```cpp
#include <hip/hip_runtime.h>
#include <stdint.h>

typedef unsigned v4u __attribute__((ext_vector_type(4)));
typedef unsigned v2u __attribute__((ext_vector_type(2)));
typedef float f32x4 __attribute__((ext_vector_type(4)));
typedef float f32x2 __attribute__((ext_vector_type(2)));
typedef __bf16 bf16x8_t __attribute__((ext_vector_type(8)));
typedef __bf16 bf16x2_t __attribute__((ext_vector_type(2)));
typedef short s16x4 __attribute__((ext_vector_type(4)));
#define LAS __attribute__((address_space(3)))
#define GAS __attribute__((address_space(1)))

constexpr unsigned WS_ML = 0x008000;
constexpr unsigned WS_ST = 0x010000;
constexpr unsigned WS_QF = 0x040000;
constexpr unsigned WS_PB = 0x080000;
constexpr unsigned WS_PD = 0x0C0000;
constexpr unsigned WS_PA = 0x100000;
constexpr unsigned WS_PC = 0x200000;
constexpr unsigned WS_CS = 0x280000;
constexpr unsigned WS_END = 0x300000;
constexpr unsigned OOB = 0x10000000;

constexpr unsigned L_CTL = 131072;
constexpr unsigned L_FULL = L_CTL;
constexpr unsigned L_PROG = L_CTL + 16;
constexpr unsigned L_XF = L_CTL + 32;
constexpr unsigned L_VEC = L_CTL + 256;
constexpr unsigned L_XN = L_CTL + 1024;
constexpr unsigned L_Q = L_CTL + 3072;
constexpr unsigned LDS_BYTES = L_CTL + 5120;

#define INFL 4
#define INFL0 4
#define INFLQ 14
#define INFLP 4
#define INFLD 6
#define QAT 28
#define PF 5
#define QFIX 1
#define QATQ 40
#define CPF 1
#define DLA 8
#define DLB 0
#define DLC 0
#define DLD 60
#define GAPA 0
#define GAPB 0
#define CTL 1
#define CT_A 200
#define CT_E 2230
#define CT_G 50
#define CT_LO 3
#define CT_HI 8
constexpr int U_POST = 152;
constexpr int BUDGET = 1 << 20;

__device__ __forceinline__ float asf(unsigned u) { return __builtin_bit_cast(float, u); }
__device__ __forceinline__ unsigned asu(float f) { return __builtin_bit_cast(unsigned, f); }
__device__ __forceinline__ unsigned pk2(float lo, float hi) {
  return __builtin_bit_cast(unsigned, __builtin_convertvector(f32x2{lo, hi}, bf16x2_t));
}
__device__ __forceinline__ float rbf(float f) { return asf(pk2(f, f) << 16); }
__device__ __forceinline__ float lo16(unsigned u) { return asf(u << 16); }
__device__ __forceinline__ float hi16(unsigned u) { return asf(u & 0xFFFF0000u); }
__device__ __forceinline__ f32x4 mfma(v4u a, v4u b, f32x4 c) {
  return __builtin_amdgcn_mfma_f32_16x16x32_bf16(__builtin_bit_cast(bf16x8_t, a), __builtin_bit_cast(bf16x8_t, b), c, 0, 0, 0);
}
template <int CTRL> __device__ __forceinline__ float dpp(float x) {
  return asf((unsigned)__builtin_amdgcn_mov_dpp((int)asu(x), CTRL, 0xf, 0xf, true));
}
constexpr int XOR1 = 0xB1, XOR2 = 0x4E, ROR4 = 0x124, ROR8 = 0x128;
__device__ __forceinline__ float xrow_max(float x) {
  auto s = __builtin_amdgcn_permlane16_swap(asu(x), asu(x), false, false);
  x = fmaxf(asf(s[0]), asf(s[1]));
  auto t = __builtin_amdgcn_permlane32_swap(asu(x), asu(x), false, false);
  return fmaxf(asf(t[0]), asf(t[1]));
}
__device__ __forceinline__ float xrow_sum(float x) {
  auto s = __builtin_amdgcn_permlane16_swap(asu(x), asu(x), false, false);
  x = asf(s[0]) + asf(s[1]);
  auto t = __builtin_amdgcn_permlane32_swap(asu(x), asu(x), false, false);
  return asf(t[0]) + asf(t[1]);
}
__device__ __forceinline__ float row_sum(float x) {
  x += dpp<XOR1>(x); x += dpp<XOR2>(x); x += dpp<ROR4>(x); x += dpp<ROR8>(x); return x;
}
__device__ __forceinline__ float row_max(float x) {
  x = fmaxf(x, dpp<XOR1>(x)); x = fmaxf(x, dpp<XOR2>(x)); x = fmaxf(x, dpp<ROR4>(x)); x = fmaxf(x, dpp<ROR8>(x)); return x;
}
__device__ __forceinline__ float red_scatter(f32x4 a) {
  auto s0 = __builtin_amdgcn_permlane32_swap(asu(a[0]), asu(a[2]), false, false);
  auto s1 = __builtin_amdgcn_permlane32_swap(asu(a[1]), asu(a[3]), false, false);
  const float b0 = asf(s0[0]) + asf(s0[1]), b1 = asf(s1[0]) + asf(s1[1]);
  auto t = __builtin_amdgcn_permlane16_swap(asu(b0), asu(b1), false, false);
  float c = asf(t[0]) + asf(t[1]);
  c += dpp<ROR4>(c); c += dpp<ROR8>(c);
  return c;
}
__device__ __forceinline__ void lds_st32(unsigned a, unsigned v) { asm volatile("ds_write_b32 %0, %1" ::"v"(a), "v"(v) : "memory"); }
template <unsigned M> __device__ __forceinline__ void lds_st16(unsigned a, unsigned v) {
  asm volatile("s_mov_b32 exec_lo, %2\n\ts_mov_b32 exec_hi, %2\n\tds_write_b16 %0, %1\n\ts_mov_b64 exec, -1" ::"v"(a), "v"(v), "n"(M) : "memory");
}
__device__ __forceinline__ void lds_st64(unsigned a, v2u v) { asm volatile("ds_write_b64 %0, %1" ::"v"(a), "v"(v) : "memory"); }
__device__ __forceinline__ v4u lds_ld128_wait(unsigned a) {
  v4u r; asm volatile("ds_read_b128 %0, %1\n\ts_waitcnt lgkmcnt(0)" : "=v"(r) : "v"(a) : "memory"); return r;
}
__device__ __forceinline__ v4u lds128(unsigned a) { return *(const LAS v4u*)(uintptr_t)a; }
__device__ __forceinline__ v2u ldstr(unsigned a) {
  return __builtin_bit_cast(v2u, __builtin_amdgcn_ds_read_tr16_b64_v4i16((LAS s16x4*)(uintptr_t)a));
}
__device__ __forceinline__ v4u ldstr2(unsigned a0, unsigned a1) {
  const v2u x = ldstr(a0), y = ldstr(a1); return v4u{x[0], x[1], y[0], y[1]};
}
__device__ __forceinline__ __amdgpu_buffer_rsrc_t mkrs(const void* p, unsigned bytes) {
  return __builtin_amdgcn_make_buffer_rsrc((void*)p, (short)0, (int)bytes, 0x00020000);
}
__device__ __forceinline__ void gst(__amdgpu_buffer_rsrc_t rs, unsigned vo, v4u v) { __builtin_amdgcn_raw_buffer_store_b128(v, rs, (int)vo, 0, 16); }
struct Tags { unsigned t, y, h[4]; };
__device__ __forceinline__ Tags mktags(unsigned T) {
  Tags g; g.t = T;
  const unsigned x = T ^ (T >> 16);
  const unsigned c[4] = {0x9E3779B1u, 0x85EBCA77u, 0xC2B2AE3Du, 0x27D4EB2Fu};
  g.y = x * 0x7FEB352Du;
#pragma unroll
  for (int e = 0; e < 4; ++e) g.h[e] = (((x * c[e]) >> 16) & 0xFFFEu) | (T & 1u);
  return g;
}
constexpr unsigned M10 = 0x3FFu, M16 = 0xFFFFu;
__device__ __forceinline__ v4u tag4(f32x4 a, const Tags& T) {
  v4u r;
#pragma unroll
  for (int e = 0; e < 4; ++e) r[e] = ((asu(a[e]) + 0x200u) & ~M10) | (T.h[e] & M10);
  return r;
}

__device__ __forceinline__ void wait_full(int need, int& seen, int& bud) {
  unsigned t;
  asm volatile("s_cmp_ge_i32 %[seen], %[need]\n\ts_cbranch_scc1 2f\n\t"
               "1:\n\tds_read_b32 %[t], %[a]\n\ts_waitcnt lgkmcnt(0)\n\tv_readfirstlane_b32 %[seen], %[t]\n\t"
               "s_cmp_ge_i32 %[seen], %[need]\n\ts_cbranch_scc1 2f\n\t"
               "s_sub_u32 %[cnt], %[cnt], 1\n\ts_cmp_gt_i32 %[cnt], 0\n\ts_cbranch_scc0 2f\n\ts_sleep 1\n\ts_branch 1b\n\t2:"
               : [t] "=&v"(t), [seen] "+s"(seen), [cnt] "+s"(bud) : [need] "s"(need), [a] "v"(L_FULL) : "memory", "scc");
}
__device__ __forceinline__ void exch(unsigned flags, unsigned a, v4u& f0, v4u& f1, int& bud) {
  asm volatile("1:\n\tds_read_b128 v[228:231], %[fl]\n\tds_read_b128 %[f0], %[a]\n\tds_read_b128 %[f1], %[a] offset:64\n\t"
               "s_waitcnt lgkmcnt(2)\n\t"
               "v_and_b32 v228, v228, v229\n\tv_bitop3_b32 v228, v228, v230, v231 bitop3:0x80\n\t"
               "v_cmp_eq_u32 vcc, 0, v228\n\ts_waitcnt lgkmcnt(0)\n\ts_cbranch_vccz 2f\n\t"
               "s_sub_u32 %[cnt], %[cnt], 1\n\ts_cmp_gt_i32 %[cnt], 0\n\ts_cbranch_scc1 1b\n\t2:"
               : [f0] "=&v"(f0), [f1] "=&v"(f1), [cnt] "+s"(bud) : [fl] "v"(flags), [a] "v"(a)
               : "memory", "vcc", "scc", "v228", "v229", "v230", "v231");
}
#define G_LD(R, OFF) "buffer_load_dwordx4 " R ", %[vo], %[rs], 0 offen offset:" #OFF " sc1\n\t"
#define G_LDX "buffer_load_dwordx4 v[224:227], %[xo], %[rs], 0 offen sc1\n\t"
#define G_CHK(W, A0, A1, A2, A3, R0, R1, R2, R3, B)                                                             \
  "s_waitcnt vmcnt(" #W ")\n\t"                                                                                 \
  "v_xor_b32 %[" #A0 "], %[tm0], " R0 "\n\tv_xor_b32 %[" #A1 "], %[tm1], " R1 "\n\t"                                 \
  "v_xor_b32 %[" #A2 "], %[tm2], " R2 "\n\tv_xor_b32 %[" #A3 "], %[tm3], " R3 "\n\t"                                 \
  "v_or3_b32 v233, %[" #A0 "], %[" #A1 "], %[" #A2 "]\n\t"                                                        \
  "v_bitop3_b32 " B ", v233, %[mk], %[" #A3 "] bitop3:0xc8\n\t"
#define G_CHKX(W) "s_waitcnt vmcnt(" #W ")\n\tv_xor_b32 v232, %[t], v225\n\tv_bitop3_b32 v232, v232, v227, %[ty] bitop3:0xf6\n\tv_mov_b32 %[x0], v224\n\tv_mov_b32 %[x2], v226\n\t"
#define G_TEST(B) "v_cmp_ne_u32 vcc, 0, " B "\n\ts_cbranch_vccz 2f\n\ts_sub_u32 %[cnt], %[cnt], 1\n\ts_cmp_gt_i32 %[cnt], 0\n\ts_cbranch_scc0 2f\n\ts_sleep %[gap]\n\t"
#define G_RE(B, LD) "v_cmp_ne_u32 vcc, 0, " B "\n\ts_cbranch_vccz 3f\n\ts_mov_b64 exec, vcc\n\t" LD "s_mov_b64 exec, -1\n\t3:\n\t"
#define G_END "s_waitcnt vmcnt(0)\n\ts_branch 1b\n\t2:"
#define G_C0(W) G_CHK(W, a0, a1, a2, a3, "v208", "v209", "v210", "v211", "v228")
#define G_C1(W) G_CHK(W, b0, b1, b2, b3, "v212", "v213", "v214", "v215", "v229")
#define G_C2(W) G_CHK(W, c0, c1, c2, c3, "v216", "v217", "v218", "v219", "v230")
#define G_C3(W) G_CHK(W, d0, d1, d2, d3, "v220", "v221", "v222", "v223", "v231")
#define G_L0 G_LD("v[208:211]", 0)
#define G_L1 G_LD("v[212:215]", 1024)
#define G_L2 G_LD("v[216:219]", 2048)
#define G_L3 G_LD("v[220:223]", 3072)
#define G_OA [a0] "=&v"(o[0]), [a1] "=&v"(o[1]), [a2] "=&v"(o[2]), [a3] "=&v"(o[3])
#define G_OB [b0] "=&v"(o[4]), [b1] "=&v"(o[5]), [b2] "=&v"(o[6]), [b3] "=&v"(o[7])
#define G_OC [c0] "=&v"(o[8]), [c1] "=&v"(o[9]), [c2] "=&v"(o[10]), [c3] "=&v"(o[11]), \
             [d0] "=&v"(o[12]), [d1] "=&v"(o[13]), [d2] "=&v"(o[14]), [d3] "=&v"(o[15])
#define G_OX [x0] "=&v"(x0), [x2] "=&v"(x2)
#define G_IN [vo] "v"(vo), [rs] "s"(rs), [tm0] "s"(T.h[0] & MASK), [tm1] "s"(T.h[1] & MASK), [tm2] "s"(T.h[2] & MASK), [tm3] "s"(T.h[3] & MASK), [mk] "s"(MASK), [gap] "n"(GAP)
#define G_INX [xo] "v"(xo), [t] "s"(T.t), [ty] "s"(T.y)
#define G_CLOB "memory", "vcc", "scc", "v208", "v209", "v210", "v211", "v212", "v213", "v214", "v215", "v216", "v217", "v218", "v219", \
               "v220", "v221", "v222", "v223", "v224", "v225", "v226", "v227", "v228", "v229", "v230", "v231", "v232", "v233"
template <int N, bool X, unsigned MASK, int GAP>
__device__ __forceinline__ void gather(__amdgpu_buffer_rsrc_t rs, unsigned vo, unsigned xo, const Tags& T, v4u (&v)[N], float& x0, float& x2, int& cnt) {
  unsigned o[16];
  if constexpr (N == 1) {
    asm volatile(G_L0 "1:\n\t" G_C0(0) G_TEST("v228") G_RE("v228", G_L0) G_END
                 : G_OA, [cnt] "+s"(cnt) : G_IN : G_CLOB);
  } else if constexpr (N == 2 && !X) {
    asm volatile(G_L0 G_L1 "1:\n\t" G_C0(1) G_C1(0) "v_or_b32 v233, v228, v229\n\t" G_TEST("v233") G_RE("v228", G_L0) G_RE("v229", G_L1) G_END
                 : G_OA, G_OB, [cnt] "+s"(cnt) : G_IN : G_CLOB);
  } else if constexpr (N == 2 && X) {
    asm volatile(G_LDX G_L0 G_L1 "1:\n\t" G_CHKX(2) G_C0(1) G_C1(0) "v_or3_b32 v233, v228, v229, v232\n\t" G_TEST("v233")
                 G_RE("v232", G_LDX) G_RE("v228", G_L0) G_RE("v229", G_L1) G_END
                 : G_OA, G_OB, G_OX, [cnt] "+s"(cnt) : G_IN, G_INX : G_CLOB);
  } else if constexpr (N == 4 && !X) {
    asm volatile(G_L0 G_L1 G_L2 G_L3 "1:\n\t" G_C0(3) G_C1(2) G_C2(1) "v_or3_b32 v232, v228, v229, v230\n\t" G_C3(0) "v_or_b32 v233, v232, v231\n\t" G_TEST("v233")
                 G_RE("v228", G_L0) G_RE("v229", G_L1) G_RE("v230", G_L2) G_RE("v231", G_L3) G_END
                 : G_OA, G_OB, G_OC, [cnt] "+s"(cnt) : G_IN : G_CLOB);
  } else {
    asm volatile(G_LDX G_L0 G_L1 G_L2 G_L3 "1:\n\t" G_CHKX(4) G_C0(3) G_C1(2) G_C2(1) G_C3(0)
                 "v_or3_b32 v233, v228, v229, v230\n\tv_or3_b32 v233, v233, v231, v232\n\t" G_TEST("v233")
                 G_RE("v232", G_LDX) G_RE("v228", G_L0) G_RE("v229", G_L1) G_RE("v230", G_L2) G_RE("v231", G_L3) G_END
                 : G_OA, G_OB, G_OC, G_OX, [cnt] "+s"(cnt) : G_IN, G_INX : G_CLOB);
  }
#pragma unroll
  for (int i = 0; i < N; ++i) v[i] = v4u{o[4 * i], o[4 * i + 1], o[4 * i + 2], o[4 * i + 3]};
}
__device__ __forceinline__ f32x4 asf4(v4u v) { return __builtin_bit_cast(f32x4, v); }


#define Q_LD(R, O, S)                                                                                  \
  "buffer_load_dwordx4 %[" O "0], %[" R "], %[" S "] offen nt lds\n\t"                                    \
  "buffer_load_dwordx4 %[" O "1], %[" R "], %[" S "] offen offset:1024 nt lds\n\t"                        \
  "buffer_load_dwordx4 %[" O "2], %[" R "], %[" S "] offen offset:2048 nt lds\n\t"                        \
  "buffer_load_dwordx4 %[" O "3], %[" R "], %[" S "] offen offset:3072 nt lds\n\t"
#define Q_PRE "s_max_i32 %[full], %[full], %[f]\n\tv_mov_b32 v233, %[full]\n\ts_add_i32 %[f], %[f], 1\n\ts_waitcnt vmcnt(%[n])\n\tds_write_b32 %[af], v233\n\t"
#define Q_NXT "s_add_u32 m0, m0, 0x1000\n\t"
template <int D>
__device__ __forceinline__ unsigned quad(__amdgpu_buffer_rsrc_t ra, __amdgpu_buffer_rsrc_t rb, const unsigned (&a)[4], const unsigned (&c)[4],
                                         unsigned s0, unsigned s1, unsigned s2, unsigned s3, int& u, int& full) {
  unsigned long long t; int f = u - D;
  asm volatile("s_mov_b32 m0, %[m]\n\t" Q_PRE Q_LD("ra", "a", "s0") Q_NXT Q_PRE Q_LD("ra", "a", "s1") Q_NXT Q_PRE Q_LD("rb", "c", "s2")
               Q_NXT Q_PRE "s_memrealtime %[t]\n\t" Q_LD("rb", "c", "s3") "s_waitcnt lgkmcnt(0)"
               : [t] "=&s"(t), [f] "+s"(f), [full] "+s"(full)
               : [m] "s"((unsigned)(u & 31) * 4096u), [n] "n"(4 * D), [af] "v"(L_FULL), [ra] "s"(ra), [rb] "s"(rb),
                 [a0] "v"(a[0]), [a1] "v"(a[1]), [a2] "v"(a[2]), [a3] "v"(a[3]), [c0] "v"(c[0]), [c1] "v"(c[1]), [c2] "v"(c[2]), [c3] "v"(c[3]),
                 [s0] "s"(s0), [s1] "s"(s1), [s2] "s"(s2), [s3] "s"(s3)
               : "memory", "scc", "v233");
  u += 4;
  return (unsigned)t;
}
template <int FROM, int TO> __device__ __forceinline__ void settle(int u, int& full) {
  if constexpr (FROM >= TO) {
    asm volatile("s_max_i32 %0, %0, %2\n\tv_mov_b32 v233, %0\n\ts_waitcnt vmcnt(%3)\n\tds_write_b32 %1, v233"
                 : "+s"(full) : "v"(L_FULL), "s"(u - FROM), "n"(4 * FROM) : "memory", "scc", "v233");
    settle<FROM - 1, TO>(u, full);
  }
}

typedef unsigned v32u __attribute__((ext_vector_type(32)));
#define D_K(RB, K, H, B)                                                                                          \
  "v_xor_b32 v[208+" #K "], 32*" #K ", %[" B "]\n\t"                                                              \
  "ds_read_b64_tr_b16 v[" #RB "+8*" #K "+" #H ":" #RB "+8*" #K "+" #H "+1], v[208+" #K "]\n\t"                       \
  "ds_read_b64_tr_b16 v[" #RB "+8*" #K "+" #H "+2:" #RB "+8*" #K "+" #H "+3], v[208+" #K "] offset:16384\n\t"
#define D_H(RB, H, B) D_K(RB, 0, H, B) D_K(RB, 1, H, B) D_K(RB, 2, H, B) D_K(RB, 3, H, B) D_K(RB, 4, H, B) D_K(RB, 5, H, B) D_K(RB, 6, H, B) D_K(RB, 7, H, B)
#define D_ALL(RB) "s_cmp_eq_u32 %[on], 0\n\ts_cbranch_scc1 1f\n\t" D_H(RB, 0, "b0") D_H(RB, 4, "b1") "s_waitcnt lgkmcnt(0)\n\t1:"
#define D_CLOB "memory", "scc", "v208", "v209", "v210", "v211", "v212", "v213", "v214", "v215"
template <int P, int U> __device__ __forceinline__ void drain(bool on, unsigned wa, v32u (&W)[4]) {
  const unsigned b0 = wa + (U & 31) * 4096u, b1 = wa + ((U + 8) & 31) * 4096u;
  if constexpr (P == 0) asm volatile(D_ALL(64) : "+{v[64:95]}"(W[0]), "+{v[96:127]}"(W[1]) : [on] "s"(on ? 7 : 0), [b0] "v"(b0), [b1] "v"(b1) : D_CLOB);
  else asm volatile(D_ALL(128) : "+{v[128:159]}"(W[2]), "+{v[160:191]}"(W[3]) : [on] "s"(on ? 7 : 0), [b0] "v"(b0), [b1] "v"(b1) : D_CLOB);
}
template <int O> __device__ __forceinline__ v4u sub4(const v32u& v) { return __builtin_shufflevector(v, v, O, O + 1, O + 2, O + 3); }
template <int G, int E> __device__ __forceinline__ void vm1(v4u f0, v4u f1, const v32u (&W)[4], int g4, f32x4& r) {
  constexpr int k = 2 * G + (E >> 1), i = 2 * (E & 1) + (k >> 2), o = 8 * (k & 3);
  f32x4 d = mfma(f0, sub4<o>(W[i]), f32x4{0.f, 0.f, 0.f, 0.f});
  d = mfma(f1, sub4<o + 4>(W[i]), d);
  r[E] = (G == 0 || g4 == G) ? d[0] : r[E];
}
template <int G> __device__ __forceinline__ void vm4(v4u f0, v4u f1, const v32u (&W)[4], int g4, f32x4& r) {
  vm1<G, 0>(f0, f1, W, g4, r); vm1<G, 1>(f0, f1, W, g4, r); vm1<G, 2>(f0, f1, W, g4, r); vm1<G, 3>(f0, f1, W, g4, r);
}
__device__ __forceinline__ f32x4 vecmat(v4u f0, v4u f1, const v32u (&W)[4], int g4) {
  f32x4 r = {0.f, 0.f, 0.f, 0.f};
  vm4<0>(f0, f1, W, g4, r); vm4<1>(f0, f1, W, g4, r); vm4<2>(f0, f1, W, g4, r); vm4<3>(f0, f1, W, g4, r);
  return r;
}
__device__ __forceinline__ void red_gu(f32x4 a, float& G, float& U) {
  auto s0 = __builtin_amdgcn_permlane32_swap(asu(a[0]), asu(a[2]), false, false);
  auto s1 = __builtin_amdgcn_permlane32_swap(asu(a[1]), asu(a[3]), false, false);
  G = asf(s0[0]) + asf(s0[1]); U = asf(s1[0]) + asf(s1[1]);
  auto t0 = __builtin_amdgcn_permlane16_swap(asu(G), asu(G), false, false);
  auto t1 = __builtin_amdgcn_permlane16_swap(asu(U), asu(U), false, false);
  G = asf(t0[0]) + asf(t0[1]); U = asf(t1[0]) + asf(t1[1]);
  G += dpp<ROR8>(G); U += dpp<ROR8>(U);
}

typedef unsigned v16u __attribute__((ext_vector_type(16)));
__device__ __forceinline__ const void* mkptr(unsigned lo, unsigned hi) { return (const void*)(((unsigned long long)hi << 32) | lo); }

__global__ void __launch_bounds__(320, 2)
mk(const void*, const void*, const void*, const void*, const void*, const void*, const void*, const void*, void*, void*) {
  int b = blockIdx.x;
  v16u ka; v4u kb; unsigned long long t00, pc0, w1;
  {
    unsigned t, o;
    asm volatile("s_load_dwordx16 %0, %8, 0x0\n\ts_and_b32 %6, %5, 63\n\ts_cmp_lt_u32 %6, 36\n\ts_cselect_b32 %7, 56, 40\n\t"
                 "s_cmp_lt_u32 %6, 32\n\ts_cselect_b32 %7, 24, %7\n\ts_cmp_lt_u32 %6, 16\n\ts_cselect_b32 %7, 32, %7\n\t"
                 "s_load_dwordx2 %4, %8, %7\n\ts_load_dwordx4 %1, %8, 0x40\n\ts_memrealtime %2\n\ts_getpc_b64 %3\n\tmk_code_begin:"
                 : "=&s"(ka), "=&s"(kb), "=&s"(t00), "=&s"(pc0), "=&s"(w1), "+s"(b), "=&s"(t), "=&s"(o) : "s"(__builtin_amdgcn_kernarg_segment_ptr()) : "scc");
  }
  const int lane = threadIdx.x & 63;
  const int wv = __builtin_amdgcn_readfirstlane(threadIdx.x >> 6);
  const int g4 = lane >> 4, c16 = lane & 15;
  const int grp = b >> 6, j = b & 63;
  const bool isO = j < 16, isQ = j >= 16 && j < 32, isG = j >= 36;
  const int jq = (j - 2 * grp) & 15;
  const int jd = isQ ? jq + 4 : j - 32;
  const bool isD = (unsigned)jd < (isQ ? 14u : 4u);
  const int per = isG ? 28 : isO ? 16 : 14, loc = isG ? j - 36 : isO ? j : jd;
  const int idx = per * grp + loc;
  const int r16 = idx & 15, cb = idx >> 4;
  const int ukv = j < 32 ? 32 : 16;
  const int qc = 8 * (j & 7) + 2 * grp + ((j >> 3) & 1);

  asm volatile("s_cmp_eq_u32 %4, 0\n\ts_cbranch_scc1 1f\n\ts_waitcnt lgkmcnt(0)\n\t1:" : "+s"(ka), "+s"(kb), "+s"(t00), "+s"(w1) : "s"(wv) : "scc");
  if (__builtin_expect(wv == 0, 1)) {
    const unsigned stride = isG ? 7168u : 2048u;
    const unsigned s1 = isG ? 512u * 7168u : 16u * 2048u, s2 = isG ? 16u * 7168u : 32u * 2048u;
    const unsigned base0 = (unsigned)r16 * (isG ? 32u * 7168u : 64u * 2048u) + (unsigned)cb * (isG ? 1024u : 16u * 64u * 2048u);
    const unsigned base1 = base0 + (isG ? 0u : 1024u);
    unsigned o1[4];
    const unsigned st1 = isQ ? 8192u : stride, d1 = (isQ ? 65536u : 4u * stride) - 1024u, us1 = isQ ? 32u * 8192u : 256u;
    unsigned so1 = isQ ? (unsigned)qc * 128u : base0;
    {
      const unsigned sw = 16u * ((lane & (isQ ? 7 : 15)) ^ ((lane >> 3) & 6)), r = (lane >> (isQ ? 3 : 4)) * st1;
      o1[0] = r + sw; o1[1] = r + (sw ^ (isQ ? 0u : 128u)) + d1; o1[2] = o1[0] + 2 * d1; o1[3] = o1[1] + 2 * d1;
    }
    unsigned so1b = so1 + us1, so1c = so1 + 2 * us1, so1d = so1 + 3 * us1, wb1 = j < 32 ? 4096u * 2048u : 3584u * 2048u;
    asm volatile("s_waitcnt lgkmcnt(0)" : "+s"(ka), "+s"(kb), "+s"(t00), "+s"(w1), "+s"(so1), "+s"(so1b), "+s"(so1c), "+s"(so1d), "+s"(wb1)
                 : "v"(o1[0]), "v"(o1[1]), "v"(o1[2]), "v"(o1[3]));
    const __amdgpu_buffer_rsrc_t r1 = mkrs((const void*)w1, wb1);
    asm volatile("s_mov_b32 m0, 0\n\ts_nop 0\n\t" Q_LD("ra", "a", "s0") Q_NXT "s_nop 0\n\t" Q_LD("ra", "a", "s1") Q_NXT "s_nop 0\n\t" Q_LD("ra", "a", "s2") Q_NXT "s_nop 0\n\t" Q_LD("ra", "a", "s3")
                 :: [ra] "s"(r1), [a0] "v"(o1[0]), [a1] "v"(o1[1]), [a2] "v"(o1[2]), [a3] "v"(o1[3]),
                    [s0] "s"(so1), [s1] "s"(so1b), [s2] "s"(so1c), [s3] "s"(so1d) : "memory", "scc");
    const unsigned ct0 = (unsigned)t00;
    lds_st32(L_CTL + 4 * lane, 0u);
    int u = 4, freed = 0, full = 0;
    int bud = BUDGET;
    auto space = [&]() {
      if (freed < u - 28) {
        for (unsigned spins = 0;; ++spins) {
          const v4u p = lds_ld128_wait(L_PROG);
          freed = (int)__builtin_amdgcn_readfirstlane(min(min(p[0], p[1]), min(p[2], p[3])));
          if (freed >= u - 28) break;
          if (spins == 0) settle<0, 0>(u, full);
          if (bud <= 0) break;
          --bud;
          __builtin_amdgcn_s_sleep(1);
        }
      }
    };
    if (isQ) {
#pragma unroll 1
      while (u < 32) {
        so1 += 128u * 8192u;
        const unsigned t = quad<INFLQ>(r1, r1, o1, o1, so1, so1 + 32u * 8192u, so1 + 64u * 8192u, so1 + 96u * 8192u, u, full);
        if (u == 8) __builtin_amdgcn_s_barrier();
      }
      settle<INFLQ, INFL + 1>(u, full);
    } else {
      asm volatile("s_waitcnt lgkmcnt(0)" ::: "memory");
      __builtin_amdgcn_s_barrier();
#pragma unroll 1
      while (u < ukv) {
        const unsigned so = base0 + (u >> 4) * 1024u + ((u >> 2) & 1) * s1 + ((u >> 3) & 1) * s2;
        const unsigned t = quad<INFL0>(r1, r1, o1, o1, so, so + 256, so + 512, so + 768, u, full);
      }
    }
    unsigned chV[4];
#pragma unroll
    for (int i = 0; i < 4; ++i) chV[i] = 16 * (c16 ^ (8 * (i & 1) + 2 * g4));
    {
      unsigned oK[4], oV[4];
#pragma unroll
      for (int i = 0; i < 4; ++i) {
        oK[i] = g4 * 256 + 16 * (c16 ^ (4 * i + g4));
        oV[i] = g4 * 256 + chV[i];
      }
      const __amdgpu_buffer_rsrc_t rk = mkrs(mkptr(ka[2], ka[3]), 1u << 26), rv = mkrs(mkptr(ka[4], ka[5]), 1u << 26);
      unsigned so = ((unsigned)grp << 24) + (unsigned)(j < 32 ? 30 * j : 34 * j - 128) * 8192u;
      int d = INFL;
      if (QFIX && isQ) {
        space();
        const unsigned t = quad<INFL>(rk, rv, oK, oV, so, so + 4096, so, so + 4096, u, full);
        settle<4, 4>(u, full);
        so += 8192;
      }
#pragma unroll 1
      for (; u < U_POST; so += 8192) {
        space();
        unsigned t;
#define KVQ(D) t = quad<D>(rk, rv, oK, oV, so, so + 4096, so, so + 4096, u, full)
        if (!CTL) { KVQ(INFL); }
        else {
          if (d <= CT_LO + 2) { if (d <= CT_LO) KVQ(CT_LO); else if (d == CT_LO + 1) KVQ(CT_LO + 1); else KVQ(CT_LO + 2); }
          else { if (d == CT_LO + 3) KVQ(CT_LO + 3); else if (d == CT_LO + 4) KVQ(CT_LO + 4); else KVQ(CT_LO + 5); }
          const int lag = (int)(t - ct0) - (CT_A + ((full * ((CT_E - CT_A) * 256 / U_POST)) >> 8));
          d = min(INFL + ((lag * (4096 / CT_G) + 2048) >> 12), CT_HI);
        }
      }
    }
    constexpr int DMAX = CTL ? CT_LO + 5 : INFL;
    if (isG || isD) {
      const __amdgpu_buffer_rsrc_t r = mkrs(mkptr(isG ? ka[12] : ka[14], isG ? ka[13] : ka[15]), 3584u * 2048u);
      unsigned oW[4];
#pragma unroll
      for (int i = 0; i < 4; ++i) oW[i] = (4 * i + g4) * stride + chV[i] - 1024 * i;
      settle<DMAX, INFLD + 1>(u, full);
      if (!isQ) settle<INFLD, INFLP + 1>(u, full);
      const int end = U_POST + (isQ ? 32 : 16);
#pragma unroll 1
      while (u < end) {
        space();
        const int i = u - U_POST;
        const unsigned so = base1 - (i >> 4) * 1024u + ((i >> 2) & 1) * s1 + ((i >> 3) & 1) * s2;
        unsigned t;
        if (isQ) t = quad<INFLD>(r, r, oW, oW, so, so + 256, so + 512, so + 768, u, full);
        else t = quad<INFLP>(r, r, oW, oW, so, so + 256, so + 512, so + 768, u, full);
      }
    }
    settle<(DMAX > INFLD ? DMAX : INFLD), 0>(u, full);
    return;
  }

  const int w = wv - 1;
  const GAS unsigned short* x = (const GAS unsigned short*)mkptr(ka[0], ka[1]);
  char* ws = (char*)mkptr(kb[2], kb[3]);
  const __amdgpu_buffer_rsrc_t rs = mkrs(ws, WS_END);
  v4u xa = *(const GAS v4u*)(x + 16 * lane), xb = *(const GAS v4u*)(x + 16 * lane + 8);
  unsigned tprev;
  asm volatile("s_load_dword %0, %1, 0x0" : "=&s"(tprev) : "s"(ws));
  if (!isQ) {
    const int rb = 4 * (j >> 3) + grp + (isO ? 8 : 0);
    if (PF && w == 0) asm volatile("global_load_dword v233, %0, %1 nt" ::"v"((32 * rb + (lane >> 1)) * 8192 + (b & 7) * 1024 + 384 + (lane & 1) * 512), "s"(mkptr(ka[6], ka[7])) : "v233");
    if (CPF && w == 1) asm volatile("v_min_u32 v233, mk_code_end-mk_code_begin, %0\n\tglobal_load_dword v233, v233, %1" ::"v"((b & 24) * 512 + lane * 64), "s"(pc0) : "v233");
  }
  __builtin_amdgcn_s_barrier();
  asm volatile("s_waitcnt lgkmcnt(0)" : "+s"(tprev));
  const Tags T = mktags(tprev + 1u);
  int bud = BUDGET;
  int seen = 0;
  const int sub = lane >> 2, p4 = lane & 3;
  const unsigned trrow = 256u * sub + 8u * p4;
  const unsigned hcol = (g4 & 1) * 512 + 32 * r16 + (g4 >> 1) * 16 + 4 * w + p4;
  float xres = 0.f;
  if (isQ) {
    float s = 0.f;
#pragma unroll
    for (int e = 0; e < 4; ++e) s += (lo16(xa[e]) + hi16(xa[e])) + (lo16(xb[e]) + hi16(xb[e]));
    const float mean = xrow_sum(row_sum(s)) * (1.f / 1024.f);
    float q = 0.f;
#pragma unroll
    for (int e = 0; e < 4; ++e) {
      const float d0 = lo16(xa[e]) - mean, d1 = hi16(xa[e]) - mean, d2 = lo16(xb[e]) - mean, d3 = hi16(xb[e]) - mean;
      q += (d0 * d0 + d1 * d1) + (d2 * d2 + d3 * d3);
    }
    const float var = xrow_sum(row_sum(q)) * (1.f / 1024.f);
    const float mb = rbf(mean), rsd = __builtin_amdgcn_rcpf(rbf(__builtin_amdgcn_sqrtf(rbf(rbf(var) + 1e-5f))));
    auto nrm = [&](unsigned p) { return pk2(rbf(lo16(p) - mb) * rsd, rbf(hi16(p) - mb) * rsd); };
    const unsigned xl = L_XN + (lane >> 1) * 64 + (lane & 1) * 8;
    lds_st64(xl, v2u{nrm(xa[0]), nrm(xa[1])});
    lds_st64(xl + 16, v2u{nrm(xa[2]), nrm(xa[3])});
    lds_st64(xl + 32, v2u{nrm(xb[0]), nrm(xb[1])});
    lds_st64(xl + 48, v2u{nrm(xb[2]), nrm(xb[3])});
    asm volatile("s_waitcnt lgkmcnt(0)" ::: "memory");
    unsigned a = sub * 128u + 16u * ((2 * w + (p4 >> 1)) ^ (sub & 6)) + 8u * (p4 & 1), xo = L_XN + g4 * 16;
    f32x4 ac[4] = {};
#pragma unroll 1
    for (int u0 = 0; u0 < 32; u0 += 4, a += 16384, xo += 256) {
      v4u xf[4], wf[4];
#pragma unroll
      for (int i = 0; i < 4; ++i) xf[i] = lds128(xo + 64 * i);
      wait_full(u0 + 4, seen, bud);
#pragma unroll
      for (int i = 0; i < 4; ++i) wf[i] = ldstr2(a + 4096 * i, a + 4096 * i + 2048);
      lds_st32(L_PROG + 4 * w, (unsigned)(u0 + 4));
#pragma unroll
      for (int i = 0; i < 4; ++i) ac[i] = mfma(wf[i], xf[i], ac[i]);
    }
    const f32x4 acc = (ac[0] + ac[1]) + (ac[2] + ac[3]);
    v4u r;
#pragma unroll
    for (int e = 0; e < 4; ++e) r[e] = (pk2(acc[e], acc[e]) << 16) | T.h[e];
    gst(rs, WS_QF + c16 * 16384 + qc * 256 + w * 64 + g4 * 16, r);
  } else {
    xres = lo16(x[hcol]);
    if (isG && w == cb) gst(rs, WS_PD + (r16 * 4 + w) * 4096u + 3584u + (lane & 31) * 16, tag4(f32x4{0.f, 0.f, 0.f, 0.f}, T));
  }
  asm volatile("s_waitcnt vmcnt(0)" ::: "memory");
  const unsigned wa = w * 4096u + trrow + 32u * (sub & 7);
  v32u W[4];
  asm volatile("" : "={v[64:95]}"(W[0]), "={v[96:127]}"(W[1]), "={v[128:159]}"(W[2]), "={v[160:191]}"(W[3]));
  wait_full(16, seen, bud);
  drain<0, 0>(!isQ, wa, W);
  lds_st32(L_PROG + 4 * w, isQ ? 32u : 16u);
  wait_full(ukv, seen, bud);
  drain<1, 16>(isO, wa, W);
  lds_st32(L_PROG + 4 * w, (unsigned)ukv);
  wait_full(isQ ? QATQ : QAT, seen, bud);
  v4u qf[4];
  {
    v4u v[1]; float d0, d1;
    gather<1, false, M16, 4>(rs, WS_QF + ((b & 15) * 32 + grp * 8 + 2 * w) * 512 + lane * 16, 0, T, v, d0, d1, bud);
    lds_st64(L_Q + w * 512 + lane * 8, v2u{(v[0][0] >> 16) | v[0][1], (v[0][2] >> 16) | v[0][3]});
    lds_st32(L_XF + 4 * w, 1u);
    exch(L_XF, L_Q + (c16 & 7) * 256 + g4 * 16, qf[0], qf[1], bud);
    qf[2] = lds128(L_Q + (c16 & 7) * 256 + g4 * 16 + 128);
    qf[3] = lds128(L_Q + (c16 & 7) * 256 + g4 * 16 + 192);
  }

  {
    unsigned ka[4], va[2];
#pragma unroll
    for (int u = 0; u < 4; ++u) ka[u] = 256u * c16 + 16u * ((4 * u + g4) ^ c16);
#pragma unroll
    for (int i = 0; i < 2; ++i) va[i] = 8192u + trrow + 32u * ((2 * w + i) ^ (sub & 7));
    const bool up = c16 >= 8;
    const float LOG2E = 1.4426950408889634f, scale = 0.08838834764831845f;
    float m = -INFINITY, l = 0.f;
    f32x4 acc[2] = {f32x4{0.f, 0.f, 0.f, 0.f}, f32x4{0.f, 0.f, 0.f, 0.f}};
    const unsigned aPA = WS_PA + (((grp * 8 + (c16 & 7)) * 8 + 2 * w + (c16 >> 3)) * 64 + j) * 64 + g4 * 16;
    const unsigned aML = 8 * w + lane < 8 ? WS_ML + (b * 8 + lane) * 16 : OOB;
#pragma unroll 1
    for (int u0 = ukv; u0 < U_POST; u0 += 4) {
      const unsigned tb = (unsigned)(u0 & 31) * 4096u;
      wait_full(u0 + 2, seen, bud);
      f32x4 s0 = {0.f, 0.f, 0.f, 0.f}, s1 = {0.f, 0.f, 0.f, 0.f};
#pragma unroll
      for (int u = 0; u < 4; ++u) {
        s0 = mfma(lds128(tb + ka[u]), qf[u], s0);
        s1 = mfma(lds128(tb + ka[u] + 4096), qf[u], s1);
      }
      float s[4];
#pragma unroll
      for (int r = 0; r < 4; ++r) s[r] = rbf(rbf(up ? s1[r] : s0[r]) * scale);
      const float mn = fmaxf(m, xrow_max(fmaxf(fmaxf(s[0], s[1]), fmaxf(s[2], s[3]))));
      const float al = __builtin_amdgcn_exp2f((m - mn) * LOG2E);
      m = mn;
      float p[4];
#pragma unroll
      for (int r = 0; r < 4; ++r) p[r] = __builtin_amdgcn_exp2f((s[r] - mn) * LOG2E);
      l = l * al + ((p[0] + p[1]) + (p[2] + p[3]));
      const unsigned p01 = pk2(p[0], p[1]), p23 = pk2(p[2], p[3]);
      v4u pb = up ? v4u{0u, 0u, p01, p23} : v4u{p01, p23, 0u, 0u};
      acc[0] *= al; acc[1] *= al;
      asm volatile("" : "+v"(pb), "+v"(acc[0]), "+v"(acc[1]), "+v"(l));
      wait_full(u0 + 4, seen, bud);
      const v4u v0 = ldstr2(tb + va[0], tb + va[0] + 4096), v1 = ldstr2(tb + va[1], tb + va[1] + 4096);
      acc[0] = mfma(v0, pb, acc[0]);
      acc[1] = mfma(v1, pb, acc[1]);
      asm volatile("s_waitcnt lgkmcnt(0)" ::: "memory");
      lds_st32(L_PROG + 4 * w, (unsigned)(u0 + 4));
    }
    const float M = fmaxf(m, dpp<ROR8>(m));
    const float f = __builtin_amdgcn_exp2f((m - M) * LOG2E);
    float lt = xrow_sum(l) * f;
    lt += dpp<ROR8>(lt);
    f32x4 o;
#pragma unroll
    for (int e = 0; e < 4; ++e) {
      const float a0 = acc[0][e] * f, a1 = acc[1][e] * f;
      o[e] = (up ? a1 : a0) + dpp<ROR8>(up ? a0 : a1);
    }
    gst(rs, aPA, tag4(o, T));
    gst(rs, aML, v4u{asu(M), T.t, asu(lt), T.y});
  }

  if (isO || isG || isD) {
    const v4u ones = {0x3F803F80u, 0x3F803F80u, 0x3F803F80u, 0x3F803F80u};
    const unsigned aPC = (((8 * cb + 2 * w) * 4 + 2 * g4 + (c16 >> 3)) * 16 + r16) * 128 + (c16 & 7) * 16;
    const unsigned aOut = isG ? WS_PC + aPC : (isO ? WS_PB : WS_PD) + (((4 * w + g4) * 4 + (c16 >> 2)) * 64 + idx) * 64 + (c16 & 3) * 16;
    float h = 0.f;
    wait_full(isO ? 0 : U_POST + 16, seen, bud);
    drain<1, U_POST>(!isO, wa, W);
    wait_full(isQ ? U_POST + 32 : 0, seen, bud);
    drain<0, U_POST + 16>(isQ, wa, W);
    if (isO) {
      v4u v[4]; float ms, ls;
      if (DLA) __builtin_amdgcn_s_sleep(DLA);
      gather<4, true, M10, GAPA>(rs, WS_PA + (idx * 4 + w) * 4096u + lane * 16,
                                 WS_ML + ((grp * 64 + 16 * p4 + sub) * 8 + (j >> 1)) * 16, T, v, ms, ls, bud);
      const float LOG2E = 1.4426950408889634f;
      const float M = xrow_max(row_max(ms));
      const float f = __builtin_amdgcn_exp2f((ms - M) * LOG2E);
      const float L = xrow_sum(row_sum(f * ls));
      f32x4 a = asf4(v[0]) * dpp<0x00>(f);
      a += asf4(v[1]) * dpp<0x55>(f);
      a += asf4(v[2]) * dpp<0xAA>(f);
      a += asf4(v[3]) * dpp<0xFF>(f);
      const float o = red_scatter(a) * __builtin_amdgcn_rcpf(L);
      lds_st16<0x000F000Fu>(L_VEC + (w >> 1) * 64 + p4 * 16 + (w & 1) * 8 + g4 * 2, pk2(o, o));
    } else {
      if (isG) {
        gst(rs, WS_CS + aPC, tag4(vecmat(ones, ones, W, g4), T));
        v4u v[4]; float d0, d1;
        if (DLB) __builtin_amdgcn_s_sleep(DLB);
        gather<4, false, M10, GAPB>(rs, WS_PB + (r16 * 4 + w) * 4096u + lane * 16, 0, T, v, d0, d1, bud);
        const f32x4 a = (asf4(v[0]) + asf4(v[1])) + (asf4(v[2]) + asf4(v[3]));
        h = rbf(xres + rbf(red_scatter(a)));
        lds_st16<0x000F000Fu>(L_VEC + (g4 >> 1) * 64 + w * 16 + (g4 & 1) * 8 + p4 * 2, asu(h) >> 16);
      } else {
        const unsigned gPC = (idx * 4 + w) * 2048 + lane * 16;
        float Cg, Cu;
        {
          v4u v[2]; float d0, d1;
          gather<2, false, M10, 4>(rs, WS_CS + gPC, 0, T, v, d0, d1, bud);
          red_gu(asf4(v[0]) + asf4(v[1]), Cg, Cu);
        }
        v4u v[2]; float sv0, sv2;
        if (DLC) __builtin_amdgcn_s_sleep(DLC);
        gather<2, true, M10, GAPB>(rs, WS_PC + gPC, WS_ST + (idx * 16 + c16) * 64 + w * 16, T, v, sv0, sv2, bud);
        const float mean = row_sum(sv0) * (1.f / 1024.f);
        const float var = fmaxf(row_sum(sv2) * (1.f / 1024.f) - mean * mean, 0.f);
        const float mb = rbf(mean), sd = rbf(__builtin_amdgcn_sqrtf(rbf(rbf(var) + 1e-5f)));
        const float rsd = __builtin_amdgcn_rcpf(sd);
        float G, U;
        red_gu(asf4(v[0]) + asf4(v[1]), G, U);
        const float g = rbf((G - mb * Cg) * rsd), u = rbf((U - mb * Cu) * rsd);
        const float y = 0.7978845608028654f * (g + 0.044715f * g * g * g);
        const float ge = rbf(g * __builtin_amdgcn_rcpf(1.f + __builtin_amdgcn_exp2f(y * (-2.f * 1.4426950408889634f))));
        const float act = ge * u;
        lds_st16<0xFFu>(L_VEC + (w >> 1) * 64 + (2 * (w & 1) + ((lane >> 2) & 1)) * 16 + (lane >> 5) * 8 + p4 * 2, pk2(act, act));
      }
    }
    lds_st32(L_XF + 16 + 4 * w, 1u);
    v4u f0, f1;
    exch(L_XF + 16, L_VEC + g4 * 16, f0, f1, bud);
    if (isG) {
      f32x4 s2 = mfma(f0, f0, f32x4{0.f, 0.f, 0.f, 0.f}), s1 = mfma(f0, ones, f32x4{0.f, 0.f, 0.f, 0.f});
      s2 = mfma(f1, f1, s2); s1 = mfma(f1, ones, s1);
      gst(rs, lane < 8 ? WS_ST + ((8 * cb + 2 * w + (lane >> 2)) * 16 + r16) * 64 + p4 * 16 : OOB, v4u{asu(s1[0]), T.t, asu(s2[0]), T.y});
    }
    gst(rs, aOut, tag4(vecmat(f0, f1, W, g4), T));
    if (isG) {
      if (idx == 111 && w == 0) __builtin_amdgcn_raw_buffer_store_b32(T.t, rs, lane ? (int)OOB : 0, 0, 16);
      if (w == cb) {
        v4u v[4]; float d0, d1;
        if (DLD) __builtin_amdgcn_s_sleep(DLD);
        gather<4, false, M10, GAPB>(rs, WS_PD + (r16 * 4 + w) * 4096u + lane * 16, 0, T, v, d0, d1, bud);
        const f32x4 a = (asf4(v[0]) + asf4(v[1])) + (asf4(v[2]) + asf4(v[3]));
        const float res = h + rbf(red_scatter(a));
        __builtin_amdgcn_raw_buffer_store_b16((unsigned short)pk2(res, res), mkrs(mkptr(kb[0], kb[1]), 2048), (int)(c16 < 4 ? 2 * hcol : OOB), 0, 16);
        asm volatile("mk_code_end:");
      }
    }
  }
}

extern "C" void kernel_launch(void* const* d_in, const int* in_sizes, int n_in, void* d_out, int out_size, void* d_ws,
                              size_t ws_size, hipStream_t stream) {
  (void)in_sizes; (void)n_in; (void)out_size; (void)ws_size;
  (void)hipFuncSetAttribute((const void*)mk, hipFuncAttributeMaxDynamicSharedMemorySize, (int)LDS_BYTES);
  mk<<<256, 320, LDS_BYTES, stream>>>(d_in[0], d_in[1], d_in[2], d_in[3], d_in[4], d_in[6], d_in[7], d_in[8], d_out, d_ws);
}
```

```cpp
#include <hip/hip_runtime.h>
#include <stdint.h>

typedef unsigned v4u __attribute__((ext_vector_type(4)));
typedef unsigned v2u __attribute__((ext_vector_type(2)));
typedef float f32x4 __attribute__((ext_vector_type(4)));
typedef float f32x2 __attribute__((ext_vector_type(2)));
typedef __bf16 bf16x8_t __attribute__((ext_vector_type(8)));
typedef __bf16 bf16x2_t __attribute__((ext_vector_type(2)));
typedef short s16x4 __attribute__((ext_vector_type(4)));
#define LAS __attribute__((address_space(3)))
#define GAS __attribute__((address_space(1)))

constexpr unsigned WS_ET = 0x004000;
constexpr unsigned WS_ML = 0x008000;
constexpr unsigned WS_ST = 0x010000;
constexpr unsigned WS_QF = 0x040000;
constexpr unsigned WS_PB = 0x080000;
constexpr unsigned WS_PD = 0x0C0000;
constexpr unsigned WS_PA = 0x100000;
constexpr unsigned WS_PC = 0x200000;
constexpr unsigned WS_CS = 0x280000;
constexpr unsigned WS_END = 0x300000;
constexpr unsigned OOB = 0x10000000;

constexpr unsigned L_CTL = 131072;
constexpr unsigned L_FULL = L_CTL;
constexpr unsigned L_PROG = L_CTL + 16;
constexpr unsigned L_XF = L_CTL + 32;
constexpr unsigned L_ETA = L_CTL + 64;
constexpr unsigned L_VEC = L_CTL + 256;
constexpr unsigned L_XN = L_CTL + 1024;
constexpr unsigned L_Q = L_CTL + 3072;
constexpr unsigned LDS_BYTES = L_CTL + 5120;

#define INFL 4
#define INFL0 4
#define INFLQ 14
#define INFLP 4
#define INFLD 6
#define QAT 28
#define PF 5
#define QATQ 40
#define CPF 1
#define NE 3
#define KE 160
#define DA 60
#define DB 240
#define DC 370
#define DD 495
#define DS DB
#define CPF2 1
#define GAPA 0
#define GAPB 0
#define CTL 1
#define CT_A 200
#define CT_E 2230
#define CT_G 50
#define CT_LO 3
#define CT_HI 8
constexpr int U_POST = 152;
constexpr int BUDGET = 1 << 20;

__device__ __forceinline__ float asf(unsigned u) { return __builtin_bit_cast(float, u); }
__device__ __forceinline__ unsigned asu(float f) { return __builtin_bit_cast(unsigned, f); }
__device__ __forceinline__ unsigned pk2(float lo, float hi) {
  return __builtin_bit_cast(unsigned, __builtin_convertvector(f32x2{lo, hi}, bf16x2_t));
}
__device__ __forceinline__ float rbf(float f) { return asf(pk2(f, f) << 16); }
__device__ __forceinline__ float lo16(unsigned u) { return asf(u << 16); }
__device__ __forceinline__ float hi16(unsigned u) { return asf(u & 0xFFFF0000u); }
__device__ __forceinline__ f32x4 mfma(v4u a, v4u b, f32x4 c) {
  return __builtin_amdgcn_mfma_f32_16x16x32_bf16(__builtin_bit_cast(bf16x8_t, a), __builtin_bit_cast(bf16x8_t, b), c, 0, 0, 0);
}
template <int CTRL> __device__ __forceinline__ float dpp(float x) {
  return asf((unsigned)__builtin_amdgcn_mov_dpp((int)asu(x), CTRL, 0xf, 0xf, true));
}
constexpr int XOR1 = 0xB1, XOR2 = 0x4E, ROR4 = 0x124, ROR8 = 0x128;
__device__ __forceinline__ float xrow_max(float x) {
  auto s = __builtin_amdgcn_permlane16_swap(asu(x), asu(x), false, false);
  x = fmaxf(asf(s[0]), asf(s[1]));
  auto t = __builtin_amdgcn_permlane32_swap(asu(x), asu(x), false, false);
  return fmaxf(asf(t[0]), asf(t[1]));
}
__device__ __forceinline__ float xrow_sum(float x) {
  auto s = __builtin_amdgcn_permlane16_swap(asu(x), asu(x), false, false);
  x = asf(s[0]) + asf(s[1]);
  auto t = __builtin_amdgcn_permlane32_swap(asu(x), asu(x), false, false);
  return asf(t[0]) + asf(t[1]);
}
__device__ __forceinline__ float row_sum(float x) {
  x += dpp<XOR1>(x); x += dpp<XOR2>(x); x += dpp<ROR4>(x); x += dpp<ROR8>(x); return x;
}
__device__ __forceinline__ float row_max(float x) {
  x = fmaxf(x, dpp<XOR1>(x)); x = fmaxf(x, dpp<XOR2>(x)); x = fmaxf(x, dpp<ROR4>(x)); x = fmaxf(x, dpp<ROR8>(x)); return x;
}
__device__ __forceinline__ float red_scatter(f32x4 a) {
  auto s0 = __builtin_amdgcn_permlane32_swap(asu(a[0]), asu(a[2]), false, false);
  auto s1 = __builtin_amdgcn_permlane32_swap(asu(a[1]), asu(a[3]), false, false);
  const float b0 = asf(s0[0]) + asf(s0[1]), b1 = asf(s1[0]) + asf(s1[1]);
  auto t = __builtin_amdgcn_permlane16_swap(asu(b0), asu(b1), false, false);
  float c = asf(t[0]) + asf(t[1]);
  c += dpp<ROR4>(c); c += dpp<ROR8>(c);
  return c;
}
__device__ __forceinline__ void lds_st32(unsigned a, unsigned v) { asm volatile("ds_write_b32 %0, %1" ::"v"(a), "v"(v) : "memory"); }
template <unsigned M> __device__ __forceinline__ void lds_st16(unsigned a, unsigned v) {
  asm volatile("s_mov_b32 exec_lo, %2\n\ts_mov_b32 exec_hi, %2\n\tds_write_b16 %0, %1\n\ts_mov_b64 exec, -1" ::"v"(a), "v"(v), "n"(M) : "memory");
}
__device__ __forceinline__ void lds_st64(unsigned a, v2u v) { asm volatile("ds_write_b64 %0, %1" ::"v"(a), "v"(v) : "memory"); }
__device__ __forceinline__ v4u lds_ld128_wait(unsigned a) {
  v4u r; asm volatile("ds_read_b128 %0, %1\n\ts_waitcnt lgkmcnt(0)" : "=v"(r) : "v"(a) : "memory"); return r;
}
__device__ __forceinline__ v4u lds128(unsigned a) { return *(const LAS v4u*)(uintptr_t)a; }
__device__ __forceinline__ v2u ldstr(unsigned a) {
  return __builtin_bit_cast(v2u, __builtin_amdgcn_ds_read_tr16_b64_v4i16((LAS s16x4*)(uintptr_t)a));
}
__device__ __forceinline__ v4u ldstr2(unsigned a0, unsigned a1) {
  const v2u x = ldstr(a0), y = ldstr(a1); return v4u{x[0], x[1], y[0], y[1]};
}
__device__ __forceinline__ __amdgpu_buffer_rsrc_t mkrs(const void* p, unsigned bytes) {
  return __builtin_amdgcn_make_buffer_rsrc((void*)p, (short)0, (int)bytes, 0x00020000);
}
__device__ __forceinline__ void gst(__amdgpu_buffer_rsrc_t rs, unsigned vo, v4u v) { __builtin_amdgcn_raw_buffer_store_b128(v, rs, (int)vo, 0, 16); }
struct Tags { unsigned t, y, h[4]; };
__device__ __forceinline__ Tags mktags(unsigned T) {
  Tags g; g.t = T;
  const unsigned x = T ^ (T >> 16);
  const unsigned c[4] = {0x9E3779B1u, 0x85EBCA77u, 0xC2B2AE3Du, 0x27D4EB2Fu};
  g.y = x * 0x7FEB352Du;
#pragma unroll
  for (int e = 0; e < 4; ++e) g.h[e] = (((x * c[e]) >> 16) & 0xFFFEu) | (T & 1u);
  return g;
}
constexpr unsigned M10 = 0x3FFu, M16 = 0xFFFFu;
__device__ __forceinline__ v4u tag4(f32x4 a, const Tags& T) {
  v4u r;
#pragma unroll
  for (int e = 0; e < 4; ++e) r[e] = ((asu(a[e]) + 0x200u) & ~M10) | (T.h[e] & M10);
  return r;
}

__device__ __forceinline__ void wait_full(int need, int& seen, int& bud) {
  unsigned t;
  asm volatile("s_cmp_ge_i32 %[seen], %[need]\n\ts_cbranch_scc1 2f\n\t"
               "1:\n\tds_read_b32 %[t], %[a]\n\ts_waitcnt lgkmcnt(0)\n\tv_readfirstlane_b32 %[seen], %[t]\n\t"
               "s_cmp_ge_i32 %[seen], %[need]\n\ts_cbranch_scc1 2f\n\t"
               "s_sub_u32 %[cnt], %[cnt], 1\n\ts_cmp_gt_i32 %[cnt], 0\n\ts_cbranch_scc0 2f\n\ts_sleep 1\n\ts_branch 1b\n\t2:"
               : [t] "=&v"(t), [seen] "+s"(seen), [cnt] "+s"(bud) : [need] "s"(need), [a] "v"(L_FULL) : "memory", "scc");
}
__device__ __forceinline__ void gate(int d, int& bud) {
  unsigned t; int e, n;
  asm volatile("1:\n\tds_read_b32 %[t], %[a]\n\ts_memrealtime vcc\n\ts_waitcnt lgkmcnt(0)\n\tv_readfirstlane_b32 %[e], %[t]\n\t"
               "s_sub_u32 %[cnt], %[cnt], 1\n\ts_cmp_gt_i32 %[cnt], 0\n\ts_cbranch_scc0 3f\n\t"
               "s_cmp_eq_u32 %[e], 0\n\ts_cbranch_scc1 1b\n\t"
               "s_add_u32 %[e], %[e], %[d]\n\t"
               "2:\n\ts_sub_u32 %[n], vcc_lo, %[e]\n\ts_cmp_ge_i32 %[n], 0\n\ts_cbranch_scc1 3f\n\t"
               "s_memrealtime vcc\n\ts_sub_u32 %[cnt], %[cnt], 1\n\ts_cmp_gt_i32 %[cnt], 0\n\ts_waitcnt lgkmcnt(0)\n\ts_cbranch_scc1 2b\n\t3:"
               : [t] "=&v"(t), [e] "=&s"(e), [n] "=&s"(n), [cnt] "+s"(bud) : [a] "v"(L_ETA), [d] "s"(d) : "memory", "scc", "vcc");
}
__device__ __forceinline__ void exch(unsigned flags, unsigned a, v4u& f0, v4u& f1, int& bud) {
  asm volatile("1:\n\tds_read_b128 v[228:231], %[fl]\n\tds_read_b128 %[f0], %[a]\n\tds_read_b128 %[f1], %[a] offset:64\n\t"
               "s_waitcnt lgkmcnt(2)\n\t"
               "v_and_b32 v228, v228, v229\n\tv_bitop3_b32 v228, v228, v230, v231 bitop3:0x80\n\t"
               "v_cmp_eq_u32 vcc, 0, v228\n\ts_waitcnt lgkmcnt(0)\n\ts_cbranch_vccz 2f\n\t"
               "s_sub_u32 %[cnt], %[cnt], 1\n\ts_cmp_gt_i32 %[cnt], 0\n\ts_cbranch_scc1 1b\n\t2:"
               : [f0] "=&v"(f0), [f1] "=&v"(f1), [cnt] "+s"(bud) : [fl] "v"(flags), [a] "v"(a)
               : "memory", "vcc", "scc", "v228", "v229", "v230", "v231");
}
#define G_LD(R, OFF) "buffer_load_dwordx4 " R ", %[vo], %[rs], 0 offen offset:" #OFF " sc1\n\t"
#define G_LDX "buffer_load_dwordx4 v[224:227], %[xo], %[rs], 0 offen sc1\n\t"
#define G_CHK(W, A0, A1, A2, A3, R0, R1, R2, R3, B)                                                             \
  "s_waitcnt vmcnt(" #W ")\n\t"                                                                                 \
  "v_xor_b32 %[" #A0 "], %[tm0], " R0 "\n\tv_xor_b32 %[" #A1 "], %[tm1], " R1 "\n\t"                                 \
  "v_xor_b32 %[" #A2 "], %[tm2], " R2 "\n\tv_xor_b32 %[" #A3 "], %[tm3], " R3 "\n\t"                                 \
  "v_or3_b32 v233, %[" #A0 "], %[" #A1 "], %[" #A2 "]\n\t"                                                        \
  "v_bitop3_b32 " B ", v233, %[mk], %[" #A3 "] bitop3:0xc8\n\t"
#define G_CHKX(W) "s_waitcnt vmcnt(" #W ")\n\tv_xor_b32 v232, %[t], v225\n\tv_bitop3_b32 v232, v232, v227, %[ty] bitop3:0xf6\n\tv_mov_b32 %[x0], v224\n\tv_mov_b32 %[x2], v226\n\t"
#define G_TEST(B) "v_cmp_ne_u32 vcc, 0, " B "\n\ts_cbranch_vccz 2f\n\ts_sub_u32 %[cnt], %[cnt], 1\n\ts_cmp_gt_i32 %[cnt], 0\n\ts_cbranch_scc0 2f\n\ts_sleep %[gap]\n\t"
#define G_RE(B, LD) "v_cmp_ne_u32 vcc, 0, " B "\n\ts_cbranch_vccz 3f\n\ts_mov_b64 exec, vcc\n\t" LD "s_mov_b64 exec, -1\n\t3:\n\t"
#define G_END "s_waitcnt vmcnt(0)\n\ts_branch 1b\n\t2:"
#define G_C0(W) G_CHK(W, a0, a1, a2, a3, "v208", "v209", "v210", "v211", "v228")
#define G_C1(W) G_CHK(W, b0, b1, b2, b3, "v212", "v213", "v214", "v215", "v229")
#define G_C2(W) G_CHK(W, c0, c1, c2, c3, "v216", "v217", "v218", "v219", "v230")
#define G_C3(W) G_CHK(W, d0, d1, d2, d3, "v220", "v221", "v222", "v223", "v231")
#define G_L0 G_LD("v[208:211]", 0)
#define G_L1 G_LD("v[212:215]", 1024)
#define G_L2 G_LD("v[216:219]", 2048)
#define G_L3 G_LD("v[220:223]", 3072)
#define G_OA [a0] "=&v"(o[0]), [a1] "=&v"(o[1]), [a2] "=&v"(o[2]), [a3] "=&v"(o[3])
#define G_OB [b0] "=&v"(o[4]), [b1] "=&v"(o[5]), [b2] "=&v"(o[6]), [b3] "=&v"(o[7])
#define G_OC [c0] "=&v"(o[8]), [c1] "=&v"(o[9]), [c2] "=&v"(o[10]), [c3] "=&v"(o[11]), \
             [d0] "=&v"(o[12]), [d1] "=&v"(o[13]), [d2] "=&v"(o[14]), [d3] "=&v"(o[15])
#define G_OX [x0] "=&v"(x0), [x2] "=&v"(x2)
#define G_IN [vo] "v"(vo), [rs] "s"(rs), [tm0] "s"(T.h[0] & MASK), [tm1] "s"(T.h[1] & MASK), [tm2] "s"(T.h[2] & MASK), [tm3] "s"(T.h[3] & MASK), [mk] "s"(MASK), [gap] "n"(GAP)
#define G_INX [xo] "v"(xo), [t] "s"(T.t), [ty] "s"(T.y)
#define G_CLOB "memory", "vcc", "scc", "v208", "v209", "v210", "v211", "v212", "v213", "v214", "v215", "v216", "v217", "v218", "v219", \
               "v220", "v221", "v222", "v223", "v224", "v225", "v226", "v227", "v228", "v229", "v230", "v231", "v232", "v233"
template <int N, bool X, unsigned MASK, int GAP>
__device__ __forceinline__ void gather(__amdgpu_buffer_rsrc_t rs, unsigned vo, unsigned xo, const Tags& T, v4u (&v)[N], float& x0, float& x2, int& cnt) {
  unsigned o[16];
  if constexpr (N == 1) {
    asm volatile(G_L0 "1:\n\t" G_C0(0) G_TEST("v228") G_RE("v228", G_L0) G_END
                 : G_OA, [cnt] "+s"(cnt) : G_IN : G_CLOB);
  } else if constexpr (N == 2 && !X) {
    asm volatile(G_L0 G_L1 "1:\n\t" G_C0(1) G_C1(0) "v_or_b32 v233, v228, v229\n\t" G_TEST("v233") G_RE("v228", G_L0) G_RE("v229", G_L1) G_END
                 : G_OA, G_OB, [cnt] "+s"(cnt) : G_IN : G_CLOB);
  } else if constexpr (N == 2 && X) {
    asm volatile(G_LDX G_L0 G_L1 "1:\n\t" G_CHKX(2) G_C0(1) G_C1(0) "v_or3_b32 v233, v228, v229, v232\n\t" G_TEST("v233")
                 G_RE("v232", G_LDX) G_RE("v228", G_L0) G_RE("v229", G_L1) G_END
                 : G_OA, G_OB, G_OX, [cnt] "+s"(cnt) : G_IN, G_INX : G_CLOB);
  } else if constexpr (N == 4 && !X) {
    asm volatile(G_L0 G_L1 G_L2 G_L3 "1:\n\t" G_C0(3) G_C1(2) G_C2(1) "v_or3_b32 v232, v228, v229, v230\n\t" G_C3(0) "v_or_b32 v233, v232, v231\n\t" G_TEST("v233")
                 G_RE("v228", G_L0) G_RE("v229", G_L1) G_RE("v230", G_L2) G_RE("v231", G_L3) G_END
                 : G_OA, G_OB, G_OC, [cnt] "+s"(cnt) : G_IN : G_CLOB);
  } else {
    asm volatile(G_LDX G_L0 G_L1 G_L2 G_L3 "1:\n\t" G_CHKX(4) G_C0(3) G_C1(2) G_C2(1) G_C3(0)
                 "v_or3_b32 v233, v228, v229, v230\n\tv_or3_b32 v233, v233, v231, v232\n\t" G_TEST("v233")
                 G_RE("v232", G_LDX) G_RE("v228", G_L0) G_RE("v229", G_L1) G_RE("v230", G_L2) G_RE("v231", G_L3) G_END
                 : G_OA, G_OB, G_OC, G_OX, [cnt] "+s"(cnt) : G_IN, G_INX : G_CLOB);
  }
#pragma unroll
  for (int i = 0; i < N; ++i) v[i] = v4u{o[4 * i], o[4 * i + 1], o[4 * i + 2], o[4 * i + 3]};
}
__device__ __forceinline__ f32x4 asf4(v4u v) { return __builtin_bit_cast(f32x4, v); }


#define Q_LD(R, O, S)                                                                                  \
  "buffer_load_dwordx4 %[" O "0], %[" R "], %[" S "] offen nt lds\n\t"                                    \
  "buffer_load_dwordx4 %[" O "1], %[" R "], %[" S "] offen offset:1024 nt lds\n\t"                        \
  "buffer_load_dwordx4 %[" O "2], %[" R "], %[" S "] offen offset:2048 nt lds\n\t"                        \
  "buffer_load_dwordx4 %[" O "3], %[" R "], %[" S "] offen offset:3072 nt lds\n\t"
#define Q_PRE "s_max_i32 %[full], %[full], %[f]\n\tv_mov_b32 v233, %[full]\n\ts_add_i32 %[f], %[f], 1\n\ts_waitcnt vmcnt(%[n])\n\tds_write_b32 %[af], v233\n\t"
#define Q_NXT "s_add_u32 m0, m0, 0x1000\n\t"
template <int D>
__device__ __forceinline__ unsigned quad(__amdgpu_buffer_rsrc_t ra, __amdgpu_buffer_rsrc_t rb, const unsigned (&a)[4], const unsigned (&c)[4],
                                         unsigned s0, unsigned s1, unsigned s2, unsigned s3, int& u, int& full) {
  unsigned long long t; int f = u - D;
  asm volatile("s_mov_b32 m0, %[m]\n\t" Q_PRE Q_LD("ra", "a", "s0") Q_NXT Q_PRE Q_LD("ra", "a", "s1") Q_NXT Q_PRE Q_LD("rb", "c", "s2")
               Q_NXT Q_PRE "s_memrealtime %[t]\n\t" Q_LD("rb", "c", "s3") "s_waitcnt lgkmcnt(0)"
               : [t] "=&s"(t), [f] "+s"(f), [full] "+s"(full)
               : [m] "s"((unsigned)(u & 31) * 4096u), [n] "n"(4 * D), [af] "v"(L_FULL), [ra] "s"(ra), [rb] "s"(rb),
                 [a0] "v"(a[0]), [a1] "v"(a[1]), [a2] "v"(a[2]), [a3] "v"(a[3]), [c0] "v"(c[0]), [c1] "v"(c[1]), [c2] "v"(c[2]), [c3] "v"(c[3]),
                 [s0] "s"(s0), [s1] "s"(s1), [s2] "s"(s2), [s3] "s"(s3)
               : "memory", "scc", "v233");
  u += 4;
  return (unsigned)t;
}
template <int FROM, int TO> __device__ __forceinline__ void settle(int u, int& full) {
  if constexpr (FROM >= TO) {
    asm volatile("s_max_i32 %0, %0, %2\n\tv_mov_b32 v233, %0\n\ts_waitcnt vmcnt(%3)\n\tds_write_b32 %1, v233"
                 : "+s"(full) : "v"(L_FULL), "s"(u - FROM), "n"(4 * FROM) : "memory", "scc", "v233");
    settle<FROM - 1, TO>(u, full);
  }
}

typedef unsigned v32u __attribute__((ext_vector_type(32)));
#define D_K(RB, K, H, B)                                                                                          \
  "v_xor_b32 v[208+" #K "], 32*" #K ", %[" B "]\n\t"                                                              \
  "ds_read_b64_tr_b16 v[" #RB "+8*" #K "+" #H ":" #RB "+8*" #K "+" #H "+1], v[208+" #K "]\n\t"                       \
  "ds_read_b64_tr_b16 v[" #RB "+8*" #K "+" #H "+2:" #RB "+8*" #K "+" #H "+3], v[208+" #K "] offset:16384\n\t"
#define D_H(RB, H, B) D_K(RB, 0, H, B) D_K(RB, 1, H, B) D_K(RB, 2, H, B) D_K(RB, 3, H, B) D_K(RB, 4, H, B) D_K(RB, 5, H, B) D_K(RB, 6, H, B) D_K(RB, 7, H, B)
#define D_ALL(RB) "s_cmp_eq_u32 %[on], 0\n\ts_cbranch_scc1 1f\n\t" D_H(RB, 0, "b0") D_H(RB, 4, "b1") "s_waitcnt lgkmcnt(0)\n\t1:"
#define D_CLOB "memory", "scc", "v208", "v209", "v210", "v211", "v212", "v213", "v214", "v215"
template <int P, int U> __device__ __forceinline__ void drain(bool on, unsigned wa, v32u (&W)[4]) {
  const unsigned b0 = wa + (U & 31) * 4096u, b1 = wa + ((U + 8) & 31) * 4096u;
  if constexpr (P == 0) asm volatile(D_ALL(64) : "+{v[64:95]}"(W[0]), "+{v[96:127]}"(W[1]) : [on] "s"(on ? 7 : 0), [b0] "v"(b0), [b1] "v"(b1) : D_CLOB);
  else asm volatile(D_ALL(128) : "+{v[128:159]}"(W[2]), "+{v[160:191]}"(W[3]) : [on] "s"(on ? 7 : 0), [b0] "v"(b0), [b1] "v"(b1) : D_CLOB);
}
template <int O> __device__ __forceinline__ v4u sub4(const v32u& v) { return __builtin_shufflevector(v, v, O, O + 1, O + 2, O + 3); }
template <int G, int E> __device__ __forceinline__ void vm1(v4u f0, v4u f1, const v32u (&W)[4], int g4, f32x4& r) {
  constexpr int k = 2 * G + (E >> 1), i = 2 * (E & 1) + (k >> 2), o = 8 * (k & 3);
  f32x4 d = mfma(f0, sub4<o>(W[i]), f32x4{0.f, 0.f, 0.f, 0.f});
  d = mfma(f1, sub4<o + 4>(W[i]), d);
  r[E] = (G == 0 || g4 == G) ? d[0] : r[E];
}
template <int G> __device__ __forceinline__ void vm4(v4u f0, v4u f1, const v32u (&W)[4], int g4, f32x4& r) {
  vm1<G, 0>(f0, f1, W, g4, r); vm1<G, 1>(f0, f1, W, g4, r); vm1<G, 2>(f0, f1, W, g4, r); vm1<G, 3>(f0, f1, W, g4, r);
}
__device__ __forceinline__ f32x4 vecmat(v4u f0, v4u f1, const v32u (&W)[4], int g4) {
  f32x4 r = {0.f, 0.f, 0.f, 0.f};
  vm4<0>(f0, f1, W, g4, r); vm4<1>(f0, f1, W, g4, r); vm4<2>(f0, f1, W, g4, r); vm4<3>(f0, f1, W, g4, r);
  return r;
}
__device__ __forceinline__ void red_gu(f32x4 a, float& G, float& U) {
  auto s0 = __builtin_amdgcn_permlane32_swap(asu(a[0]), asu(a[2]), false, false);
  auto s1 = __builtin_amdgcn_permlane32_swap(asu(a[1]), asu(a[3]), false, false);
  G = asf(s0[0]) + asf(s0[1]); U = asf(s1[0]) + asf(s1[1]);
  auto t0 = __builtin_amdgcn_permlane16_swap(asu(G), asu(G), false, false);
  auto t1 = __builtin_amdgcn_permlane16_swap(asu(U), asu(U), false, false);
  G = asf(t0[0]) + asf(t0[1]); U = asf(t1[0]) + asf(t1[1]);
  G += dpp<ROR8>(G); U += dpp<ROR8>(U);
}

typedef unsigned v16u __attribute__((ext_vector_type(16)));
__device__ __forceinline__ const void* mkptr(unsigned lo, unsigned hi) { return (const void*)(((unsigned long long)hi << 32) | lo); }

__global__ void __launch_bounds__(320, 2)
mk(const void*, const void*, const void*, const void*, const void*, const void*, const void*, const void*, void*, void*) {
  int b = blockIdx.x;
  v16u ka; v4u kb; unsigned long long t00, pc0, w1;
  {
    unsigned t, o;
    asm volatile("s_load_dwordx16 %0, %8, 0x0\n\ts_and_b32 %6, %5, 63\n\ts_cmp_lt_u32 %6, 36\n\ts_cselect_b32 %7, 56, 40\n\t"
                 "s_cmp_lt_u32 %6, 32\n\ts_cselect_b32 %7, 24, %7\n\ts_cmp_lt_u32 %6, 16\n\ts_cselect_b32 %7, 32, %7\n\t"
                 "s_load_dwordx2 %4, %8, %7\n\ts_load_dwordx4 %1, %8, 0x40\n\ts_memrealtime %2\n\ts_getpc_b64 %3\n\tmk_code_begin:"
                 : "=&s"(ka), "=&s"(kb), "=&s"(t00), "=&s"(pc0), "=&s"(w1), "+s"(b), "=&s"(t), "=&s"(o) : "s"(__builtin_amdgcn_kernarg_segment_ptr()) : "scc");
  }
  const int lane = threadIdx.x & 63;
  const int wv = __builtin_amdgcn_readfirstlane(threadIdx.x >> 6);
  const int g4 = lane >> 4, c16 = lane & 15;
  const int grp = b >> 6, j = b & 63;
  const bool isO = j < 16, isQ = j >= 16 && j < 32, isG = j >= 36;
  const int jq = (j - 2 * grp) & 15;
  const int jd = isQ ? jq + 4 : j - 32;
  const bool isD = (unsigned)jd < (isQ ? 14u : 4u);
  const int per = isG ? 28 : isO ? 16 : 14, loc = isG ? j - 36 : isO ? j : jd;
  const int idx = per * grp + loc;
  const int r16 = idx & 15, cb = idx >> 4;
  const int ukv = j < 32 ? 32 : 16;
  const int qc = 8 * (j & 7) + 2 * grp + ((j >> 3) & 1);

  asm volatile("s_cmp_eq_u32 %4, 0\n\ts_cbranch_scc1 1f\n\ts_waitcnt lgkmcnt(0)\n\t1:" : "+s"(ka), "+s"(kb), "+s"(t00), "+s"(w1) : "s"(wv) : "scc");
  if (__builtin_expect(wv == 0, 1)) {
    const unsigned stride = isG ? 7168u : 2048u;
    const unsigned s1 = isG ? 512u * 7168u : 16u * 2048u, s2 = isG ? 16u * 7168u : 32u * 2048u;
    const unsigned base0 = (unsigned)r16 * (isG ? 32u * 7168u : 64u * 2048u) + (unsigned)cb * (isG ? 1024u : 16u * 64u * 2048u);
    const unsigned base1 = base0 + (isG ? 0u : 1024u);
    unsigned o1[4];
    const unsigned st1 = isQ ? 8192u : stride, d1 = (isQ ? 65536u : 4u * stride) - 1024u, us1 = isQ ? 32u * 8192u : 256u;
    unsigned so1 = isQ ? (unsigned)qc * 128u : base0;
    {
      const unsigned sw = 16u * ((lane & (isQ ? 7 : 15)) ^ ((lane >> 3) & 6)), r = (lane >> (isQ ? 3 : 4)) * st1;
      o1[0] = r + sw; o1[1] = r + (sw ^ (isQ ? 0u : 128u)) + d1; o1[2] = o1[0] + 2 * d1; o1[3] = o1[1] + 2 * d1;
    }
    unsigned so1b = so1 + us1, so1c = so1 + 2 * us1, so1d = so1 + 3 * us1, wb1 = j < 32 ? 4096u * 2048u : 3584u * 2048u;
    asm volatile("s_waitcnt lgkmcnt(0)" : "+s"(ka), "+s"(kb), "+s"(t00), "+s"(w1), "+s"(so1), "+s"(so1b), "+s"(so1c), "+s"(so1d), "+s"(wb1)
                 : "v"(o1[0]), "v"(o1[1]), "v"(o1[2]), "v"(o1[3]));
    const __amdgpu_buffer_rsrc_t r1 = mkrs((const void*)w1, wb1);
    asm volatile("s_mov_b32 m0, 0\n\ts_nop 0\n\t" Q_LD("ra", "a", "s0") Q_NXT "s_nop 0\n\t" Q_LD("ra", "a", "s1") Q_NXT "s_nop 0\n\t" Q_LD("ra", "a", "s2") Q_NXT "s_nop 0\n\t" Q_LD("ra", "a", "s3")
                 :: [ra] "s"(r1), [a0] "v"(o1[0]), [a1] "v"(o1[1]), [a2] "v"(o1[2]), [a3] "v"(o1[3]),
                    [s0] "s"(so1), [s1] "s"(so1b), [s2] "s"(so1c), [s3] "s"(so1d) : "memory", "scc");
    const unsigned ct0 = (unsigned)t00;
    lds_st32(L_CTL + 4 * lane, 0u);
    int u = 4, freed = 0, full = 0;
    int bud = BUDGET;
    auto space = [&]() {
      if (freed < u - 28) {
        for (unsigned spins = 0;; ++spins) {
          const v4u p = lds_ld128_wait(L_PROG);
          freed = (int)__builtin_amdgcn_readfirstlane(min(min(p[0], p[1]), min(p[2], p[3])));
          if (freed >= u - 28) break;
          if (spins == 0) settle<0, 0>(u, full);
          if (bud <= 0) break;
          --bud;
          __builtin_amdgcn_s_sleep(1);
        }
      }
    };
    if (isQ) {
#pragma unroll 1
      while (u < 32) {
        so1 += 128u * 8192u;
        const unsigned t = quad<INFLQ>(r1, r1, o1, o1, so1, so1 + 32u * 8192u, so1 + 64u * 8192u, so1 + 96u * 8192u, u, full);
        if (u == 8) __builtin_amdgcn_s_barrier();
      }
      settle<INFLQ, INFL + 1>(u, full);
    } else {
      asm volatile("s_waitcnt lgkmcnt(0)" ::: "memory");
      __builtin_amdgcn_s_barrier();
#pragma unroll 1
      while (u < ukv) {
        const unsigned so = base0 + (u >> 4) * 1024u + ((u >> 2) & 1) * s1 + ((u >> 3) & 1) * s2;
        const unsigned t = quad<INFL0>(r1, r1, o1, o1, so, so + 256, so + 512, so + 768, u, full);
      }
    }
    unsigned chV[4];
#pragma unroll
    for (int i = 0; i < 4; ++i) chV[i] = 16 * (c16 ^ (8 * (i & 1) + 2 * g4));
    {
      unsigned oK[4], oV[4];
#pragma unroll
      for (int i = 0; i < 4; ++i) {
        oK[i] = g4 * 256 + 16 * (c16 ^ (4 * i + g4));
        oV[i] = g4 * 256 + chV[i];
      }
      const __amdgpu_buffer_rsrc_t rk = mkrs(mkptr(ka[2], ka[3]), 1u << 26), rv = mkrs(mkptr(ka[4], ka[5]), 1u << 26);
      unsigned so = ((unsigned)grp << 24) + (unsigned)(j < 32 ? 30 * j : 34 * j - 128) * 8192u;
      int d = INFL;
#pragma unroll 1
      for (; u < U_POST; so += 8192) {
        space();
        unsigned t;
#define KVQ(D) t = quad<D>(rk, rv, oK, oV, so, so + 4096, so, so + 4096, u, full)
        if (!CTL) { KVQ(INFL); }
        else {
          if (d <= CT_LO + 2) { if (d <= CT_LO) KVQ(CT_LO); else if (d == CT_LO + 1) KVQ(CT_LO + 1); else KVQ(CT_LO + 2); }
          else { if (d == CT_LO + 3) KVQ(CT_LO + 3); else if (d == CT_LO + 4) KVQ(CT_LO + 4); else KVQ(CT_LO + 5); }
          const int lag = (int)(t - ct0) - (CT_A + ((full * ((CT_E - CT_A) * 256 / U_POST)) >> 8));
          d = min(INFL + ((lag * (4096 / CT_G) + 2048) >> 12), CT_HI);
        }
        if (u == 36) settle<4, 4>(u, full);
      }
    }
    constexpr int DMAX = CTL ? CT_LO + 5 : INFL;
    if (isG || isD) {
      const __amdgpu_buffer_rsrc_t r = mkrs(mkptr(isG ? ka[12] : ka[14], isG ? ka[13] : ka[15]), 3584u * 2048u);
      unsigned oW[4];
#pragma unroll
      for (int i = 0; i < 4; ++i) oW[i] = (4 * i + g4) * stride + chV[i] - 1024 * i;
      settle<DMAX, INFLD + 1>(u, full);
      if (!isQ) settle<INFLD, INFLP + 1>(u, full);
      const int end = U_POST + (isQ ? 32 : 16);
#pragma unroll 1
      while (u < end) {
        space();
        const int i = u - U_POST;
        const unsigned so = base1 - (i >> 4) * 1024u + ((i >> 2) & 1) * s1 + ((i >> 3) & 1) * s2;
        unsigned t;
        if (isQ) t = quad<INFLD>(r, r, oW, oW, so, so + 256, so + 512, so + 768, u, full);
        else t = quad<INFLP>(r, r, oW, oW, so, so + 256, so + 512, so + 768, u, full);
      }
    }
    settle<(DMAX > INFLD ? DMAX : INFLD), 0>(u, full);
    return;
  }

  const int w = wv - 1;
  const GAS unsigned short* x = (const GAS unsigned short*)mkptr(ka[0], ka[1]);
  char* ws = (char*)mkptr(kb[2], kb[3]);
  const __amdgpu_buffer_rsrc_t rs = mkrs(ws, WS_END);
  v4u xa = *(const GAS v4u*)(x + 16 * lane), xb = *(const GAS v4u*)(x + 16 * lane + 8);
  unsigned tprev;
  asm volatile("s_load_dword %0, %1, 0x0" : "=&s"(tprev) : "s"(ws));
  if (!isQ) {
    const int rb = 4 * (j >> 3) + grp + (isO ? 8 : 0);
    if (PF && w == 0) asm volatile("global_load_dword v234, %0, %1 nt" ::"v"((32 * rb + (lane >> 1)) * 8192 + (b & 7) * 1024 + 384 + (lane & 1) * 512), "s"(mkptr(ka[6], ka[7])) : "v234");
    if (CPF && w == 1) asm volatile("v_min_u32 v233, mk_code_end-mk_code_begin, %0\n\tglobal_load_dword v234, v233, %1" ::"v"((b & 24) * 512 + (b & 64) * 256 + lane * 64), "s"(pc0) : "v233", "v234");
  }
  __builtin_amdgcn_s_barrier();
  asm volatile("s_waitcnt lgkmcnt(0)" : "+s"(tprev));
  const Tags T = mktags(tprev + 1u);
  int bud = BUDGET;
  int seen = 0;
  const int sub = lane >> 2, p4 = lane & 3;
  const unsigned trrow = 256u * sub + 8u * p4;
  const unsigned hcol = (g4 & 1) * 512 + 32 * r16 + (g4 >> 1) * 16 + 4 * w + p4;
  float xres = 0.f;
  if (isQ) {
    float s = 0.f;
#pragma unroll
    for (int e = 0; e < 4; ++e) s += (lo16(xa[e]) + hi16(xa[e])) + (lo16(xb[e]) + hi16(xb[e]));
    const float mean = xrow_sum(row_sum(s)) * (1.f / 1024.f);
    float q = 0.f;
#pragma unroll
    for (int e = 0; e < 4; ++e) {
      const float d0 = lo16(xa[e]) - mean, d1 = hi16(xa[e]) - mean, d2 = lo16(xb[e]) - mean, d3 = hi16(xb[e]) - mean;
      q += (d0 * d0 + d1 * d1) + (d2 * d2 + d3 * d3);
    }
    const float var = xrow_sum(row_sum(q)) * (1.f / 1024.f);
    const float mb = rbf(mean), rsd = __builtin_amdgcn_rcpf(rbf(__builtin_amdgcn_sqrtf(rbf(rbf(var) + 1e-5f))));
    auto nrm = [&](unsigned p) { return pk2(rbf(lo16(p) - mb) * rsd, rbf(hi16(p) - mb) * rsd); };
    const unsigned xl = L_XN + (lane >> 1) * 64 + (lane & 1) * 8;
    lds_st64(xl, v2u{nrm(xa[0]), nrm(xa[1])});
    lds_st64(xl + 16, v2u{nrm(xa[2]), nrm(xa[3])});
    lds_st64(xl + 32, v2u{nrm(xb[0]), nrm(xb[1])});
    lds_st64(xl + 48, v2u{nrm(xb[2]), nrm(xb[3])});
    asm volatile("s_waitcnt lgkmcnt(0)" ::: "memory");
    unsigned a = sub * 128u + 16u * ((2 * w + (p4 >> 1)) ^ (sub & 6)) + 8u * (p4 & 1), xo = L_XN + g4 * 16;
    f32x4 ac[4] = {};
#pragma unroll 1
    for (int u0 = 0; u0 < 32; u0 += 4, a += 16384, xo += 256) {
      v4u xf[4], wf[4];
#pragma unroll
      for (int i = 0; i < 4; ++i) xf[i] = lds128(xo + 64 * i);
      wait_full(u0 + 4, seen, bud);
#pragma unroll
      for (int i = 0; i < 4; ++i) wf[i] = ldstr2(a + 4096 * i, a + 4096 * i + 2048);
      lds_st32(L_PROG + 4 * w, (unsigned)(u0 + 4));
#pragma unroll
      for (int i = 0; i < 4; ++i) ac[i] = mfma(wf[i], xf[i], ac[i]);
    }
    const f32x4 acc = (ac[0] + ac[1]) + (ac[2] + ac[3]);
    v4u r;
#pragma unroll
    for (int e = 0; e < 4; ++e) r[e] = (pk2(acc[e], acc[e]) << 16) | T.h[e];
    gst(rs, WS_QF + c16 * 16384 + qc * 256 + w * 64 + g4 * 16, r);
  } else {
    xres = lo16(x[hcol]);
    if (isG && w == cb) gst(rs, WS_PD + (r16 * 4 + w) * 4096u + 3584u + (lane & 31) * 16, tag4(f32x4{0.f, 0.f, 0.f, 0.f}, T));
  }
  asm volatile("s_waitcnt vmcnt(0)" ::: "memory");
  const unsigned wa = w * 4096u + trrow + 32u * (sub & 7);
  v32u W[4];
  asm volatile("" : "={v[64:95]}"(W[0]), "={v[96:127]}"(W[1]), "={v[128:159]}"(W[2]), "={v[160:191]}"(W[3]));
  wait_full(16, seen, bud);
  drain<0, 0>(!isQ, wa, W);
  lds_st32(L_PROG + 4 * w, isQ ? 32u : 16u);
  wait_full(ukv, seen, bud);
  drain<1, 16>(isO, wa, W);
  lds_st32(L_PROG + 4 * w, (unsigned)ukv);
  wait_full(isQ ? QATQ : QAT, seen, bud);
  v4u qf[4];
  {
    v4u v[1]; float d0, d1;
    gather<1, false, M16, 4>(rs, WS_QF + ((b & 15) * 32 + grp * 8 + 2 * w) * 512 + lane * 16, 0, T, v, d0, d1, bud);
    lds_st64(L_Q + w * 512 + lane * 8, v2u{(v[0][0] >> 16) | v[0][1], (v[0][2] >> 16) | v[0][3]});
    lds_st32(L_XF + 4 * w, 1u);
    exch(L_XF, L_Q + (c16 & 7) * 256 + g4 * 16, qf[0], qf[1], bud);
    qf[2] = lds128(L_Q + (c16 & 7) * 256 + g4 * 16 + 128);
    qf[3] = lds128(L_Q + (c16 & 7) * 256 + g4 * 16 + 192);
  }

  {
    unsigned ka[4], va[2];
#pragma unroll
    for (int u = 0; u < 4; ++u) ka[u] = 256u * c16 + 16u * ((4 * u + g4) ^ c16);
#pragma unroll
    for (int i = 0; i < 2; ++i) va[i] = 8192u + trrow + 32u * ((2 * w + i) ^ (sub & 7));
    const bool up = c16 >= 8;
    const float LOG2E = 1.4426950408889634f, scale = 0.08838834764831845f;
    float m = -INFINITY, l = 0.f;
    f32x4 acc[2] = {f32x4{0.f, 0.f, 0.f, 0.f}, f32x4{0.f, 0.f, 0.f, 0.f}};
    const unsigned aPA = WS_PA + (((grp * 8 + (c16 & 7)) * 8 + 2 * w + (c16 >> 3)) * 64 + j) * 64 + g4 * 16;
    const unsigned aML = 8 * w + lane < 8 ? WS_ML + (b * 8 + lane) * 16 : OOB;
#pragma unroll 1
    for (int u0 = ukv; u0 < U_POST; u0 += 4) {
      const unsigned tb = (unsigned)(u0 & 31) * 4096u;
      wait_full(u0 + 2, seen, bud);
      if (u0 == U_POST - 4 * NE) {
        if (w == 0) {
          const unsigned eta = (unsigned)__builtin_amdgcn_s_memrealtime() + KE;
          __builtin_amdgcn_raw_buffer_store_b32((eta << 16) | ((b & 2) ? (b & 1) ? T.h[3] : T.h[2] : (b & 1) ? T.h[1] : T.h[0]), rs, (int)(lane < 16 ? WS_ET + lane * 1024 + b * 4 : OOB), 0, 16);
        }
        if (CPF2 && w == 1) asm volatile("v_min_u32 v233, mk_code_end-mk_code_begin, %0\n\tglobal_load_dword v234, v233, %1" ::"v"((b & 24) * 512 + (b & 64) * 256 + lane * 64), "s"(pc0) : "v233", "v234");
      }
      f32x4 s0 = {0.f, 0.f, 0.f, 0.f}, s1 = {0.f, 0.f, 0.f, 0.f};
#pragma unroll
      for (int u = 0; u < 4; ++u) {
        s0 = mfma(lds128(tb + ka[u]), qf[u], s0);
        s1 = mfma(lds128(tb + ka[u] + 4096), qf[u], s1);
      }
      float s[4];
#pragma unroll
      for (int r = 0; r < 4; ++r) s[r] = rbf(rbf(up ? s1[r] : s0[r]) * scale);
      const float mn = fmaxf(m, xrow_max(fmaxf(fmaxf(s[0], s[1]), fmaxf(s[2], s[3]))));
      const float al = __builtin_amdgcn_exp2f((m - mn) * LOG2E);
      m = mn;
      float p[4];
#pragma unroll
      for (int r = 0; r < 4; ++r) p[r] = __builtin_amdgcn_exp2f((s[r] - mn) * LOG2E);
      l = l * al + ((p[0] + p[1]) + (p[2] + p[3]));
      const unsigned p01 = pk2(p[0], p[1]), p23 = pk2(p[2], p[3]);
      v4u pb = up ? v4u{0u, 0u, p01, p23} : v4u{p01, p23, 0u, 0u};
      acc[0] *= al; acc[1] *= al;
      asm volatile("" : "+v"(pb), "+v"(acc[0]), "+v"(acc[1]), "+v"(l));
      wait_full(u0 + 4, seen, bud);
      const v4u v0 = ldstr2(tb + va[0], tb + va[0] + 4096), v1 = ldstr2(tb + va[1], tb + va[1] + 4096);
      acc[0] = mfma(v0, pb, acc[0]);
      acc[1] = mfma(v1, pb, acc[1]);
      asm volatile("s_waitcnt lgkmcnt(0)" ::: "memory");
      lds_st32(L_PROG + 4 * w, (unsigned)(u0 + 4));
    }
    const float M = fmaxf(m, dpp<ROR8>(m));
    const float f = __builtin_amdgcn_exp2f((m - M) * LOG2E);
    float lt = xrow_sum(l) * f;
    lt += dpp<ROR8>(lt);
    f32x4 o;
#pragma unroll
    for (int e = 0; e < 4; ++e) {
      const float a0 = acc[0][e] * f, a1 = acc[1][e] * f;
      o[e] = (up ? a1 : a0) + dpp<ROR8>(up ? a0 : a1);
    }
    gst(rs, aPA, tag4(o, T));
    gst(rs, aML, v4u{asu(M), T.t, asu(lt), T.y});
  }

  if (isO || isG || isD) {
    const v4u ones = {0x3F803F80u, 0x3F803F80u, 0x3F803F80u, 0x3F803F80u};
    const unsigned aPC = (((8 * cb + 2 * w) * 4 + 2 * g4 + (c16 >> 3)) * 16 + r16) * 128 + (c16 & 7) * 16;
    const unsigned aOut = isG ? WS_PC + aPC : (isO ? WS_PB : WS_PD) + (((4 * w + g4) * 4 + (c16 >> 2)) * 64 + idx) * 64 + (c16 & 3) * 16;
    float h = 0.f;
    wait_full(isO ? 0 : U_POST + 16, seen, bud);
    drain<1, U_POST>(!isO, wa, W);
    wait_full(isQ ? U_POST + 32 : 0, seen, bud);
    drain<0, U_POST + 16>(isQ, wa, W);
    if (isG) gst(rs, WS_CS + aPC, tag4(vecmat(ones, ones, W, g4), T));
    if (w == 0) {
      v4u v[1]; float d0, d1;
      gather<1, false, M16, 8>(rs, WS_ET + (b & 15) * 1024 + (isO ? grp * 256 + c16 * 16 : lane * 16), 0, T, v, d0, d1, bud);
      const unsigned now = (unsigned)__builtin_amdgcn_s_memrealtime(), n16 = now << 16;
      const int dm = max(max((int)(v[0][0] - n16), (int)(v[0][1] - n16)), max((int)(v[0][2] - n16), (int)(v[0][3] - n16))) >> 16;
      const unsigned E = (now + (unsigned)min((int)xrow_max(row_max((float)dm)), 300)) | 1u;
      lds_st32(L_ETA, E);
    }
#define NF0
#define NF1(sh)
    if (isO) {
      v4u v[4]; float ms, ls;
      unsigned a0 = WS_PA + (idx * 4 + w) * 4096u + lane * 16, a1 = WS_ML + ((grp * 64 + 16 * p4 + sub) * 8 + (j >> 1)) * 16;
      asm volatile("" : "+v"(a0), "+v"(a1));
      gate(DA, bud);
      NF0;
      gather<4, true, M10, GAPA>(rs, a0, a1, T, v, ms, ls, bud);
      NF1(0);
      const float LOG2E = 1.4426950408889634f;
      const float M = xrow_max(row_max(ms));
      const float f = __builtin_amdgcn_exp2f((ms - M) * LOG2E);
      const float L = xrow_sum(row_sum(f * ls));
      f32x4 a = asf4(v[0]) * dpp<0x00>(f);
      a += asf4(v[1]) * dpp<0x55>(f);
      a += asf4(v[2]) * dpp<0xAA>(f);
      a += asf4(v[3]) * dpp<0xFF>(f);
      const float o = red_scatter(a) * __builtin_amdgcn_rcpf(L);
      lds_st16<0x000F000Fu>(L_VEC + (w >> 1) * 64 + p4 * 16 + (w & 1) * 8 + g4 * 2, pk2(o, o));
    } else {
      if (isG) {
        v4u v[4]; float d0, d1;
        unsigned a0 = WS_PB + (r16 * 4 + w) * 4096u + lane * 16;
        asm volatile("" : "+v"(a0));
        gate(DB, bud);
        NF0;
        gather<4, false, M10, GAPB>(rs, a0, 0, T, v, d0, d1, bud);
        NF1(0);
        const f32x4 a = (asf4(v[0]) + asf4(v[1])) + (asf4(v[2]) + asf4(v[3]));
        h = rbf(xres + rbf(red_scatter(a)));
        lds_st16<0x000F000Fu>(L_VEC + (g4 >> 1) * 64 + w * 16 + (g4 & 1) * 8 + p4 * 2, asu(h) >> 16);
      } else {
        const unsigned gPC = (idx * 4 + w) * 2048 + lane * 16;
        float Cg, Cu;
        {
          v4u v[2]; float d0, d1;
          gate(DS, bud);
          gather<2, false, M10, 4>(rs, WS_CS + gPC, 0, T, v, d0, d1, bud);
          red_gu(asf4(v[0]) + asf4(v[1]), Cg, Cu);
        }
        v4u v[2]; float sv0, sv2;
        unsigned a0 = WS_PC + gPC, a1 = WS_ST + (idx * 16 + c16) * 64 + w * 16;
        asm volatile("" : "+v"(a0), "+v"(a1), "+v"(Cg), "+v"(Cu));
        gate(DC, bud);
        NF0;
        gather<2, true, M10, GAPB>(rs, a0, a1, T, v, sv0, sv2, bud);
        NF1(0);
        const float mean = row_sum(sv0) * (1.f / 1024.f);
        const float var = fmaxf(row_sum(sv2) * (1.f / 1024.f) - mean * mean, 0.f);
        const float mb = rbf(mean), sd = rbf(__builtin_amdgcn_sqrtf(rbf(rbf(var) + 1e-5f)));
        const float rsd = __builtin_amdgcn_rcpf(sd);
        float G, U;
        red_gu(asf4(v[0]) + asf4(v[1]), G, U);
        const float g = rbf((G - mb * Cg) * rsd), u = rbf((U - mb * Cu) * rsd);
        const float y = 0.7978845608028654f * (g + 0.044715f * g * g * g);
        const float ge = rbf(g * __builtin_amdgcn_rcpf(1.f + __builtin_amdgcn_exp2f(y * (-2.f * 1.4426950408889634f))));
        const float act = ge * u;
        lds_st16<0xFFu>(L_VEC + (w >> 1) * 64 + (2 * (w & 1) + ((lane >> 2) & 1)) * 16 + (lane >> 5) * 8 + p4 * 2, pk2(act, act));
      }
    }
    lds_st32(L_XF + 16 + 4 * w, 1u);
    v4u f0, f1;
    exch(L_XF + 16, L_VEC + g4 * 16, f0, f1, bud);
    if (isG) {
      f32x4 s2 = mfma(f0, f0, f32x4{0.f, 0.f, 0.f, 0.f}), s1 = mfma(f0, ones, f32x4{0.f, 0.f, 0.f, 0.f});
      s2 = mfma(f1, f1, s2); s1 = mfma(f1, ones, s1);
      gst(rs, lane < 8 ? WS_ST + ((8 * cb + 2 * w + (lane >> 2)) * 16 + r16) * 64 + p4 * 16 : OOB, v4u{asu(s1[0]), T.t, asu(s2[0]), T.y});
    }
    gst(rs, aOut, tag4(vecmat(f0, f1, W, g4), T));
    if (isG) {
      if (idx == 111 && w == 0) __builtin_amdgcn_raw_buffer_store_b32(T.t, rs, lane ? (int)OOB : 0, 0, 16);
      if (w == cb) {
        v4u v[4]; float d0, d1;
        unsigned a0 = WS_PD + (r16 * 4 + w) * 4096u + lane * 16;
        asm volatile("" : "+v"(a0));
        gate(DD, bud);
        NF0;
        gather<4, false, M10, GAPB>(rs, a0, 0, T, v, d0, d1, bud);
        NF1(8);
        const f32x4 a = (asf4(v[0]) + asf4(v[1])) + (asf4(v[2]) + asf4(v[3]));
        const float res = h + rbf(red_scatter(a));
        __builtin_amdgcn_raw_buffer_store_b16((unsigned short)pk2(res, res), mkrs(mkptr(kb[0], kb[1]), 2048), (int)(c16 < 4 ? 2 * hcol : OOB), 0, 16);
        asm volatile("mk_code_end:");
      }
    }
  }
}

extern "C" void kernel_launch(void* const* d_in, const int* in_sizes, int n_in, void* d_out, int out_size, void* d_ws,
                              size_t ws_size, hipStream_t stream) {
  (void)in_sizes; (void)n_in; (void)out_size; (void)ws_size;
  (void)hipFuncSetAttribute((const void*)mk, hipFuncAttributeMaxDynamicSharedMemorySize, (int)LDS_BYTES);
  mk<<<256, 320, LDS_BYTES, stream>>>(d_in[0], d_in[1], d_in[2], d_in[3], d_in[4], d_in[6], d_in[7], d_in[8], d_out, d_ws);
}
```

```cpp
#include <hip/hip_runtime.h>
#include <stdint.h>

typedef unsigned v4u __attribute__((ext_vector_type(4)));
typedef unsigned v2u __attribute__((ext_vector_type(2)));
typedef float f32x4 __attribute__((ext_vector_type(4)));
typedef float f32x2 __attribute__((ext_vector_type(2)));
typedef __bf16 bf16x8_t __attribute__((ext_vector_type(8)));
typedef __bf16 bf16x2_t __attribute__((ext_vector_type(2)));
typedef short s16x4 __attribute__((ext_vector_type(4)));
#define LAS __attribute__((address_space(3)))
#define GAS __attribute__((address_space(1)))

constexpr unsigned WS_ET = 0x004000;
constexpr unsigned WS_ML = 0x008000;
constexpr unsigned WS_ST = 0x010000;
constexpr unsigned WS_QF = 0x040000;
constexpr unsigned WS_PB = 0x080000;
constexpr unsigned WS_PD = 0x0C0000;
constexpr unsigned WS_PA = 0x100000;
constexpr unsigned WS_PC = 0x200000;
constexpr unsigned WS_CS = 0x280000;
constexpr unsigned WS_END = 0x300000;
constexpr unsigned OOB = 0x10000000;

constexpr unsigned L_CTL = 131072;
constexpr unsigned L_FULL = L_CTL;
constexpr unsigned L_PROG = L_CTL + 16;
constexpr unsigned L_XF = L_CTL + 32;
constexpr unsigned L_ETA = L_CTL + 64;
constexpr unsigned L_VEC = L_CTL + 256;
constexpr unsigned L_XN = L_CTL + 1024;
constexpr unsigned L_Q = L_CTL + 3072;
constexpr unsigned LDS_BYTES = L_CTL + 5120;

#define INFL 4
#define INFL0 4
#define INFLQ 14
#define INFLP 4
#define INFLD 6
#define QAT 28
#define PF 5
#define QATQ 40
#define CPF 1
#define NE 3
#define KE 160
#define DA 60
#define DB 240
#define DC 360
#define DD 475
#define DS DB
#define CPF2 1
#define GAPA 0
#define GAPB 0
#define CTL 1
#define CT_A 250
#define CT_E 2280
#define CT_G 50
#define CT_LO 3
#define CT_HI 8
constexpr int U_POST = 152;
constexpr int BUDGET = 1 << 20;

__device__ __forceinline__ float asf(unsigned u) { return __builtin_bit_cast(float, u); }
__device__ __forceinline__ unsigned asu(float f) { return __builtin_bit_cast(unsigned, f); }
__device__ __forceinline__ unsigned pk2(float lo, float hi) {
  return __builtin_bit_cast(unsigned, __builtin_convertvector(f32x2{lo, hi}, bf16x2_t));
}
__device__ __forceinline__ float rbf(float f) { return asf(pk2(f, f) << 16); }
__device__ __forceinline__ float lo16(unsigned u) { return asf(u << 16); }
__device__ __forceinline__ float hi16(unsigned u) { return asf(u & 0xFFFF0000u); }
__device__ __forceinline__ f32x4 mfma(v4u a, v4u b, f32x4 c) {
  return __builtin_amdgcn_mfma_f32_16x16x32_bf16(__builtin_bit_cast(bf16x8_t, a), __builtin_bit_cast(bf16x8_t, b), c, 0, 0, 0);
}
template <int CTRL> __device__ __forceinline__ float dpp(float x) {
  return asf((unsigned)__builtin_amdgcn_mov_dpp((int)asu(x), CTRL, 0xf, 0xf, true));
}
constexpr int XOR1 = 0xB1, XOR2 = 0x4E, ROR4 = 0x124, ROR8 = 0x128;
__device__ __forceinline__ float xrow_max(float x) {
  auto s = __builtin_amdgcn_permlane16_swap(asu(x), asu(x), false, false);
  x = fmaxf(asf(s[0]), asf(s[1]));
  auto t = __builtin_amdgcn_permlane32_swap(asu(x), asu(x), false, false);
  return fmaxf(asf(t[0]), asf(t[1]));
}
__device__ __forceinline__ float xrow_sum(float x) {
  auto s = __builtin_amdgcn_permlane16_swap(asu(x), asu(x), false, false);
  x = asf(s[0]) + asf(s[1]);
  auto t = __builtin_amdgcn_permlane32_swap(asu(x), asu(x), false, false);
  return asf(t[0]) + asf(t[1]);
}
__device__ __forceinline__ float row_sum(float x) {
  x += dpp<XOR1>(x); x += dpp<XOR2>(x); x += dpp<ROR4>(x); x += dpp<ROR8>(x); return x;
}
__device__ __forceinline__ float row_max(float x) {
  x = fmaxf(x, dpp<XOR1>(x)); x = fmaxf(x, dpp<XOR2>(x)); x = fmaxf(x, dpp<ROR4>(x)); x = fmaxf(x, dpp<ROR8>(x)); return x;
}
__device__ __forceinline__ float red_scatter(f32x4 a) {
  auto s0 = __builtin_amdgcn_permlane32_swap(asu(a[0]), asu(a[2]), false, false);
  auto s1 = __builtin_amdgcn_permlane32_swap(asu(a[1]), asu(a[3]), false, false);
  const float b0 = asf(s0[0]) + asf(s0[1]), b1 = asf(s1[0]) + asf(s1[1]);
  auto t = __builtin_amdgcn_permlane16_swap(asu(b0), asu(b1), false, false);
  float c = asf(t[0]) + asf(t[1]);
  c += dpp<ROR4>(c); c += dpp<ROR8>(c);
  return c;
}
__device__ __forceinline__ void lds_st32(unsigned a, unsigned v) { asm volatile("ds_write_b32 %0, %1" ::"v"(a), "v"(v) : "memory"); }
template <unsigned M> __device__ __forceinline__ void lds_st16(unsigned a, unsigned v) {
  asm volatile("s_mov_b32 exec_lo, %2\n\ts_mov_b32 exec_hi, %2\n\tds_write_b16 %0, %1\n\ts_mov_b64 exec, -1" ::"v"(a), "v"(v), "n"(M) : "memory");
}
__device__ __forceinline__ void lds_st64(unsigned a, v2u v) { asm volatile("ds_write_b64 %0, %1" ::"v"(a), "v"(v) : "memory"); }
__device__ __forceinline__ v4u lds_ld128_wait(unsigned a) {
  v4u r; asm volatile("ds_read_b128 %0, %1\n\ts_waitcnt lgkmcnt(0)" : "=v"(r) : "v"(a) : "memory"); return r;
}
__device__ __forceinline__ v4u lds128(unsigned a) { return *(const LAS v4u*)(uintptr_t)a; }
__device__ __forceinline__ v2u ldstr(unsigned a) {
  return __builtin_bit_cast(v2u, __builtin_amdgcn_ds_read_tr16_b64_v4i16((LAS s16x4*)(uintptr_t)a));
}
__device__ __forceinline__ v4u ldstr2(unsigned a0, unsigned a1) {
  const v2u x = ldstr(a0), y = ldstr(a1); return v4u{x[0], x[1], y[0], y[1]};
}
__device__ __forceinline__ __amdgpu_buffer_rsrc_t mkrs(const void* p, unsigned bytes) {
  return __builtin_amdgcn_make_buffer_rsrc((void*)p, (short)0, (int)bytes, 0x00020000);
}
__device__ __forceinline__ void gst(__amdgpu_buffer_rsrc_t rs, unsigned vo, v4u v) { __builtin_amdgcn_raw_buffer_store_b128(v, rs, (int)vo, 0, 16); }
struct Tags { unsigned t, y, h[4]; };
__device__ __forceinline__ Tags mktags(unsigned T) {
  Tags g; g.t = T;
  const unsigned x = T ^ (T >> 16);
  const unsigned c[4] = {0x9E3779B1u, 0x85EBCA77u, 0xC2B2AE3Du, 0x27D4EB2Fu};
  g.y = x * 0x7FEB352Du;
#pragma unroll
  for (int e = 0; e < 4; ++e) g.h[e] = (((x * c[e]) >> 16) & 0xFFFEu) | (T & 1u);
  return g;
}
constexpr unsigned M10 = 0x3FFu, M16 = 0xFFFFu;
__device__ __forceinline__ v4u tag4(f32x4 a, const Tags& T) {
  v4u r;
#pragma unroll
  for (int e = 0; e < 4; ++e) r[e] = ((asu(a[e]) + 0x200u) & ~M10) | (T.h[e] & M10);
  return r;
}

__device__ __forceinline__ void wait_full(int need, int& seen, int& bud) {
  unsigned t;
  asm volatile("s_cmp_ge_i32 %[seen], %[need]\n\ts_cbranch_scc1 2f\n\t"
               "1:\n\tds_read_b32 %[t], %[a]\n\ts_waitcnt lgkmcnt(0)\n\tv_readfirstlane_b32 %[seen], %[t]\n\t"
               "s_cmp_ge_i32 %[seen], %[need]\n\ts_cbranch_scc1 2f\n\t"
               "s_sub_u32 %[cnt], %[cnt], 1\n\ts_cmp_gt_i32 %[cnt], 0\n\ts_cbranch_scc0 2f\n\ts_sleep 1\n\ts_branch 1b\n\t2:"
               : [t] "=&v"(t), [seen] "+s"(seen), [cnt] "+s"(bud) : [need] "s"(need), [a] "v"(L_FULL) : "memory", "scc");
}
__device__ __forceinline__ void gate(int d, int& bud) {
  unsigned t; int e, n;
  asm volatile("1:\n\tds_read_b32 %[t], %[a]\n\ts_memrealtime vcc\n\ts_waitcnt lgkmcnt(0)\n\tv_readfirstlane_b32 %[e], %[t]\n\t"
               "s_sub_u32 %[cnt], %[cnt], 1\n\ts_cmp_gt_i32 %[cnt], 0\n\ts_cbranch_scc0 3f\n\t"
               "s_cmp_eq_u32 %[e], 0\n\ts_cbranch_scc1 1b\n\t"
               "s_add_u32 %[e], %[e], %[d]\n\t"
               "2:\n\ts_sub_u32 %[n], vcc_lo, %[e]\n\ts_cmp_ge_i32 %[n], 0\n\ts_cbranch_scc1 3f\n\t"
               "s_memrealtime vcc\n\ts_sub_u32 %[cnt], %[cnt], 1\n\ts_cmp_gt_i32 %[cnt], 0\n\ts_waitcnt lgkmcnt(0)\n\ts_cbranch_scc1 2b\n\t3:"
               : [t] "=&v"(t), [e] "=&s"(e), [n] "=&s"(n), [cnt] "+s"(bud) : [a] "v"(L_ETA), [d] "s"(d) : "memory", "scc", "vcc");
}
__device__ __forceinline__ void exch(unsigned flags, unsigned a, v4u& f0, v4u& f1, int& bud) {
  asm volatile("1:\n\tds_read_b128 v[228:231], %[fl]\n\tds_read_b128 %[f0], %[a]\n\tds_read_b128 %[f1], %[a] offset:64\n\t"
               "s_waitcnt lgkmcnt(2)\n\t"
               "v_and_b32 v228, v228, v229\n\tv_bitop3_b32 v228, v228, v230, v231 bitop3:0x80\n\t"
               "v_cmp_eq_u32 vcc, 0, v228\n\ts_waitcnt lgkmcnt(0)\n\ts_cbranch_vccz 2f\n\t"
               "s_sub_u32 %[cnt], %[cnt], 1\n\ts_cmp_gt_i32 %[cnt], 0\n\ts_cbranch_scc1 1b\n\t2:"
               : [f0] "=&v"(f0), [f1] "=&v"(f1), [cnt] "+s"(bud) : [fl] "v"(flags), [a] "v"(a)
               : "memory", "vcc", "scc", "v228", "v229", "v230", "v231");
}
#define G_LD(R, OFF) "buffer_load_dwordx4 " R ", %[vo], %[rs], 0 offen offset:" #OFF " sc1\n\t"
#define G_LDX "buffer_load_dwordx4 v[224:227], %[xo], %[rs], 0 offen sc1\n\t"
#define G_CHK(W, A0, A1, A2, A3, R0, R1, R2, R3, B)                                                             \
  "s_waitcnt vmcnt(" #W ")\n\t"                                                                                 \
  "v_xor_b32 %[" #A0 "], %[tm0], " R0 "\n\tv_xor_b32 %[" #A1 "], %[tm1], " R1 "\n\t"                                 \
  "v_xor_b32 %[" #A2 "], %[tm2], " R2 "\n\tv_xor_b32 %[" #A3 "], %[tm3], " R3 "\n\t"                                 \
  "v_or3_b32 v233, %[" #A0 "], %[" #A1 "], %[" #A2 "]\n\t"                                                        \
  "v_bitop3_b32 " B ", v233, %[mk], %[" #A3 "] bitop3:0xc8\n\t"
#define G_CHKX(W) "s_waitcnt vmcnt(" #W ")\n\tv_xor_b32 v232, %[t], v225\n\tv_bitop3_b32 v232, v232, v227, %[ty] bitop3:0xf6\n\tv_mov_b32 %[x0], v224\n\tv_mov_b32 %[x2], v226\n\t"
#define G_TEST(B) "v_cmp_ne_u32 vcc, 0, " B "\n\ts_cbranch_vccz 2f\n\ts_sub_u32 %[cnt], %[cnt], 1\n\ts_cmp_gt_i32 %[cnt], 0\n\ts_cbranch_scc0 2f\n\ts_sleep %[gap]\n\t"
#define G_RE(B, LD) "v_cmp_ne_u32 vcc, 0, " B "\n\ts_cbranch_vccz 3f\n\ts_mov_b64 exec, vcc\n\t" LD "s_mov_b64 exec, -1\n\t3:\n\t"
#define G_END "s_waitcnt vmcnt(0)\n\ts_branch 1b\n\t2:"
#define G_C0(W) G_CHK(W, a0, a1, a2, a3, "v208", "v209", "v210", "v211", "v228")
#define G_C1(W) G_CHK(W, b0, b1, b2, b3, "v212", "v213", "v214", "v215", "v229")
#define G_C2(W) G_CHK(W, c0, c1, c2, c3, "v216", "v217", "v218", "v219", "v230")
#define G_C3(W) G_CHK(W, d0, d1, d2, d3, "v220", "v221", "v222", "v223", "v231")
#define G_L0 G_LD("v[208:211]", 0)
#define G_L1 G_LD("v[212:215]", 1024)
#define G_L2 G_LD("v[216:219]", 2048)
#define G_L3 G_LD("v[220:223]", 3072)
#define G_OA [a0] "=&v"(o[0]), [a1] "=&v"(o[1]), [a2] "=&v"(o[2]), [a3] "=&v"(o[3])
#define G_OB [b0] "=&v"(o[4]), [b1] "=&v"(o[5]), [b2] "=&v"(o[6]), [b3] "=&v"(o[7])
#define G_OC [c0] "=&v"(o[8]), [c1] "=&v"(o[9]), [c2] "=&v"(o[10]), [c3] "=&v"(o[11]), \
             [d0] "=&v"(o[12]), [d1] "=&v"(o[13]), [d2] "=&v"(o[14]), [d3] "=&v"(o[15])
#define G_OX [x0] "=&v"(x0), [x2] "=&v"(x2)
#define G_IN [vo] "v"(vo), [rs] "s"(rs), [tm0] "s"(T.h[0] & MASK), [tm1] "s"(T.h[1] & MASK), [tm2] "s"(T.h[2] & MASK), [tm3] "s"(T.h[3] & MASK), [mk] "s"(MASK), [gap] "n"(GAP)
#define G_INX [xo] "v"(xo), [t] "s"(T.t), [ty] "s"(T.y)
#define G_CLOB "memory", "vcc", "scc", "v208", "v209", "v210", "v211", "v212", "v213", "v214", "v215", "v216", "v217", "v218", "v219", \
               "v220", "v221", "v222", "v223", "v224", "v225", "v226", "v227", "v228", "v229", "v230", "v231", "v232", "v233"
template <int N, bool X, unsigned MASK, int GAP>
__device__ __forceinline__ void gather(__amdgpu_buffer_rsrc_t rs, unsigned vo, unsigned xo, const Tags& T, v4u (&v)[N], float& x0, float& x2, int& cnt) {
  unsigned o[16];
  if constexpr (N == 1) {
    asm volatile(G_L0 "1:\n\t" G_C0(0) G_TEST("v228") G_RE("v228", G_L0) G_END
                 : G_OA, [cnt] "+s"(cnt) : G_IN : G_CLOB);
  } else if constexpr (N == 2 && !X) {
    asm volatile(G_L0 G_L1 "1:\n\t" G_C0(1) G_C1(0) "v_or_b32 v233, v228, v229\n\t" G_TEST("v233") G_RE("v228", G_L0) G_RE("v229", G_L1) G_END
                 : G_OA, G_OB, [cnt] "+s"(cnt) : G_IN : G_CLOB);
  } else if constexpr (N == 2 && X) {
    asm volatile(G_LDX G_L0 G_L1 "1:\n\t" G_CHKX(2) G_C0(1) G_C1(0) "v_or3_b32 v233, v228, v229, v232\n\t" G_TEST("v233")
                 G_RE("v232", G_LDX) G_RE("v228", G_L0) G_RE("v229", G_L1) G_END
                 : G_OA, G_OB, G_OX, [cnt] "+s"(cnt) : G_IN, G_INX : G_CLOB);
  } else if constexpr (N == 4 && !X) {
    asm volatile(G_L0 G_L1 G_L2 G_L3 "1:\n\t" G_C0(3) G_C1(2) G_C2(1) "v_or3_b32 v232, v228, v229, v230\n\t" G_C3(0) "v_or_b32 v233, v232, v231\n\t" G_TEST("v233")
                 G_RE("v228", G_L0) G_RE("v229", G_L1) G_RE("v230", G_L2) G_RE("v231", G_L3) G_END
                 : G_OA, G_OB, G_OC, [cnt] "+s"(cnt) : G_IN : G_CLOB);
  } else {
    asm volatile(G_LDX G_L0 G_L1 G_L2 G_L3 "1:\n\t" G_CHKX(4) G_C0(3) G_C1(2) G_C2(1) G_C3(0)
                 "v_or3_b32 v233, v228, v229, v230\n\tv_or3_b32 v233, v233, v231, v232\n\t" G_TEST("v233")
                 G_RE("v232", G_LDX) G_RE("v228", G_L0) G_RE("v229", G_L1) G_RE("v230", G_L2) G_RE("v231", G_L3) G_END
                 : G_OA, G_OB, G_OC, G_OX, [cnt] "+s"(cnt) : G_IN, G_INX : G_CLOB);
  }
#pragma unroll
  for (int i = 0; i < N; ++i) v[i] = v4u{o[4 * i], o[4 * i + 1], o[4 * i + 2], o[4 * i + 3]};
}
__device__ __forceinline__ f32x4 asf4(v4u v) { return __builtin_bit_cast(f32x4, v); }


#define Q_LD(R, O, S)                                                                                  \
  "buffer_load_dwordx4 %[" O "0], %[" R "], %[" S "] offen nt lds\n\t"                                    \
  "buffer_load_dwordx4 %[" O "1], %[" R "], %[" S "] offen offset:1024 nt lds\n\t"                        \
  "buffer_load_dwordx4 %[" O "2], %[" R "], %[" S "] offen offset:2048 nt lds\n\t"                        \
  "buffer_load_dwordx4 %[" O "3], %[" R "], %[" S "] offen offset:3072 nt lds\n\t"
#define Q_PRE "s_max_i32 %[full], %[full], %[f]\n\tv_mov_b32 v233, %[full]\n\ts_add_i32 %[f], %[f], 1\n\ts_waitcnt vmcnt(%[n])\n\tds_write_b32 %[af], v233\n\t"
#define Q_NXT "s_add_u32 m0, m0, 0x1000\n\t"
template <int D>
__device__ __forceinline__ unsigned quad(__amdgpu_buffer_rsrc_t ra, __amdgpu_buffer_rsrc_t rb, const unsigned (&a)[4], const unsigned (&c)[4],
                                         unsigned s0, unsigned s1, unsigned s2, unsigned s3, int& u, int& full) {
  unsigned long long t; int f = u - D;
  asm volatile("s_mov_b32 m0, %[m]\n\t" Q_PRE Q_LD("ra", "a", "s0") Q_NXT Q_PRE Q_LD("ra", "a", "s1") Q_NXT Q_PRE Q_LD("rb", "c", "s2")
               Q_NXT Q_PRE "s_memrealtime %[t]\n\t" Q_LD("rb", "c", "s3") "s_waitcnt lgkmcnt(0)"
               : [t] "=&s"(t), [f] "+s"(f), [full] "+s"(full)
               : [m] "s"((unsigned)(u & 31) * 4096u), [n] "n"(4 * D), [af] "v"(L_FULL), [ra] "s"(ra), [rb] "s"(rb),
                 [a0] "v"(a[0]), [a1] "v"(a[1]), [a2] "v"(a[2]), [a3] "v"(a[3]), [c0] "v"(c[0]), [c1] "v"(c[1]), [c2] "v"(c[2]), [c3] "v"(c[3]),
                 [s0] "s"(s0), [s1] "s"(s1), [s2] "s"(s2), [s3] "s"(s3)
               : "memory", "scc", "v233");
  u += 4;
  return (unsigned)t;
}
template <int FROM, int TO> __device__ __forceinline__ void settle(int u, int& full) {
  if constexpr (FROM >= TO) {
    asm volatile("s_max_i32 %0, %0, %2\n\tv_mov_b32 v233, %0\n\ts_waitcnt vmcnt(%3)\n\tds_write_b32 %1, v233"
                 : "+s"(full) : "v"(L_FULL), "s"(u - FROM), "n"(4 * FROM) : "memory", "scc", "v233");
    settle<FROM - 1, TO>(u, full);
  }
}

typedef unsigned v32u __attribute__((ext_vector_type(32)));
#define D_K(RB, K, H, B)                                                                                          \
  "v_xor_b32 v[208+" #K "], 32*" #K ", %[" B "]\n\t"                                                              \
  "ds_read_b64_tr_b16 v[" #RB "+8*" #K "+" #H ":" #RB "+8*" #K "+" #H "+1], v[208+" #K "]\n\t"                       \
  "ds_read_b64_tr_b16 v[" #RB "+8*" #K "+" #H "+2:" #RB "+8*" #K "+" #H "+3], v[208+" #K "] offset:16384\n\t"
#define D_H(RB, H, B) D_K(RB, 0, H, B) D_K(RB, 1, H, B) D_K(RB, 2, H, B) D_K(RB, 3, H, B) D_K(RB, 4, H, B) D_K(RB, 5, H, B) D_K(RB, 6, H, B) D_K(RB, 7, H, B)
#define D_ALL(RB) "s_cmp_eq_u32 %[on], 0\n\ts_cbranch_scc1 1f\n\t" D_H(RB, 0, "b0") D_H(RB, 4, "b1") "s_waitcnt lgkmcnt(0)\n\t1:"
#define D_CLOB "memory", "scc", "v208", "v209", "v210", "v211", "v212", "v213", "v214", "v215"
template <int P, int U> __device__ __forceinline__ void drain(bool on, unsigned wa, v32u (&W)[4]) {
  const unsigned b0 = wa + (U & 31) * 4096u, b1 = wa + ((U + 8) & 31) * 4096u;
  if constexpr (P == 0) asm volatile(D_ALL(64) : "+{v[64:95]}"(W[0]), "+{v[96:127]}"(W[1]) : [on] "s"(on ? 7 : 0), [b0] "v"(b0), [b1] "v"(b1) : D_CLOB);
  else asm volatile(D_ALL(128) : "+{v[128:159]}"(W[2]), "+{v[160:191]}"(W[3]) : [on] "s"(on ? 7 : 0), [b0] "v"(b0), [b1] "v"(b1) : D_CLOB);
}
template <int O> __device__ __forceinline__ v4u sub4(const v32u& v) { return __builtin_shufflevector(v, v, O, O + 1, O + 2, O + 3); }
template <int G, int E> __device__ __forceinline__ void vm1(v4u f0, v4u f1, const v32u (&W)[4], int g4, f32x4& r) {
  constexpr int k = 2 * G + (E >> 1), i = 2 * (E & 1) + (k >> 2), o = 8 * (k & 3);
  f32x4 d = mfma(f0, sub4<o>(W[i]), f32x4{0.f, 0.f, 0.f, 0.f});
  d = mfma(f1, sub4<o + 4>(W[i]), d);
  r[E] = (G == 0 || g4 == G) ? d[0] : r[E];
}
template <int G> __device__ __forceinline__ void vm4(v4u f0, v4u f1, const v32u (&W)[4], int g4, f32x4& r) {
  vm1<G, 0>(f0, f1, W, g4, r); vm1<G, 1>(f0, f1, W, g4, r); vm1<G, 2>(f0, f1, W, g4, r); vm1<G, 3>(f0, f1, W, g4, r);
}
__device__ __forceinline__ f32x4 vecmat(v4u f0, v4u f1, const v32u (&W)[4], int g4) {
  f32x4 r = {0.f, 0.f, 0.f, 0.f};
  vm4<0>(f0, f1, W, g4, r); vm4<1>(f0, f1, W, g4, r); vm4<2>(f0, f1, W, g4, r); vm4<3>(f0, f1, W, g4, r);
  return r;
}
__device__ __forceinline__ void red_gu(f32x4 a, float& G, float& U) {
  auto s0 = __builtin_amdgcn_permlane32_swap(asu(a[0]), asu(a[2]), false, false);
  auto s1 = __builtin_amdgcn_permlane32_swap(asu(a[1]), asu(a[3]), false, false);
  G = asf(s0[0]) + asf(s0[1]); U = asf(s1[0]) + asf(s1[1]);
  auto t0 = __builtin_amdgcn_permlane16_swap(asu(G), asu(G), false, false);
  auto t1 = __builtin_amdgcn_permlane16_swap(asu(U), asu(U), false, false);
  G = asf(t0[0]) + asf(t0[1]); U = asf(t1[0]) + asf(t1[1]);
  G += dpp<ROR8>(G); U += dpp<ROR8>(U);
}

typedef unsigned v16u __attribute__((ext_vector_type(16)));
__device__ __forceinline__ const void* mkptr(unsigned lo, unsigned hi) { return (const void*)(((unsigned long long)hi << 32) | lo); }

__global__ void __launch_bounds__(320, 2)
mk(const void*, const void*, const void*, const void*, const void*, const void*, const void*, const void*, void*, void*) {
  int b = blockIdx.x;
  v16u ka; v4u kb; unsigned long long t00, pc0, w1;
  {
    unsigned t, o;
    asm volatile("s_load_dwordx16 %0, %8, 0x0\n\ts_and_b32 %6, %5, 63\n\ts_cmp_lt_u32 %6, 36\n\ts_cselect_b32 %7, 56, 40\n\t"
                 "s_cmp_lt_u32 %6, 32\n\ts_cselect_b32 %7, 24, %7\n\ts_cmp_lt_u32 %6, 16\n\ts_cselect_b32 %7, 32, %7\n\t"
                 "s_load_dwordx2 %4, %8, %7\n\ts_load_dwordx4 %1, %8, 0x40\n\ts_memrealtime %2\n\ts_getpc_b64 %3\n\tmk_code_begin:"
                 : "=&s"(ka), "=&s"(kb), "=&s"(t00), "=&s"(pc0), "=&s"(w1), "+s"(b), "=&s"(t), "=&s"(o) : "s"(__builtin_amdgcn_kernarg_segment_ptr()) : "scc");
  }
  const int lane = threadIdx.x & 63;
  const int wv = __builtin_amdgcn_readfirstlane(threadIdx.x >> 6);
  const int g4 = lane >> 4, c16 = lane & 15;
  const int grp = b >> 6, j = b & 63;
  const bool isO = j < 16, isQ = j >= 16 && j < 32, isG = j >= 36;
  const int jq = (j - 2 * grp) & 15;
  const int jd = isQ ? jq + 4 : j - 32;
  const bool isD = (unsigned)jd < (isQ ? 14u : 4u);
  const int per = isG ? 28 : isO ? 16 : 14, loc = isG ? j - 36 : isO ? j : jd;
  const int idx = per * grp + loc;
  const int r16 = idx & 15, cb = idx >> 4;
  const int ukv = j < 32 ? 32 : 16;
  const int qc = 8 * (j & 7) + 2 * grp + ((j >> 3) & 1);

  asm volatile("s_cmp_eq_u32 %4, 0\n\ts_cbranch_scc1 1f\n\ts_waitcnt lgkmcnt(0)\n\t1:" : "+s"(ka), "+s"(kb), "+s"(t00), "+s"(w1) : "s"(wv) : "scc");
  if (__builtin_expect(wv == 0, 1)) {
    const unsigned stride = isG ? 7168u : 2048u;
    const unsigned s1 = isG ? 512u * 7168u : 16u * 2048u, s2 = isG ? 16u * 7168u : 32u * 2048u;
    const unsigned base0 = (unsigned)r16 * (isG ? 32u * 7168u : 64u * 2048u) + (unsigned)cb * (isG ? 1024u : 16u * 64u * 2048u);
    const unsigned base1 = base0 + (isG ? 0u : 1024u);
    unsigned o1[4];
    const unsigned st1 = isQ ? 8192u : stride, d1 = (isQ ? 65536u : 4u * stride) - 1024u, us1 = isQ ? 32u * 8192u : 256u;
    unsigned so1 = isQ ? (unsigned)qc * 128u : base0;
    {
      const unsigned sw = 16u * ((lane & (isQ ? 7 : 15)) ^ ((lane >> 3) & 6)), r = (lane >> (isQ ? 3 : 4)) * st1;
      o1[0] = r + sw; o1[1] = r + (sw ^ (isQ ? 0u : 128u)) + d1; o1[2] = o1[0] + 2 * d1; o1[3] = o1[1] + 2 * d1;
    }
    unsigned so1b = so1 + us1, so1c = so1 + 2 * us1, so1d = so1 + 3 * us1, wb1 = j < 32 ? 4096u * 2048u : 3584u * 2048u;
    asm volatile("s_waitcnt lgkmcnt(0)" : "+s"(ka), "+s"(kb), "+s"(t00), "+s"(w1), "+s"(so1), "+s"(so1b), "+s"(so1c), "+s"(so1d), "+s"(wb1)
                 : "v"(o1[0]), "v"(o1[1]), "v"(o1[2]), "v"(o1[3]));
    const __amdgpu_buffer_rsrc_t r1 = mkrs((const void*)w1, wb1);
    asm volatile("s_mov_b32 m0, 0\n\ts_nop 0\n\t" Q_LD("ra", "a", "s0") Q_NXT "s_nop 0\n\t" Q_LD("ra", "a", "s1") Q_NXT "s_nop 0\n\t" Q_LD("ra", "a", "s2") Q_NXT "s_nop 0\n\t" Q_LD("ra", "a", "s3")
                 :: [ra] "s"(r1), [a0] "v"(o1[0]), [a1] "v"(o1[1]), [a2] "v"(o1[2]), [a3] "v"(o1[3]),
                    [s0] "s"(so1), [s1] "s"(so1b), [s2] "s"(so1c), [s3] "s"(so1d) : "memory", "scc");
    const unsigned ct0 = (unsigned)t00;
    lds_st32(L_CTL + 4 * lane, 0u);
    int u = 4, freed = 0, full = 0;
    int bud = BUDGET;
    auto space = [&]() {
      if (freed < u - 28) {
        for (unsigned spins = 0;; ++spins) {
          const v4u p = lds_ld128_wait(L_PROG);
          freed = (int)__builtin_amdgcn_readfirstlane(min(min(p[0], p[1]), min(p[2], p[3])));
          if (freed >= u - 28) break;
          if (spins == 0) settle<0, 0>(u, full);
          if (bud <= 0) break;
          --bud;
          __builtin_amdgcn_s_sleep(1);
        }
      }
    };
    if (isQ) {
#pragma unroll 1
      while (u < 32) {
        so1 += 128u * 8192u;
        const unsigned t = quad<INFLQ>(r1, r1, o1, o1, so1, so1 + 32u * 8192u, so1 + 64u * 8192u, so1 + 96u * 8192u, u, full);
        if (u == 8) __builtin_amdgcn_s_barrier();
      }
      settle<INFLQ, INFL + 1>(u, full);
    } else {
      asm volatile("s_waitcnt lgkmcnt(0)" ::: "memory");
      __builtin_amdgcn_s_barrier();
#pragma unroll 1
      while (u < ukv) {
        const unsigned so = base0 + (u >> 4) * 1024u + ((u >> 2) & 1) * s1 + ((u >> 3) & 1) * s2;
        const unsigned t = quad<INFL0>(r1, r1, o1, o1, so, so + 256, so + 512, so + 768, u, full);
      }
    }
    unsigned chV[4];
#pragma unroll
    for (int i = 0; i < 4; ++i) chV[i] = 16 * (c16 ^ (8 * (i & 1) + 2 * g4));
    {
      unsigned oK[4], oV[4];
#pragma unroll
      for (int i = 0; i < 4; ++i) {
        oK[i] = g4 * 256 + 16 * (c16 ^ (4 * i + g4));
        oV[i] = g4 * 256 + chV[i];
      }
      const __amdgpu_buffer_rsrc_t rk = mkrs(mkptr(ka[2], ka[3]), 1u << 26), rv = mkrs(mkptr(ka[4], ka[5]), 1u << 26);
      unsigned so = ((unsigned)grp << 24) + (unsigned)(j < 32 ? 30 * j : 34 * j - 128) * 8192u;
      int d = INFL;
#pragma unroll 1
      for (; u < U_POST; so += 8192) {
        space();
        unsigned t;
#define KVQ(D) t = quad<D>(rk, rv, oK, oV, so, so + 4096, so, so + 4096, u, full)
        if (!CTL) { KVQ(INFL); }
        else {
          if (d <= CT_LO + 2) { if (d <= CT_LO) KVQ(CT_LO); else if (d == CT_LO + 1) KVQ(CT_LO + 1); else KVQ(CT_LO + 2); }
          else { if (d == CT_LO + 3) KVQ(CT_LO + 3); else if (d == CT_LO + 4) KVQ(CT_LO + 4); else KVQ(CT_LO + 5); }
          const int lag = (int)(t - ct0) - (CT_A + ((full * ((CT_E - CT_A) * 256 / U_POST)) >> 8));
          d = min(INFL + ((lag * (4096 / CT_G) + 2048) >> 12), CT_HI);
        }
        if (u == 36) settle<4, 4>(u, full);
      }
    }
    constexpr int DMAX = CTL ? CT_LO + 5 : INFL;
    if (isG || isD) {
      const __amdgpu_buffer_rsrc_t r = mkrs(mkptr(isG ? ka[12] : ka[14], isG ? ka[13] : ka[15]), 3584u * 2048u);
      unsigned oW[4];
#pragma unroll
      for (int i = 0; i < 4; ++i) oW[i] = (4 * i + g4) * stride + chV[i] - 1024 * i;
      settle<DMAX, INFLD + 1>(u, full);
      if (!isQ) settle<INFLD, INFLP + 1>(u, full);
      const int end = U_POST + (isQ ? 32 : 16);
#pragma unroll 1
      while (u < end) {
        space();
        const int i = u - U_POST;
        const unsigned so = base1 - (i >> 4) * 1024u + ((i >> 2) & 1) * s1 + ((i >> 3) & 1) * s2;
        unsigned t;
        if (isQ) t = quad<INFLD>(r, r, oW, oW, so, so + 256, so + 512, so + 768, u, full);
        else t = quad<INFLP>(r, r, oW, oW, so, so + 256, so + 512, so + 768, u, full);
      }
    }
    settle<(DMAX > INFLD ? DMAX : INFLD), 0>(u, full);
    return;
  }

  const int w = wv - 1;
  const GAS unsigned short* x = (const GAS unsigned short*)mkptr(ka[0], ka[1]);
  char* ws = (char*)mkptr(kb[2], kb[3]);
  const __amdgpu_buffer_rsrc_t rs = mkrs(ws, WS_END);
  v4u xa = *(const GAS v4u*)(x + 16 * lane), xb = *(const GAS v4u*)(x + 16 * lane + 8);
  unsigned tprev;
  asm volatile("s_load_dword %0, %1, 0x0" : "=&s"(tprev) : "s"(ws));
  if (!isQ) {
    const int rb = 4 * (j >> 3) + grp + (isO ? 8 : 0);
    if (PF && w == 0) asm volatile("global_load_dword v234, %0, %1 nt" ::"v"((32 * rb + (lane >> 1)) * 8192 + (b & 7) * 1024 + 384 + (lane & 1) * 512), "s"(mkptr(ka[6], ka[7])) : "v234");
    if (CPF && w == 1) asm volatile("v_min_u32 v233, mk_code_end-mk_code_begin, %0\n\tglobal_load_dword v234, v233, %1" ::"v"((b & 24) * 512 + (b & 64) * 256 + lane * 64), "s"(pc0) : "v233", "v234");
  }
  __builtin_amdgcn_s_barrier();
  asm volatile("s_waitcnt lgkmcnt(0)" : "+s"(tprev));
  const Tags T = mktags(tprev + 1u);
  int bud = BUDGET;
  int seen = 0;
  const int sub = lane >> 2, p4 = lane & 3;
  const unsigned trrow = 256u * sub + 8u * p4;
  const unsigned hcol = (g4 & 1) * 512 + 32 * r16 + (g4 >> 1) * 16 + 4 * w + p4;
  float xres = 0.f;
  if (isQ) {
    float s = 0.f;
#pragma unroll
    for (int e = 0; e < 4; ++e) s += (lo16(xa[e]) + hi16(xa[e])) + (lo16(xb[e]) + hi16(xb[e]));
    const float mean = xrow_sum(row_sum(s)) * (1.f / 1024.f);
    float q = 0.f;
#pragma unroll
    for (int e = 0; e < 4; ++e) {
      const float d0 = lo16(xa[e]) - mean, d1 = hi16(xa[e]) - mean, d2 = lo16(xb[e]) - mean, d3 = hi16(xb[e]) - mean;
      q += (d0 * d0 + d1 * d1) + (d2 * d2 + d3 * d3);
    }
    const float var = xrow_sum(row_sum(q)) * (1.f / 1024.f);
    const float mb = rbf(mean), rsd = __builtin_amdgcn_rcpf(rbf(__builtin_amdgcn_sqrtf(rbf(rbf(var) + 1e-5f))));
    auto nrm = [&](unsigned p) { return pk2(rbf(lo16(p) - mb) * rsd, rbf(hi16(p) - mb) * rsd); };
    const unsigned xl = L_XN + (lane >> 1) * 64 + (lane & 1) * 8;
    lds_st64(xl, v2u{nrm(xa[0]), nrm(xa[1])});
    lds_st64(xl + 16, v2u{nrm(xa[2]), nrm(xa[3])});
    lds_st64(xl + 32, v2u{nrm(xb[0]), nrm(xb[1])});
    lds_st64(xl + 48, v2u{nrm(xb[2]), nrm(xb[3])});
    asm volatile("s_waitcnt lgkmcnt(0)" ::: "memory");
    unsigned a = sub * 128u + 16u * ((2 * w + (p4 >> 1)) ^ (sub & 6)) + 8u * (p4 & 1), xo = L_XN + g4 * 16;
    f32x4 ac[4] = {};
#pragma unroll 1
    for (int u0 = 0; u0 < 32; u0 += 4, a += 16384, xo += 256) {
      v4u xf[4], wf[4];
#pragma unroll
      for (int i = 0; i < 4; ++i) xf[i] = lds128(xo + 64 * i);
      wait_full(u0 + 4, seen, bud);
#pragma unroll
      for (int i = 0; i < 4; ++i) wf[i] = ldstr2(a + 4096 * i, a + 4096 * i + 2048);
      lds_st32(L_PROG + 4 * w, (unsigned)(u0 + 4));
#pragma unroll
      for (int i = 0; i < 4; ++i) ac[i] = mfma(wf[i], xf[i], ac[i]);
    }
    const f32x4 acc = (ac[0] + ac[1]) + (ac[2] + ac[3]);
    v4u r;
#pragma unroll
    for (int e = 0; e < 4; ++e) r[e] = (pk2(acc[e], acc[e]) << 16) | T.h[e];
    gst(rs, WS_QF + c16 * 16384 + qc * 256 + w * 64 + g4 * 16, r);
  } else {
    xres = lo16(x[hcol]);
    if (isG && w == cb) gst(rs, WS_PD + (r16 * 4 + w) * 4096u + 3584u + (lane & 31) * 16, tag4(f32x4{0.f, 0.f, 0.f, 0.f}, T));
  }
  asm volatile("s_waitcnt vmcnt(0)" ::: "memory");
  const unsigned wa = w * 4096u + trrow + 32u * (sub & 7);
  v32u W[4];
  asm volatile("" : "={v[64:95]}"(W[0]), "={v[96:127]}"(W[1]), "={v[128:159]}"(W[2]), "={v[160:191]}"(W[3]));
  wait_full(16, seen, bud);
  drain<0, 0>(!isQ, wa, W);
  lds_st32(L_PROG + 4 * w, isQ ? 32u : 16u);
  wait_full(ukv, seen, bud);
  drain<1, 16>(isO, wa, W);
  lds_st32(L_PROG + 4 * w, (unsigned)ukv);
  wait_full(isQ ? QATQ : QAT, seen, bud);
  v4u qf[4];
  {
    v4u v[1]; float d0, d1;
    gather<1, false, M16, 4>(rs, WS_QF + ((b & 15) * 32 + grp * 8 + 2 * w) * 512 + lane * 16, 0, T, v, d0, d1, bud);
    lds_st64(L_Q + w * 512 + lane * 8, v2u{(v[0][0] >> 16) | v[0][1], (v[0][2] >> 16) | v[0][3]});
    lds_st32(L_XF + 4 * w, 1u);
    exch(L_XF, L_Q + (c16 & 7) * 256 + g4 * 16, qf[0], qf[1], bud);
    qf[2] = lds128(L_Q + (c16 & 7) * 256 + g4 * 16 + 128);
    qf[3] = lds128(L_Q + (c16 & 7) * 256 + g4 * 16 + 192);
  }

  {
    unsigned ka[4], va[2];
#pragma unroll
    for (int u = 0; u < 4; ++u) ka[u] = 256u * c16 + 16u * ((4 * u + g4) ^ c16);
#pragma unroll
    for (int i = 0; i < 2; ++i) va[i] = 8192u + trrow + 32u * ((2 * w + i) ^ (sub & 7));
    const bool up = c16 >= 8;
    const float LOG2E = 1.4426950408889634f, scale = 0.08838834764831845f;
    float m = -INFINITY, l = 0.f;
    f32x4 acc[2] = {f32x4{0.f, 0.f, 0.f, 0.f}, f32x4{0.f, 0.f, 0.f, 0.f}};
    const unsigned aPA = WS_PA + (((grp * 8 + (c16 & 7)) * 8 + 2 * w + (c16 >> 3)) * 64 + j) * 64 + g4 * 16;
    const unsigned aML = 8 * w + lane < 8 ? WS_ML + (b * 8 + lane) * 16 : OOB;
#pragma unroll 1
    for (int u0 = ukv; u0 < U_POST; u0 += 4) {
      const unsigned tb = (unsigned)(u0 & 31) * 4096u;
      wait_full(u0 + 2, seen, bud);
      if (u0 == U_POST - 4 * NE) {
        if (w == 0) {
          const unsigned eta = (unsigned)__builtin_amdgcn_s_memrealtime() + KE;
          __builtin_amdgcn_raw_buffer_store_b32((eta << 16) | ((b & 2) ? (b & 1) ? T.h[3] : T.h[2] : (b & 1) ? T.h[1] : T.h[0]), rs, (int)(lane < 16 ? WS_ET + lane * 1024 + b * 4 : OOB), 0, 16);
        }
        if (CPF2 && w == 1) asm volatile("v_min_u32 v233, mk_code_end-mk_code_begin, %0\n\tglobal_load_dword v234, v233, %1" ::"v"((b & 24) * 512 + (b & 64) * 256 + lane * 64), "s"(pc0) : "v233", "v234");
      }
      f32x4 s0 = {0.f, 0.f, 0.f, 0.f}, s1 = {0.f, 0.f, 0.f, 0.f};
#pragma unroll
      for (int u = 0; u < 4; ++u) {
        s0 = mfma(lds128(tb + ka[u]), qf[u], s0);
        s1 = mfma(lds128(tb + ka[u] + 4096), qf[u], s1);
      }
      float s[4];
#pragma unroll
      for (int r = 0; r < 4; ++r) s[r] = rbf(rbf(up ? s1[r] : s0[r]) * scale);
      const float mn = fmaxf(m, xrow_max(fmaxf(fmaxf(s[0], s[1]), fmaxf(s[2], s[3]))));
      const float al = __builtin_amdgcn_exp2f((m - mn) * LOG2E);
      m = mn;
      float p[4];
#pragma unroll
      for (int r = 0; r < 4; ++r) p[r] = __builtin_amdgcn_exp2f((s[r] - mn) * LOG2E);
      l = l * al + ((p[0] + p[1]) + (p[2] + p[3]));
      const unsigned p01 = pk2(p[0], p[1]), p23 = pk2(p[2], p[3]);
      v4u pb = up ? v4u{0u, 0u, p01, p23} : v4u{p01, p23, 0u, 0u};
      acc[0] *= al; acc[1] *= al;
      asm volatile("" : "+v"(pb), "+v"(acc[0]), "+v"(acc[1]), "+v"(l));
      wait_full(u0 + 4, seen, bud);
      const v4u v0 = ldstr2(tb + va[0], tb + va[0] + 4096), v1 = ldstr2(tb + va[1], tb + va[1] + 4096);
      acc[0] = mfma(v0, pb, acc[0]);
      acc[1] = mfma(v1, pb, acc[1]);
      asm volatile("s_waitcnt lgkmcnt(0)" ::: "memory");
      lds_st32(L_PROG + 4 * w, (unsigned)(u0 + 4));
    }
    const float M = fmaxf(m, dpp<ROR8>(m));
    const float f = __builtin_amdgcn_exp2f((m - M) * LOG2E);
    float lt = xrow_sum(l) * f;
    lt += dpp<ROR8>(lt);
    f32x4 o;
#pragma unroll
    for (int e = 0; e < 4; ++e) {
      const float a0 = acc[0][e] * f, a1 = acc[1][e] * f;
      o[e] = (up ? a1 : a0) + dpp<ROR8>(up ? a0 : a1);
    }
    gst(rs, aPA, tag4(o, T));
    gst(rs, aML, v4u{asu(M), T.t, asu(lt), T.y});
  }

  if (isO || isG || isD) {
    const v4u ones = {0x3F803F80u, 0x3F803F80u, 0x3F803F80u, 0x3F803F80u};
    const unsigned aPC = (((8 * cb + 2 * w) * 4 + 2 * g4 + (c16 >> 3)) * 16 + r16) * 128 + (c16 & 7) * 16;
    const unsigned aOut = isG ? WS_PC + aPC : (isO ? WS_PB : WS_PD) + (((4 * w + g4) * 4 + (c16 >> 2)) * 64 + idx) * 64 + (c16 & 3) * 16;
    float h = 0.f;
    wait_full(isO ? 0 : U_POST + 16, seen, bud);
    drain<1, U_POST>(!isO, wa, W);
    wait_full(isQ ? U_POST + 32 : 0, seen, bud);
    drain<0, U_POST + 16>(isQ, wa, W);
    if (isG) gst(rs, WS_CS + aPC, tag4(vecmat(ones, ones, W, g4), T));
    if (w == 0) {
      v4u v[1]; float d0, d1;
      gather<1, false, M16, 8>(rs, WS_ET + (b & 15) * 1024 + (isO ? grp * 256 + c16 * 16 : lane * 16), 0, T, v, d0, d1, bud);
      const unsigned now = (unsigned)__builtin_amdgcn_s_memrealtime(), n16 = now << 16;
      const int dm = max(max((int)(v[0][0] - n16), (int)(v[0][1] - n16)), max((int)(v[0][2] - n16), (int)(v[0][3] - n16))) >> 16;
      const unsigned E = (now + (unsigned)min((int)xrow_max(row_max((float)dm)), 300)) | 1u;
      lds_st32(L_ETA, E);
    }
#define NF0
#define NF1(sh)
    if (isO) {
      v4u v[4]; float ms, ls;
      unsigned a0 = WS_PA + (idx * 4 + w) * 4096u + lane * 16, a1 = WS_ML + ((grp * 64 + 16 * p4 + sub) * 8 + (j >> 1)) * 16;
      asm volatile("" : "+v"(a0), "+v"(a1));
      gate(DA, bud);
      NF0;
      gather<4, true, M10, GAPA>(rs, a0, a1, T, v, ms, ls, bud);
      NF1(0);
      const float LOG2E = 1.4426950408889634f;
      const float M = xrow_max(row_max(ms));
      const float f = __builtin_amdgcn_exp2f((ms - M) * LOG2E);
      const float L = xrow_sum(row_sum(f * ls));
      f32x4 a = asf4(v[0]) * dpp<0x00>(f);
      a += asf4(v[1]) * dpp<0x55>(f);
      a += asf4(v[2]) * dpp<0xAA>(f);
      a += asf4(v[3]) * dpp<0xFF>(f);
      const float o = red_scatter(a) * __builtin_amdgcn_rcpf(L);
      lds_st16<0x000F000Fu>(L_VEC + (w >> 1) * 64 + p4 * 16 + (w & 1) * 8 + g4 * 2, pk2(o, o));
    } else {
      if (isG) {
        v4u v[4]; float d0, d1;
        unsigned a0 = WS_PB + (r16 * 4 + w) * 4096u + lane * 16;
        asm volatile("" : "+v"(a0));
        gate(DB, bud);
        NF0;
        gather<4, false, M10, GAPB>(rs, a0, 0, T, v, d0, d1, bud);
        NF1(0);
        const f32x4 a = (asf4(v[0]) + asf4(v[1])) + (asf4(v[2]) + asf4(v[3]));
        h = rbf(xres + rbf(red_scatter(a)));
        lds_st16<0x000F000Fu>(L_VEC + (g4 >> 1) * 64 + w * 16 + (g4 & 1) * 8 + p4 * 2, asu(h) >> 16);
      } else {
        const unsigned gPC = (idx * 4 + w) * 2048 + lane * 16;
        float Cg, Cu;
        {
          v4u v[2]; float d0, d1;
          gate(DS, bud);
          gather<2, false, M10, 4>(rs, WS_CS + gPC, 0, T, v, d0, d1, bud);
          red_gu(asf4(v[0]) + asf4(v[1]), Cg, Cu);
        }
        v4u v[2]; float sv0, sv2;
        unsigned a0 = WS_PC + gPC, a1 = WS_ST + (idx * 16 + c16) * 64 + w * 16;
        asm volatile("" : "+v"(a0), "+v"(a1), "+v"(Cg), "+v"(Cu));
        gate(DC, bud);
        NF0;
        gather<2, true, M10, GAPB>(rs, a0, a1, T, v, sv0, sv2, bud);
        NF1(0);
        const float mean = row_sum(sv0) * (1.f / 1024.f);
        const float var = fmaxf(row_sum(sv2) * (1.f / 1024.f) - mean * mean, 0.f);
        const float mb = rbf(mean), sd = rbf(__builtin_amdgcn_sqrtf(rbf(rbf(var) + 1e-5f)));
        const float rsd = __builtin_amdgcn_rcpf(sd);
        float G, U;
        red_gu(asf4(v[0]) + asf4(v[1]), G, U);
        const float g = rbf((G - mb * Cg) * rsd), u = rbf((U - mb * Cu) * rsd);
        const float y = 0.7978845608028654f * (g + 0.044715f * g * g * g);
        const float ge = rbf(g * __builtin_amdgcn_rcpf(1.f + __builtin_amdgcn_exp2f(y * (-2.f * 1.4426950408889634f))));
        const float act = ge * u;
        lds_st16<0xFFu>(L_VEC + (w >> 1) * 64 + (2 * (w & 1) + ((lane >> 2) & 1)) * 16 + (lane >> 5) * 8 + p4 * 2, pk2(act, act));
      }
    }
    lds_st32(L_XF + 16 + 4 * w, 1u);
    v4u f0, f1;
    exch(L_XF + 16, L_VEC + g4 * 16, f0, f1, bud);
    if (isG) {
      f32x4 s2 = mfma(f0, f0, f32x4{0.f, 0.f, 0.f, 0.f}), s1 = mfma(f0, ones, f32x4{0.f, 0.f, 0.f, 0.f});
      s2 = mfma(f1, f1, s2); s1 = mfma(f1, ones, s1);
      gst(rs, lane < 8 ? WS_ST + ((8 * cb + 2 * w + (lane >> 2)) * 16 + r16) * 64 + p4 * 16 : OOB, v4u{asu(s1[0]), T.t, asu(s2[0]), T.y});
    }
    gst(rs, aOut, tag4(vecmat(f0, f1, W, g4), T));
    if (isG) {
      if (idx == 111 && w == 0) __builtin_amdgcn_raw_buffer_store_b32(T.t, rs, lane ? (int)OOB : 0, 0, 16);
      if (w == cb) {
        v4u v[4]; float d0, d1;
        unsigned a0 = WS_PD + (r16 * 4 + w) * 4096u + lane * 16;
        asm volatile("" : "+v"(a0));
        gate(DD, bud);
        NF0;
        gather<4, false, M10, GAPB>(rs, a0, 0, T, v, d0, d1, bud);
        NF1(8);
        const f32x4 a = (asf4(v[0]) + asf4(v[1])) + (asf4(v[2]) + asf4(v[3]));
        const float res = h + rbf(red_scatter(a));
        __builtin_amdgcn_raw_buffer_store_b16((unsigned short)pk2(res, res), mkrs(mkptr(kb[0], kb[1]), 2048), (int)(c16 < 4 ? 2 * hcol : OOB), 0, 16);
        asm volatile("mk_code_end:");
      }
    }
  }
}

extern "C" void kernel_launch(void* const* d_in, const int* in_sizes, int n_in, void* d_out, int out_size, void* d_ws,
                              size_t ws_size, hipStream_t stream) {
  (void)in_sizes; (void)n_in; (void)out_size; (void)ws_size;
  (void)hipFuncSetAttribute((const void*)mk, hipFuncAttributeMaxDynamicSharedMemorySize, (int)LDS_BYTES);
  mk<<<256, 320, LDS_BYTES, stream>>>(d_in[0], d_in[1], d_in[2], d_in[3], d_in[4], d_in[6], d_in[7], d_in[8], d_out, d_ws);
}
```

```cpp
#include <hip/hip_runtime.h>
#include <stdint.h>

typedef unsigned v4u __attribute__((ext_vector_type(4)));
typedef unsigned v2u __attribute__((ext_vector_type(2)));
typedef float f32x4 __attribute__((ext_vector_type(4)));
typedef float f32x2 __attribute__((ext_vector_type(2)));
typedef __bf16 bf16x8_t __attribute__((ext_vector_type(8)));
typedef __bf16 bf16x2_t __attribute__((ext_vector_type(2)));
typedef short s16x4 __attribute__((ext_vector_type(4)));
#define LAS __attribute__((address_space(3)))
#define GAS __attribute__((address_space(1)))

constexpr unsigned WS_ET = 0x004000;
constexpr unsigned WS_ML = 0x008000;
constexpr unsigned WS_ST = 0x010000;
constexpr unsigned WS_QF = 0x040000;
constexpr unsigned WS_PB = 0x080000;
constexpr unsigned WS_PD = 0x0C0000;
constexpr unsigned WS_PA = 0x100000;
constexpr unsigned WS_PC = 0x200000;
constexpr unsigned WS_CS = 0x280000;
constexpr unsigned WS_END = 0x300000;
constexpr unsigned OOB = 0x10000000;

constexpr unsigned L_CTL = 131072;
constexpr unsigned L_FULL = L_CTL;
constexpr unsigned L_PROG = L_CTL + 16;
constexpr unsigned L_XF = L_CTL + 32;
constexpr unsigned L_ETA = L_CTL + 64;
constexpr unsigned L_VEC = L_CTL + 256;
constexpr unsigned L_XN = L_CTL + 1024;
constexpr unsigned L_Q = L_CTL + 3072;
constexpr unsigned LDS_BYTES = L_CTL + 5120;

#define INFL 4
#define INFL0 4
#define INFLQ 14
#define INFLP 4
#define INFLD 6
#define QAT 28
#define PF 3
#define QATQ 40
#define CPF 1
#define NE 3
#define KE 160
#define DA 60
#define DB 240
#define DC 370
#define DD 495
#define DS DB
#define CPF2 1
#define GAPA 0
#define GAPB 0
#define CTL 1
#define CT_A 200
#define CT_E 2230
#define CT_G 50
#define CT_LO 3
#define CT_HI 8
constexpr int U_POST = 152;
constexpr int BUDGET = 1 << 20;

__device__ __forceinline__ float asf(unsigned u) { return __builtin_bit_cast(float, u); }
__device__ __forceinline__ unsigned asu(float f) { return __builtin_bit_cast(unsigned, f); }
__device__ __forceinline__ unsigned pk2(float lo, float hi) {
  return __builtin_bit_cast(unsigned, __builtin_convertvector(f32x2{lo, hi}, bf16x2_t));
}
__device__ __forceinline__ float rbf(float f) { return asf(pk2(f, f) << 16); }
__device__ __forceinline__ float lo16(unsigned u) { return asf(u << 16); }
__device__ __forceinline__ float hi16(unsigned u) { return asf(u & 0xFFFF0000u); }
__device__ __forceinline__ f32x4 mfma(v4u a, v4u b, f32x4 c) {
  return __builtin_amdgcn_mfma_f32_16x16x32_bf16(__builtin_bit_cast(bf16x8_t, a), __builtin_bit_cast(bf16x8_t, b), c, 0, 0, 0);
}
template <int CTRL> __device__ __forceinline__ float dpp(float x) {
  return asf((unsigned)__builtin_amdgcn_mov_dpp((int)asu(x), CTRL, 0xf, 0xf, true));
}
constexpr int XOR1 = 0xB1, XOR2 = 0x4E, ROR4 = 0x124, ROR8 = 0x128;
__device__ __forceinline__ float xrow_max(float x) {
  auto s = __builtin_amdgcn_permlane16_swap(asu(x), asu(x), false, false);
  x = fmaxf(asf(s[0]), asf(s[1]));
  auto t = __builtin_amdgcn_permlane32_swap(asu(x), asu(x), false, false);
  return fmaxf(asf(t[0]), asf(t[1]));
}
__device__ __forceinline__ float xrow_sum(float x) {
  auto s = __builtin_amdgcn_permlane16_swap(asu(x), asu(x), false, false);
  x = asf(s[0]) + asf(s[1]);
  auto t = __builtin_amdgcn_permlane32_swap(asu(x), asu(x), false, false);
  return asf(t[0]) + asf(t[1]);
}
__device__ __forceinline__ float row_sum(float x) {
  x += dpp<XOR1>(x); x += dpp<XOR2>(x); x += dpp<ROR4>(x); x += dpp<ROR8>(x); return x;
}
__device__ __forceinline__ float row_max(float x) {
  x = fmaxf(x, dpp<XOR1>(x)); x = fmaxf(x, dpp<XOR2>(x)); x = fmaxf(x, dpp<ROR4>(x)); x = fmaxf(x, dpp<ROR8>(x)); return x;
}
__device__ __forceinline__ float red_scatter(f32x4 a) {
  auto s0 = __builtin_amdgcn_permlane32_swap(asu(a[0]), asu(a[2]), false, false);
  auto s1 = __builtin_amdgcn_permlane32_swap(asu(a[1]), asu(a[3]), false, false);
  const float b0 = asf(s0[0]) + asf(s0[1]), b1 = asf(s1[0]) + asf(s1[1]);
  auto t = __builtin_amdgcn_permlane16_swap(asu(b0), asu(b1), false, false);
  float c = asf(t[0]) + asf(t[1]);
  c += dpp<ROR4>(c); c += dpp<ROR8>(c);
  return c;
}
__device__ __forceinline__ void lds_st32(unsigned a, unsigned v) { asm volatile("ds_write_b32 %0, %1" ::"v"(a), "v"(v) : "memory"); }
template <unsigned M> __device__ __forceinline__ void lds_st16(unsigned a, unsigned v) {
  asm volatile("s_mov_b32 exec_lo, %2\n\ts_mov_b32 exec_hi, %2\n\tds_write_b16 %0, %1\n\ts_mov_b64 exec, -1" ::"v"(a), "v"(v), "n"(M) : "memory");
}
__device__ __forceinline__ void lds_st64(unsigned a, v2u v) { asm volatile("ds_write_b64 %0, %1" ::"v"(a), "v"(v) : "memory"); }
__device__ __forceinline__ v4u lds_ld128_wait(unsigned a) {
  v4u r; asm volatile("ds_read_b128 %0, %1\n\ts_waitcnt lgkmcnt(0)" : "=v"(r) : "v"(a) : "memory"); return r;
}
__device__ __forceinline__ v4u lds128(unsigned a) { return *(const LAS v4u*)(uintptr_t)a; }
__device__ __forceinline__ v2u ldstr(unsigned a) {
  return __builtin_bit_cast(v2u, __builtin_amdgcn_ds_read_tr16_b64_v4i16((LAS s16x4*)(uintptr_t)a));
}
__device__ __forceinline__ v4u ldstr2(unsigned a0, unsigned a1) {
  const v2u x = ldstr(a0), y = ldstr(a1); return v4u{x[0], x[1], y[0], y[1]};
}
__device__ __forceinline__ __amdgpu_buffer_rsrc_t mkrs(const void* p, unsigned bytes) {
  return __builtin_amdgcn_make_buffer_rsrc((void*)p, (short)0, (int)bytes, 0x00020000);
}
__device__ __forceinline__ void gst(__amdgpu_buffer_rsrc_t rs, unsigned vo, v4u v) { __builtin_amdgcn_raw_buffer_store_b128(v, rs, (int)vo, 0, 16); }
struct Tags { unsigned t, y, h[4]; };
__device__ __forceinline__ Tags mktags(unsigned T) {
  Tags g; g.t = T;
  const unsigned x = T ^ (T >> 16);
  const unsigned c[4] = {0x9E3779B1u, 0x85EBCA77u, 0xC2B2AE3Du, 0x27D4EB2Fu};
  g.y = x * 0x7FEB352Du;
#pragma unroll
  for (int e = 0; e < 4; ++e) g.h[e] = (((x * c[e]) >> 16) & 0xFFFEu) | (T & 1u);
  return g;
}
constexpr unsigned M10 = 0x3FFu, M16 = 0xFFFFu;
__device__ __forceinline__ v4u tag4(f32x4 a, const Tags& T) {
  v4u r;
#pragma unroll
  for (int e = 0; e < 4; ++e) r[e] = ((asu(a[e]) + 0x200u) & ~M10) | (T.h[e] & M10);
  return r;
}

__device__ __forceinline__ void wait_full(int need, int& seen, int& bud) {
  unsigned t;
  asm volatile("s_cmp_ge_i32 %[seen], %[need]\n\ts_cbranch_scc1 2f\n\t"
               "1:\n\tds_read_b32 %[t], %[a]\n\ts_waitcnt lgkmcnt(0)\n\tv_readfirstlane_b32 %[seen], %[t]\n\t"
               "s_cmp_ge_i32 %[seen], %[need]\n\ts_cbranch_scc1 2f\n\t"
               "s_sub_u32 %[cnt], %[cnt], 1\n\ts_cmp_gt_i32 %[cnt], 0\n\ts_cbranch_scc0 2f\n\ts_sleep 1\n\ts_branch 1b\n\t2:"
               : [t] "=&v"(t), [seen] "+s"(seen), [cnt] "+s"(bud) : [need] "s"(need), [a] "v"(L_FULL) : "memory", "scc");
}
__device__ __forceinline__ void gate(int d, int& bud) {
  unsigned t; int e, n;
  asm volatile("1:\n\tds_read_b32 %[t], %[a]\n\ts_memrealtime vcc\n\ts_waitcnt lgkmcnt(0)\n\tv_readfirstlane_b32 %[e], %[t]\n\t"
               "s_sub_u32 %[cnt], %[cnt], 1\n\ts_cmp_gt_i32 %[cnt], 0\n\ts_cbranch_scc0 3f\n\t"
               "s_cmp_eq_u32 %[e], 0\n\ts_cbranch_scc1 1b\n\t"
               "s_add_u32 %[e], %[e], %[d]\n\t"
               "2:\n\ts_sub_u32 %[n], vcc_lo, %[e]\n\ts_cmp_ge_i32 %[n], 0\n\ts_cbranch_scc1 3f\n\t"
               "s_memrealtime vcc\n\ts_sub_u32 %[cnt], %[cnt], 1\n\ts_cmp_gt_i32 %[cnt], 0\n\ts_waitcnt lgkmcnt(0)\n\ts_cbranch_scc1 2b\n\t3:"
               : [t] "=&v"(t), [e] "=&s"(e), [n] "=&s"(n), [cnt] "+s"(bud) : [a] "v"(L_ETA), [d] "s"(d) : "memory", "scc", "vcc");
}
__device__ __forceinline__ void exch(unsigned flags, unsigned a, v4u& f0, v4u& f1, int& bud) {
  asm volatile("1:\n\tds_read_b128 v[228:231], %[fl]\n\tds_read_b128 %[f0], %[a]\n\tds_read_b128 %[f1], %[a] offset:64\n\t"
               "s_waitcnt lgkmcnt(2)\n\t"
               "v_and_b32 v228, v228, v229\n\tv_bitop3_b32 v228, v228, v230, v231 bitop3:0x80\n\t"
               "v_cmp_eq_u32 vcc, 0, v228\n\ts_waitcnt lgkmcnt(0)\n\ts_cbranch_vccz 2f\n\t"
               "s_sub_u32 %[cnt], %[cnt], 1\n\ts_cmp_gt_i32 %[cnt], 0\n\ts_cbranch_scc1 1b\n\t2:"
               : [f0] "=&v"(f0), [f1] "=&v"(f1), [cnt] "+s"(bud) : [fl] "v"(flags), [a] "v"(a)
               : "memory", "vcc", "scc", "v228", "v229", "v230", "v231");
}
#define G_LD(R, OFF) "buffer_load_dwordx4 " R ", %[vo], %[rs], 0 offen offset:" #OFF " sc1\n\t"
#define G_LDX "buffer_load_dwordx4 v[224:227], %[xo], %[rs], 0 offen sc1\n\t"
#define G_CHK(W, A0, A1, A2, A3, R0, R1, R2, R3, B)                                                             \
  "s_waitcnt vmcnt(" #W ")\n\t"                                                                                 \
  "v_xor_b32 %[" #A0 "], %[tm0], " R0 "\n\tv_xor_b32 %[" #A1 "], %[tm1], " R1 "\n\t"                                 \
  "v_xor_b32 %[" #A2 "], %[tm2], " R2 "\n\tv_xor_b32 %[" #A3 "], %[tm3], " R3 "\n\t"                                 \
  "v_or3_b32 v233, %[" #A0 "], %[" #A1 "], %[" #A2 "]\n\t"                                                        \
  "v_bitop3_b32 " B ", v233, %[mk], %[" #A3 "] bitop3:0xc8\n\t"
#define G_CHKX(W) "s_waitcnt vmcnt(" #W ")\n\tv_xor_b32 v232, %[t], v225\n\tv_bitop3_b32 v232, v232, v227, %[ty] bitop3:0xf6\n\tv_mov_b32 %[x0], v224\n\tv_mov_b32 %[x2], v226\n\t"
#define G_TEST(B) "v_cmp_ne_u32 vcc, 0, " B "\n\ts_cbranch_vccz 2f\n\ts_sub_u32 %[cnt], %[cnt], 1\n\ts_cmp_gt_i32 %[cnt], 0\n\ts_cbranch_scc0 2f\n\ts_sleep %[gap]\n\t"
#define G_RE(B, LD) "v_cmp_ne_u32 vcc, 0, " B "\n\ts_cbranch_vccz 3f\n\ts_mov_b64 exec, vcc\n\t" LD "s_mov_b64 exec, -1\n\t3:\n\t"
#define G_END "s_waitcnt vmcnt(0)\n\ts_branch 1b\n\t2:"
#define G_C0(W) G_CHK(W, a0, a1, a2, a3, "v208", "v209", "v210", "v211", "v228")
#define G_C1(W) G_CHK(W, b0, b1, b2, b3, "v212", "v213", "v214", "v215", "v229")
#define G_C2(W) G_CHK(W, c0, c1, c2, c3, "v216", "v217", "v218", "v219", "v230")
#define G_C3(W) G_CHK(W, d0, d1, d2, d3, "v220", "v221", "v222", "v223", "v231")
#define G_L0 G_LD("v[208:211]", 0)
#define G_L1 G_LD("v[212:215]", 1024)
#define G_L2 G_LD("v[216:219]", 2048)
#define G_L3 G_LD("v[220:223]", 3072)
#define G_OA [a0] "=&v"(o[0]), [a1] "=&v"(o[1]), [a2] "=&v"(o[2]), [a3] "=&v"(o[3])
#define G_OB [b0] "=&v"(o[4]), [b1] "=&v"(o[5]), [b2] "=&v"(o[6]), [b3] "=&v"(o[7])
#define G_OC [c0] "=&v"(o[8]), [c1] "=&v"(o[9]), [c2] "=&v"(o[10]), [c3] "=&v"(o[11]), \
             [d0] "=&v"(o[12]), [d1] "=&v"(o[13]), [d2] "=&v"(o[14]), [d3] "=&v"(o[15])
#define G_OX [x0] "=&v"(x0), [x2] "=&v"(x2)
#define G_IN [vo] "v"(vo), [rs] "s"(rs), [tm0] "s"(T.h[0] & MASK), [tm1] "s"(T.h[1] & MASK), [tm2] "s"(T.h[2] & MASK), [tm3] "s"(T.h[3] & MASK), [mk] "s"(MASK), [gap] "n"(GAP)
#define G_INX [xo] "v"(xo), [t] "s"(T.t), [ty] "s"(T.y)
#define G_CLOB "memory", "vcc", "scc", "v208", "v209", "v210", "v211", "v212", "v213", "v214", "v215", "v216", "v217", "v218", "v219", \
               "v220", "v221", "v222", "v223", "v224", "v225", "v226", "v227", "v228", "v229", "v230", "v231", "v232", "v233"
template <int N, bool X, unsigned MASK, int GAP>
__device__ __forceinline__ void gather(__amdgpu_buffer_rsrc_t rs, unsigned vo, unsigned xo, const Tags& T, v4u (&v)[N], float& x0, float& x2, int& cnt) {
  unsigned o[16];
  if constexpr (N == 1) {
    asm volatile(G_L0 "1:\n\t" G_C0(0) G_TEST("v228") G_RE("v228", G_L0) G_END
                 : G_OA, [cnt] "+s"(cnt) : G_IN : G_CLOB);
  } else if constexpr (N == 2 && !X) {
    asm volatile(G_L0 G_L1 "1:\n\t" G_C0(1) G_C1(0) "v_or_b32 v233, v228, v229\n\t" G_TEST("v233") G_RE("v228", G_L0) G_RE("v229", G_L1) G_END
                 : G_OA, G_OB, [cnt] "+s"(cnt) : G_IN : G_CLOB);
  } else if constexpr (N == 2 && X) {
    asm volatile(G_LDX G_L0 G_L1 "1:\n\t" G_CHKX(2) G_C0(1) G_C1(0) "v_or3_b32 v233, v228, v229, v232\n\t" G_TEST("v233")
                 G_RE("v232", G_LDX) G_RE("v228", G_L0) G_RE("v229", G_L1) G_END
                 : G_OA, G_OB, G_OX, [cnt] "+s"(cnt) : G_IN, G_INX : G_CLOB);
  } else if constexpr (N == 4 && !X) {
    asm volatile(G_L0 G_L1 G_L2 G_L3 "1:\n\t" G_C0(3) G_C1(2) G_C2(1) "v_or3_b32 v232, v228, v229, v230\n\t" G_C3(0) "v_or_b32 v233, v232, v231\n\t" G_TEST("v233")
                 G_RE("v228", G_L0) G_RE("v229", G_L1) G_RE("v230", G_L2) G_RE("v231", G_L3) G_END
                 : G_OA, G_OB, G_OC, [cnt] "+s"(cnt) : G_IN : G_CLOB);
  } else {
    asm volatile(G_LDX G_L0 G_L1 G_L2 G_L3 "1:\n\t" G_CHKX(4) G_C0(3) G_C1(2) G_C2(1) G_C3(0)
                 "v_or3_b32 v233, v228, v229, v230\n\tv_or3_b32 v233, v233, v231, v232\n\t" G_TEST("v233")
                 G_RE("v232", G_LDX) G_RE("v228", G_L0) G_RE("v229", G_L1) G_RE("v230", G_L2) G_RE("v231", G_L3) G_END
                 : G_OA, G_OB, G_OC, G_OX, [cnt] "+s"(cnt) : G_IN, G_INX : G_CLOB);
  }
#pragma unroll
  for (int i = 0; i < N; ++i) v[i] = v4u{o[4 * i], o[4 * i + 1], o[4 * i + 2], o[4 * i + 3]};
}
__device__ __forceinline__ f32x4 asf4(v4u v) { return __builtin_bit_cast(f32x4, v); }


#define Q_LD(R, O, S)                                                                                  \
  "buffer_load_dwordx4 %[" O "0], %[" R "], %[" S "] offen nt lds\n\t"                                    \
  "buffer_load_dwordx4 %[" O "1], %[" R "], %[" S "] offen offset:1024 nt lds\n\t"                        \
  "buffer_load_dwordx4 %[" O "2], %[" R "], %[" S "] offen offset:2048 nt lds\n\t"                        \
  "buffer_load_dwordx4 %[" O "3], %[" R "], %[" S "] offen offset:3072 nt lds\n\t"
#define Q_PRE "s_max_i32 %[full], %[full], %[f]\n\tv_mov_b32 v233, %[full]\n\ts_add_i32 %[f], %[f], 1\n\ts_waitcnt vmcnt(%[n])\n\tds_write_b32 %[af], v233\n\t"
#define Q_NXT "s_add_u32 m0, m0, 0x1000\n\t"
template <int D>
__device__ __forceinline__ unsigned quad(__amdgpu_buffer_rsrc_t ra, __amdgpu_buffer_rsrc_t rb, const unsigned (&a)[4], const unsigned (&c)[4],
                                         unsigned s0, unsigned s1, unsigned s2, unsigned s3, int& u, int& full) {
  unsigned long long t; int f = u - D;
  asm volatile("s_mov_b32 m0, %[m]\n\t" Q_PRE Q_LD("ra", "a", "s0") Q_NXT Q_PRE Q_LD("ra", "a", "s1") Q_NXT Q_PRE Q_LD("rb", "c", "s2")
               Q_NXT Q_PRE "s_memrealtime %[t]\n\t" Q_LD("rb", "c", "s3") "s_waitcnt lgkmcnt(0)"
               : [t] "=&s"(t), [f] "+s"(f), [full] "+s"(full)
               : [m] "s"((unsigned)(u & 31) * 4096u), [n] "n"(4 * D), [af] "v"(L_FULL), [ra] "s"(ra), [rb] "s"(rb),
                 [a0] "v"(a[0]), [a1] "v"(a[1]), [a2] "v"(a[2]), [a3] "v"(a[3]), [c0] "v"(c[0]), [c1] "v"(c[1]), [c2] "v"(c[2]), [c3] "v"(c[3]),
                 [s0] "s"(s0), [s1] "s"(s1), [s2] "s"(s2), [s3] "s"(s3)
               : "memory", "scc", "v233");
  u += 4;
  return (unsigned)t;
}
template <int FROM, int TO> __device__ __forceinline__ void settle(int u, int& full) {
  if constexpr (FROM >= TO) {
    asm volatile("s_max_i32 %0, %0, %2\n\tv_mov_b32 v233, %0\n\ts_waitcnt vmcnt(%3)\n\tds_write_b32 %1, v233"
                 : "+s"(full) : "v"(L_FULL), "s"(u - FROM), "n"(4 * FROM) : "memory", "scc", "v233");
    settle<FROM - 1, TO>(u, full);
  }
}

typedef unsigned v32u __attribute__((ext_vector_type(32)));
#define D_K(RB, K, H, B)                                                                                          \
  "v_xor_b32 v[208+" #K "], 32*" #K ", %[" B "]\n\t"                                                              \
  "ds_read_b64_tr_b16 v[" #RB "+8*" #K "+" #H ":" #RB "+8*" #K "+" #H "+1], v[208+" #K "]\n\t"                       \
  "ds_read_b64_tr_b16 v[" #RB "+8*" #K "+" #H "+2:" #RB "+8*" #K "+" #H "+3], v[208+" #K "] offset:16384\n\t"
#define D_H(RB, H, B) D_K(RB, 0, H, B) D_K(RB, 1, H, B) D_K(RB, 2, H, B) D_K(RB, 3, H, B) D_K(RB, 4, H, B) D_K(RB, 5, H, B) D_K(RB, 6, H, B) D_K(RB, 7, H, B)
#define D_ALL(RB) "s_cmp_eq_u32 %[on], 0\n\ts_cbranch_scc1 1f\n\t" D_H(RB, 0, "b0") D_H(RB, 4, "b1") "s_waitcnt lgkmcnt(0)\n\t1:"
#define D_CLOB "memory", "scc", "v208", "v209", "v210", "v211", "v212", "v213", "v214", "v215"
template <int P, int U> __device__ __forceinline__ void drain(bool on, unsigned wa, v32u (&W)[4]) {
  const unsigned b0 = wa + (U & 31) * 4096u, b1 = wa + ((U + 8) & 31) * 4096u;
  if constexpr (P == 0) asm volatile(D_ALL(64) : "+{v[64:95]}"(W[0]), "+{v[96:127]}"(W[1]) : [on] "s"(on ? 7 : 0), [b0] "v"(b0), [b1] "v"(b1) : D_CLOB);
  else asm volatile(D_ALL(128) : "+{v[128:159]}"(W[2]), "+{v[160:191]}"(W[3]) : [on] "s"(on ? 7 : 0), [b0] "v"(b0), [b1] "v"(b1) : D_CLOB);
}
template <int O> __device__ __forceinline__ v4u sub4(const v32u& v) { return __builtin_shufflevector(v, v, O, O + 1, O + 2, O + 3); }
template <int G, int E> __device__ __forceinline__ void vm1(v4u f0, v4u f1, const v32u (&W)[4], int g4, f32x4& r) {
  constexpr int k = 2 * G + (E >> 1), i = 2 * (E & 1) + (k >> 2), o = 8 * (k & 3);
  f32x4 d = mfma(f0, sub4<o>(W[i]), f32x4{0.f, 0.f, 0.f, 0.f});
  d = mfma(f1, sub4<o + 4>(W[i]), d);
  r[E] = (G == 0 || g4 == G) ? d[0] : r[E];
}
template <int G> __device__ __forceinline__ void vm4(v4u f0, v4u f1, const v32u (&W)[4], int g4, f32x4& r) {
  vm1<G, 0>(f0, f1, W, g4, r); vm1<G, 1>(f0, f1, W, g4, r); vm1<G, 2>(f0, f1, W, g4, r); vm1<G, 3>(f0, f1, W, g4, r);
}
__device__ __forceinline__ f32x4 vecmat(v4u f0, v4u f1, const v32u (&W)[4], int g4) {
  f32x4 r = {0.f, 0.f, 0.f, 0.f};
  vm4<0>(f0, f1, W, g4, r); vm4<1>(f0, f1, W, g4, r); vm4<2>(f0, f1, W, g4, r); vm4<3>(f0, f1, W, g4, r);
  return r;
}
__device__ __forceinline__ void red_gu(f32x4 a, float& G, float& U) {
  auto s0 = __builtin_amdgcn_permlane32_swap(asu(a[0]), asu(a[2]), false, false);
  auto s1 = __builtin_amdgcn_permlane32_swap(asu(a[1]), asu(a[3]), false, false);
  G = asf(s0[0]) + asf(s0[1]); U = asf(s1[0]) + asf(s1[1]);
  auto t0 = __builtin_amdgcn_permlane16_swap(asu(G), asu(G), false, false);
  auto t1 = __builtin_amdgcn_permlane16_swap(asu(U), asu(U), false, false);
  G = asf(t0[0]) + asf(t0[1]); U = asf(t1[0]) + asf(t1[1]);
  G += dpp<ROR8>(G); U += dpp<ROR8>(U);
}

typedef unsigned v16u __attribute__((ext_vector_type(16)));
__device__ __forceinline__ const void* mkptr(unsigned lo, unsigned hi) { return (const void*)(((unsigned long long)hi << 32) | lo); }

__global__ void __launch_bounds__(320, 2)
mk(const void*, const void*, const void*, const void*, const void*, const void*, const void*, const void*, void*, void*) {
  int b = blockIdx.x;
  v16u ka; v4u kb; unsigned long long t00, pc0, w1;
  {
    unsigned t, o;
    asm volatile("s_load_dwordx16 %0, %8, 0x0\n\ts_and_b32 %6, %5, 63\n\ts_cmp_lt_u32 %6, 36\n\ts_cselect_b32 %7, 56, 40\n\t"
                 "s_cmp_lt_u32 %6, 32\n\ts_cselect_b32 %7, 24, %7\n\ts_cmp_lt_u32 %6, 16\n\ts_cselect_b32 %7, 32, %7\n\t"
                 "s_load_dwordx2 %4, %8, %7\n\ts_load_dwordx4 %1, %8, 0x40\n\ts_memrealtime %2\n\ts_getpc_b64 %3\n\tmk_code_begin:"
                 : "=&s"(ka), "=&s"(kb), "=&s"(t00), "=&s"(pc0), "=&s"(w1), "+s"(b), "=&s"(t), "=&s"(o) : "s"(__builtin_amdgcn_kernarg_segment_ptr()) : "scc");
  }
  const int lane = threadIdx.x & 63;
  const int wv = __builtin_amdgcn_readfirstlane(threadIdx.x >> 6);
  const int g4 = lane >> 4, c16 = lane & 15;
  const int grp = b >> 6, j = b & 63;
  const bool isO = j < 16, isQ = j >= 16 && j < 32, isG = j >= 36;
  const int jq = (j - 2 * grp) & 15;
  const int jd = isQ ? jq + 4 : j - 32;
  const bool isD = (unsigned)jd < (isQ ? 14u : 4u);
  const int per = isG ? 28 : isO ? 16 : 14, loc = isG ? j - 36 : isO ? j : jd;
  const int idx = per * grp + loc;
  const int r16 = idx & 15, cb = idx >> 4;
  const int ukv = j < 32 ? 32 : 16;
  const int qc = 8 * (j & 7) + 2 * grp + ((j >> 3) & 1);

  asm volatile("s_cmp_eq_u32 %4, 0\n\ts_cbranch_scc1 1f\n\ts_waitcnt lgkmcnt(0)\n\t1:" : "+s"(ka), "+s"(kb), "+s"(t00), "+s"(w1) : "s"(wv) : "scc");
  if (__builtin_expect(wv == 0, 1)) {
    const unsigned stride = isG ? 7168u : 2048u;
    const unsigned s1 = isG ? 512u * 7168u : 16u * 2048u, s2 = isG ? 16u * 7168u : 32u * 2048u;
    const unsigned base0 = (unsigned)r16 * (isG ? 32u * 7168u : 64u * 2048u) + (unsigned)cb * (isG ? 1024u : 16u * 64u * 2048u);
    const unsigned base1 = base0 + (isG ? 0u : 1024u);
    unsigned o1[4];
    const unsigned st1 = isQ ? 8192u : stride, d1 = (isQ ? 65536u : 4u * stride) - 1024u, us1 = isQ ? 32u * 8192u : 256u;
    unsigned so1 = isQ ? (unsigned)qc * 128u : base0;
    {
      const unsigned sw = 16u * ((lane & (isQ ? 7 : 15)) ^ ((lane >> 3) & 6)), r = (lane >> (isQ ? 3 : 4)) * st1;
      o1[0] = r + sw; o1[1] = r + (sw ^ (isQ ? 0u : 128u)) + d1; o1[2] = o1[0] + 2 * d1; o1[3] = o1[1] + 2 * d1;
    }
    unsigned so1b = so1 + us1, so1c = so1 + 2 * us1, so1d = so1 + 3 * us1, wb1 = j < 32 ? 4096u * 2048u : 3584u * 2048u;
    asm volatile("s_waitcnt lgkmcnt(0)" : "+s"(ka), "+s"(kb), "+s"(t00), "+s"(w1), "+s"(so1), "+s"(so1b), "+s"(so1c), "+s"(so1d), "+s"(wb1)
                 : "v"(o1[0]), "v"(o1[1]), "v"(o1[2]), "v"(o1[3]));
    const __amdgpu_buffer_rsrc_t r1 = mkrs((const void*)w1, wb1);
    asm volatile("s_mov_b32 m0, 0\n\ts_nop 0\n\t" Q_LD("ra", "a", "s0") Q_NXT "s_nop 0\n\t" Q_LD("ra", "a", "s1") Q_NXT "s_nop 0\n\t" Q_LD("ra", "a", "s2") Q_NXT "s_nop 0\n\t" Q_LD("ra", "a", "s3")
                 :: [ra] "s"(r1), [a0] "v"(o1[0]), [a1] "v"(o1[1]), [a2] "v"(o1[2]), [a3] "v"(o1[3]),
                    [s0] "s"(so1), [s1] "s"(so1b), [s2] "s"(so1c), [s3] "s"(so1d) : "memory", "scc");
    const unsigned ct0 = (unsigned)t00;
    lds_st32(L_CTL + 4 * lane, 0u);
    int u = 4, freed = 0, full = 0;
    int bud = BUDGET;
    auto space = [&]() {
      if (freed < u - 28) {
        for (unsigned spins = 0;; ++spins) {
          const v4u p = lds_ld128_wait(L_PROG);
          freed = (int)__builtin_amdgcn_readfirstlane(min(min(p[0], p[1]), min(p[2], p[3])));
          if (freed >= u - 28) break;
          if (spins == 0) settle<0, 0>(u, full);
          if (bud <= 0) break;
          --bud;
          __builtin_amdgcn_s_sleep(1);
        }
      }
    };
    if (isQ) {
#pragma unroll 1
      while (u < 32) {
        so1 += 128u * 8192u;
        const unsigned t = quad<INFLQ>(r1, r1, o1, o1, so1, so1 + 32u * 8192u, so1 + 64u * 8192u, so1 + 96u * 8192u, u, full);
        if (u == 8) __builtin_amdgcn_s_barrier();
      }
      settle<INFLQ, INFL + 1>(u, full);
    } else {
      asm volatile("s_waitcnt lgkmcnt(0)" ::: "memory");
      __builtin_amdgcn_s_barrier();
#pragma unroll 1
      while (u < ukv) {
        const unsigned so = base0 + (u >> 4) * 1024u + ((u >> 2) & 1) * s1 + ((u >> 3) & 1) * s2;
        const unsigned t = quad<INFL0>(r1, r1, o1, o1, so, so + 256, so + 512, so + 768, u, full);
      }
    }
    unsigned chV[4];
#pragma unroll
    for (int i = 0; i < 4; ++i) chV[i] = 16 * (c16 ^ (8 * (i & 1) + 2 * g4));
    {
      unsigned oK[4], oV[4];
#pragma unroll
      for (int i = 0; i < 4; ++i) {
        oK[i] = g4 * 256 + 16 * (c16 ^ (4 * i + g4));
        oV[i] = g4 * 256 + chV[i];
      }
      const __amdgpu_buffer_rsrc_t rk = mkrs(mkptr(ka[2], ka[3]), 1u << 26), rv = mkrs(mkptr(ka[4], ka[5]), 1u << 26);
      unsigned so = ((unsigned)grp << 24) + (unsigned)(j < 32 ? 30 * j : 34 * j - 128) * 8192u;
      int d = INFL;
#pragma unroll 1
      for (; u < U_POST; so += 8192) {
        space();
        unsigned t;
#define KVQ(D) t = quad<D>(rk, rv, oK, oV, so, so + 4096, so, so + 4096, u, full)
        if (!CTL) { KVQ(INFL); }
        else {
          if (d <= CT_LO + 2) { if (d <= CT_LO) KVQ(CT_LO); else if (d == CT_LO + 1) KVQ(CT_LO + 1); else KVQ(CT_LO + 2); }
          else { if (d == CT_LO + 3) KVQ(CT_LO + 3); else if (d == CT_LO + 4) KVQ(CT_LO + 4); else KVQ(CT_LO + 5); }
          const int lag = (int)(t - ct0) - (CT_A + ((full * ((CT_E - CT_A) * 256 / U_POST)) >> 8));
          d = min(INFL + ((lag * (4096 / CT_G) + 2048) >> 12), CT_HI);
        }
        if (u == 36) settle<4, 4>(u, full);
      }
    }
    constexpr int DMAX = CTL ? CT_LO + 5 : INFL;
    if (isG || isD) {
      const __amdgpu_buffer_rsrc_t r = mkrs(mkptr(isG ? ka[12] : ka[14], isG ? ka[13] : ka[15]), 3584u * 2048u);
      unsigned oW[4];
#pragma unroll
      for (int i = 0; i < 4; ++i) oW[i] = (4 * i + g4) * stride + chV[i] - 1024 * i;
      settle<DMAX, INFLD + 1>(u, full);
      if (!isQ) settle<INFLD, INFLP + 1>(u, full);
      const int end = U_POST + (isQ ? 32 : 16);
#pragma unroll 1
      while (u < end) {
        space();
        const int i = u - U_POST;
        const unsigned so = base1 - (i >> 4) * 1024u + ((i >> 2) & 1) * s1 + ((i >> 3) & 1) * s2;
        unsigned t;
        if (isQ) t = quad<INFLD>(r, r, oW, oW, so, so + 256, so + 512, so + 768, u, full);
        else t = quad<INFLP>(r, r, oW, oW, so, so + 256, so + 512, so + 768, u, full);
      }
    }
    settle<(DMAX > INFLD ? DMAX : INFLD), 0>(u, full);
    return;
  }

  const int w = wv - 1;
  const GAS unsigned short* x = (const GAS unsigned short*)mkptr(ka[0], ka[1]);
  char* ws = (char*)mkptr(kb[2], kb[3]);
  const __amdgpu_buffer_rsrc_t rs = mkrs(ws, WS_END);
  v4u xa = *(const GAS v4u*)(x + 16 * lane), xb = *(const GAS v4u*)(x + 16 * lane + 8);
  unsigned tprev;
  asm volatile("s_load_dword %0, %1, 0x0" : "=&s"(tprev) : "s"(ws));
  if (!isQ) {
    const int rb = 4 * (j >> 3) + grp + (isO ? 8 : 0);
    if (PF == 3) asm volatile("global_load_dword v234, %0, %1 nt" ::"v"((32 * rb + (lane >> 1)) * 8192 + (b & 7) * 1024 + w * 256 + (lane & 1) * 128), "s"(mkptr(ka[6], ka[7])) : "v234");
    if (PF == 5 && w == 0) asm volatile("global_load_dword v234, %0, %1 nt" ::"v"((32 * rb + (lane >> 1)) * 8192 + (b & 7) * 1024 + 384 + (lane & 1) * 512), "s"(mkptr(ka[6], ka[7])) : "v234");
    if (CPF && w == 1) asm volatile("v_min_u32 v233, mk_code_end-mk_code_begin, %0\n\tglobal_load_dword v234, v233, %1" ::"v"((b & 24) * 512 + (b & 64) * 256 + lane * 64), "s"(pc0) : "v233", "v234");
  }
  __builtin_amdgcn_s_barrier();
  asm volatile("s_waitcnt lgkmcnt(0)" : "+s"(tprev));
  const Tags T = mktags(tprev + 1u);
  int bud = BUDGET;
  int seen = 0;
  const int sub = lane >> 2, p4 = lane & 3;
  const unsigned trrow = 256u * sub + 8u * p4;
  const unsigned hcol = (g4 & 1) * 512 + 32 * r16 + (g4 >> 1) * 16 + 4 * w + p4;
  float xres = 0.f;
  if (isQ) {
    float s = 0.f;
#pragma unroll
    for (int e = 0; e < 4; ++e) s += (lo16(xa[e]) + hi16(xa[e])) + (lo16(xb[e]) + hi16(xb[e]));
    const float mean = xrow_sum(row_sum(s)) * (1.f / 1024.f);
    float q = 0.f;
#pragma unroll
    for (int e = 0; e < 4; ++e) {
      const float d0 = lo16(xa[e]) - mean, d1 = hi16(xa[e]) - mean, d2 = lo16(xb[e]) - mean, d3 = hi16(xb[e]) - mean;
      q += (d0 * d0 + d1 * d1) + (d2 * d2 + d3 * d3);
    }
    const float var = xrow_sum(row_sum(q)) * (1.f / 1024.f);
    const float mb = rbf(mean), rsd = __builtin_amdgcn_rcpf(rbf(__builtin_amdgcn_sqrtf(rbf(rbf(var) + 1e-5f))));
    auto nrm = [&](unsigned p) { return pk2(rbf(lo16(p) - mb) * rsd, rbf(hi16(p) - mb) * rsd); };
    const unsigned xl = L_XN + (lane >> 1) * 64 + (lane & 1) * 8;
    lds_st64(xl, v2u{nrm(xa[0]), nrm(xa[1])});
    lds_st64(xl + 16, v2u{nrm(xa[2]), nrm(xa[3])});
    lds_st64(xl + 32, v2u{nrm(xb[0]), nrm(xb[1])});
    lds_st64(xl + 48, v2u{nrm(xb[2]), nrm(xb[3])});
    asm volatile("s_waitcnt lgkmcnt(0)" ::: "memory");
    unsigned a = sub * 128u + 16u * ((2 * w + (p4 >> 1)) ^ (sub & 6)) + 8u * (p4 & 1), xo = L_XN + g4 * 16;
    f32x4 ac[4] = {};
#pragma unroll 1
    for (int u0 = 0; u0 < 32; u0 += 4, a += 16384, xo += 256) {
      v4u xf[4], wf[4];
#pragma unroll
      for (int i = 0; i < 4; ++i) xf[i] = lds128(xo + 64 * i);
      wait_full(u0 + 4, seen, bud);
#pragma unroll
      for (int i = 0; i < 4; ++i) wf[i] = ldstr2(a + 4096 * i, a + 4096 * i + 2048);
      lds_st32(L_PROG + 4 * w, (unsigned)(u0 + 4));
#pragma unroll
      for (int i = 0; i < 4; ++i) ac[i] = mfma(wf[i], xf[i], ac[i]);
    }
    const f32x4 acc = (ac[0] + ac[1]) + (ac[2] + ac[3]);
    v4u r;
#pragma unroll
    for (int e = 0; e < 4; ++e) r[e] = (pk2(acc[e], acc[e]) << 16) | T.h[e];
    gst(rs, WS_QF + c16 * 16384 + qc * 256 + w * 64 + g4 * 16, r);
  } else {
    xres = lo16(x[hcol]);
    if (isG && w == cb) gst(rs, WS_PD + (r16 * 4 + w) * 4096u + 3584u + (lane & 31) * 16, tag4(f32x4{0.f, 0.f, 0.f, 0.f}, T));
  }
  asm volatile("s_waitcnt vmcnt(0)" ::: "memory");
  const unsigned wa = w * 4096u + trrow + 32u * (sub & 7);
  v32u W[4];
  asm volatile("" : "={v[64:95]}"(W[0]), "={v[96:127]}"(W[1]), "={v[128:159]}"(W[2]), "={v[160:191]}"(W[3]));
  wait_full(16, seen, bud);
  drain<0, 0>(!isQ, wa, W);
  lds_st32(L_PROG + 4 * w, isQ ? 32u : 16u);
  wait_full(ukv, seen, bud);
  drain<1, 16>(isO, wa, W);
  lds_st32(L_PROG + 4 * w, (unsigned)ukv);
  wait_full(isQ ? QATQ : QAT, seen, bud);
  v4u qf[4];
  {
    v4u v[1]; float d0, d1;
    gather<1, false, M16, 4>(rs, WS_QF + ((b & 15) * 32 + grp * 8 + 2 * w) * 512 + lane * 16, 0, T, v, d0, d1, bud);
    lds_st64(L_Q + w * 512 + lane * 8, v2u{(v[0][0] >> 16) | v[0][1], (v[0][2] >> 16) | v[0][3]});
    lds_st32(L_XF + 4 * w, 1u);
    exch(L_XF, L_Q + (c16 & 7) * 256 + g4 * 16, qf[0], qf[1], bud);
    qf[2] = lds128(L_Q + (c16 & 7) * 256 + g4 * 16 + 128);
    qf[3] = lds128(L_Q + (c16 & 7) * 256 + g4 * 16 + 192);
  }

  {
    unsigned ka[4], va[2];
#pragma unroll
    for (int u = 0; u < 4; ++u) ka[u] = 256u * c16 + 16u * ((4 * u + g4) ^ c16);
#pragma unroll
    for (int i = 0; i < 2; ++i) va[i] = 8192u + trrow + 32u * ((2 * w + i) ^ (sub & 7));
    const bool up = c16 >= 8;
    const float LOG2E = 1.4426950408889634f, scale = 0.08838834764831845f;
    float m = -INFINITY, l = 0.f;
    f32x4 acc[2] = {f32x4{0.f, 0.f, 0.f, 0.f}, f32x4{0.f, 0.f, 0.f, 0.f}};
    const unsigned aPA = WS_PA + (((grp * 8 + (c16 & 7)) * 8 + 2 * w + (c16 >> 3)) * 64 + j) * 64 + g4 * 16;
    const unsigned aML = 8 * w + lane < 8 ? WS_ML + (b * 8 + lane) * 16 : OOB;
#pragma unroll 1
    for (int u0 = ukv; u0 < U_POST; u0 += 4) {
      const unsigned tb = (unsigned)(u0 & 31) * 4096u;
      wait_full(u0 + 2, seen, bud);
      if (u0 == U_POST - 4 * NE) {
        if (w == 0) {
          const unsigned eta = (unsigned)__builtin_amdgcn_s_memrealtime() + KE;
          __builtin_amdgcn_raw_buffer_store_b32((eta << 16) | ((b & 2) ? (b & 1) ? T.h[3] : T.h[2] : (b & 1) ? T.h[1] : T.h[0]), rs, (int)(lane < 16 ? WS_ET + lane * 1024 + b * 4 : OOB), 0, 16);
        }
        if (CPF2 && w == 1) asm volatile("v_min_u32 v233, mk_code_end-mk_code_begin, %0\n\tglobal_load_dword v234, v233, %1" ::"v"((b & 24) * 512 + (b & 64) * 256 + lane * 64), "s"(pc0) : "v233", "v234");
      }
      f32x4 s0 = {0.f, 0.f, 0.f, 0.f}, s1 = {0.f, 0.f, 0.f, 0.f};
#pragma unroll
      for (int u = 0; u < 4; ++u) {
        s0 = mfma(lds128(tb + ka[u]), qf[u], s0);
        s1 = mfma(lds128(tb + ka[u] + 4096), qf[u], s1);
      }
      float s[4];
#pragma unroll
      for (int r = 0; r < 4; ++r) s[r] = rbf(rbf(up ? s1[r] : s0[r]) * scale);
      const float mn = fmaxf(m, xrow_max(fmaxf(fmaxf(s[0], s[1]), fmaxf(s[2], s[3]))));
      const float al = __builtin_amdgcn_exp2f((m - mn) * LOG2E);
      m = mn;
      float p[4];
#pragma unroll
      for (int r = 0; r < 4; ++r) p[r] = __builtin_amdgcn_exp2f((s[r] - mn) * LOG2E);
      l = l * al + ((p[0] + p[1]) + (p[2] + p[3]));
      const unsigned p01 = pk2(p[0], p[1]), p23 = pk2(p[2], p[3]);
      v4u pb = up ? v4u{0u, 0u, p01, p23} : v4u{p01, p23, 0u, 0u};
      acc[0] *= al; acc[1] *= al;
      asm volatile("" : "+v"(pb), "+v"(acc[0]), "+v"(acc[1]), "+v"(l));
      wait_full(u0 + 4, seen, bud);
      const v4u v0 = ldstr2(tb + va[0], tb + va[0] + 4096), v1 = ldstr2(tb + va[1], tb + va[1] + 4096);
      acc[0] = mfma(v0, pb, acc[0]);
      acc[1] = mfma(v1, pb, acc[1]);
      asm volatile("s_waitcnt lgkmcnt(0)" ::: "memory");
      lds_st32(L_PROG + 4 * w, (unsigned)(u0 + 4));
    }
    const float M = fmaxf(m, dpp<ROR8>(m));
    const float f = __builtin_amdgcn_exp2f((m - M) * LOG2E);
    float lt = xrow_sum(l) * f;
    lt += dpp<ROR8>(lt);
    f32x4 o;
#pragma unroll
    for (int e = 0; e < 4; ++e) {
      const float a0 = acc[0][e] * f, a1 = acc[1][e] * f;
      o[e] = (up ? a1 : a0) + dpp<ROR8>(up ? a0 : a1);
    }
    gst(rs, aPA, tag4(o, T));
    gst(rs, aML, v4u{asu(M), T.t, asu(lt), T.y});
  }

  if (isO || isG || isD) {
    const v4u ones = {0x3F803F80u, 0x3F803F80u, 0x3F803F80u, 0x3F803F80u};
    const unsigned aPC = (((8 * cb + 2 * w) * 4 + 2 * g4 + (c16 >> 3)) * 16 + r16) * 128 + (c16 & 7) * 16;
    const unsigned aOut = isG ? WS_PC + aPC : (isO ? WS_PB : WS_PD) + (((4 * w + g4) * 4 + (c16 >> 2)) * 64 + idx) * 64 + (c16 & 3) * 16;
    float h = 0.f;
    wait_full(isO ? 0 : U_POST + 16, seen, bud);
    drain<1, U_POST>(!isO, wa, W);
    wait_full(isQ ? U_POST + 32 : 0, seen, bud);
    drain<0, U_POST + 16>(isQ, wa, W);
    if (isG) gst(rs, WS_CS + aPC, tag4(vecmat(ones, ones, W, g4), T));
    if (w == 0) {
      v4u v[1]; float d0, d1;
      gather<1, false, M16, 8>(rs, WS_ET + (b & 15) * 1024 + (isO ? grp * 256 + c16 * 16 : lane * 16), 0, T, v, d0, d1, bud);
      const unsigned now = (unsigned)__builtin_amdgcn_s_memrealtime(), n16 = now << 16;
      const int dm = max(max((int)(v[0][0] - n16), (int)(v[0][1] - n16)), max((int)(v[0][2] - n16), (int)(v[0][3] - n16))) >> 16;
      const unsigned E = (now + (unsigned)min((int)xrow_max(row_max((float)dm)), 300)) | 1u;
      lds_st32(L_ETA, E);
    }
#define NF0
#define NF1(sh)
    if (isO) {
      v4u v[4]; float ms, ls;
      unsigned a0 = WS_PA + (idx * 4 + w) * 4096u + lane * 16, a1 = WS_ML + ((grp * 64 + 16 * p4 + sub) * 8 + (j >> 1)) * 16;
      asm volatile("" : "+v"(a0), "+v"(a1));
      gate(DA, bud);
      NF0;
      gather<4, true, M10, GAPA>(rs, a0, a1, T, v, ms, ls, bud);
      NF1(0);
      const float LOG2E = 1.4426950408889634f;
      const float M = xrow_max(row_max(ms));
      const float f = __builtin_amdgcn_exp2f((ms - M) * LOG2E);
      const float L = xrow_sum(row_sum(f * ls));
      f32x4 a = asf4(v[0]) * dpp<0x00>(f);
      a += asf4(v[1]) * dpp<0x55>(f);
      a += asf4(v[2]) * dpp<0xAA>(f);
      a += asf4(v[3]) * dpp<0xFF>(f);
      const float o = red_scatter(a) * __builtin_amdgcn_rcpf(L);
      lds_st16<0x000F000Fu>(L_VEC + (w >> 1) * 64 + p4 * 16 + (w & 1) * 8 + g4 * 2, pk2(o, o));
    } else {
      if (isG) {
        v4u v[4]; float d0, d1;
        unsigned a0 = WS_PB + (r16 * 4 + w) * 4096u + lane * 16;
        asm volatile("" : "+v"(a0));
        gate(DB, bud);
        NF0;
        gather<4, false, M10, GAPB>(rs, a0, 0, T, v, d0, d1, bud);
        NF1(0);
        const f32x4 a = (asf4(v[0]) + asf4(v[1])) + (asf4(v[2]) + asf4(v[3]));
        h = rbf(xres + rbf(red_scatter(a)));
        lds_st16<0x000F000Fu>(L_VEC + (g4 >> 1) * 64 + w * 16 + (g4 & 1) * 8 + p4 * 2, asu(h) >> 16);
      } else {
        const unsigned gPC = (idx * 4 + w) * 2048 + lane * 16;
        float Cg, Cu;
        {
          v4u v[2]; float d0, d1;
          gate(DS, bud);
          gather<2, false, M10, 4>(rs, WS_CS + gPC, 0, T, v, d0, d1, bud);
          red_gu(asf4(v[0]) + asf4(v[1]), Cg, Cu);
        }
        v4u v[2]; float sv0, sv2;
        unsigned a0 = WS_PC + gPC, a1 = WS_ST + (idx * 16 + c16) * 64 + w * 16;
        asm volatile("" : "+v"(a0), "+v"(a1), "+v"(Cg), "+v"(Cu));
        gate(DC, bud);
        NF0;
        gather<2, true, M10, GAPB>(rs, a0, a1, T, v, sv0, sv2, bud);
        NF1(0);
        const float mean = row_sum(sv0) * (1.f / 1024.f);
        const float var = fmaxf(row_sum(sv2) * (1.f / 1024.f) - mean * mean, 0.f);
        const float mb = rbf(mean), sd = rbf(__builtin_amdgcn_sqrtf(rbf(rbf(var) + 1e-5f)));
        const float rsd = __builtin_amdgcn_rcpf(sd);
        float G, U;
        red_gu(asf4(v[0]) + asf4(v[1]), G, U);
        const float g = rbf((G - mb * Cg) * rsd), u = rbf((U - mb * Cu) * rsd);
        const float y = 0.7978845608028654f * (g + 0.044715f * g * g * g);
        const float ge = rbf(g * __builtin_amdgcn_rcpf(1.f + __builtin_amdgcn_exp2f(y * (-2.f * 1.4426950408889634f))));
        const float act = ge * u;
        lds_st16<0xFFu>(L_VEC + (w >> 1) * 64 + (2 * (w & 1) + ((lane >> 2) & 1)) * 16 + (lane >> 5) * 8 + p4 * 2, pk2(act, act));
      }
    }
    lds_st32(L_XF + 16 + 4 * w, 1u);
    v4u f0, f1;
    exch(L_XF + 16, L_VEC + g4 * 16, f0, f1, bud);
    if (isG) {
      f32x4 s2 = mfma(f0, f0, f32x4{0.f, 0.f, 0.f, 0.f}), s1 = mfma(f0, ones, f32x4{0.f, 0.f, 0.f, 0.f});
      s2 = mfma(f1, f1, s2); s1 = mfma(f1, ones, s1);
      gst(rs, lane < 8 ? WS_ST + ((8 * cb + 2 * w + (lane >> 2)) * 16 + r16) * 64 + p4 * 16 : OOB, v4u{asu(s1[0]), T.t, asu(s2[0]), T.y});
    }
    gst(rs, aOut, tag4(vecmat(f0, f1, W, g4), T));
    if (isG) {
      if (idx == 111 && w == 0) __builtin_amdgcn_raw_buffer_store_b32(T.t, rs, lane ? (int)OOB : 0, 0, 16);
      if (w == cb) {
        v4u v[4]; float d0, d1;
        unsigned a0 = WS_PD + (r16 * 4 + w) * 4096u + lane * 16;
        asm volatile("" : "+v"(a0));
        gate(DD, bud);
        NF0;
        gather<4, false, M10, GAPB>(rs, a0, 0, T, v, d0, d1, bud);
        NF1(8);
        const f32x4 a = (asf4(v[0]) + asf4(v[1])) + (asf4(v[2]) + asf4(v[3]));
        const float res = h + rbf(red_scatter(a));
        __builtin_amdgcn_raw_buffer_store_b16((unsigned short)pk2(res, res), mkrs(mkptr(kb[0], kb[1]), 2048), (int)(c16 < 4 ? 2 * hcol : OOB), 0, 16);
        asm volatile("mk_code_end:");
      }
    }
  }
}

extern "C" void kernel_launch(void* const* d_in, const int* in_sizes, int n_in, void* d_out, int out_size, void* d_ws,
                              size_t ws_size, hipStream_t stream) {
  (void)in_sizes; (void)n_in; (void)out_size; (void)ws_size;
  (void)hipFuncSetAttribute((const void*)mk, hipFuncAttributeMaxDynamicSharedMemorySize, (int)LDS_BYTES);
  mk<<<256, 320, LDS_BYTES, stream>>>(d_in[0], d_in[1], d_in[2], d_in[3], d_in[4], d_in[6], d_in[7], d_in[8], d_out, (char*)d_ws + ((ws_size - (4u << 20)) & ~(size_t)0x1FFFFF));
}
```

```cpp
#include <hip/hip_runtime.h>
#include <stdint.h>

typedef unsigned v4u __attribute__((ext_vector_type(4)));
typedef unsigned v2u __attribute__((ext_vector_type(2)));
typedef float f32x4 __attribute__((ext_vector_type(4)));
typedef float f32x2 __attribute__((ext_vector_type(2)));
typedef __bf16 bf16x8_t __attribute__((ext_vector_type(8)));
typedef __bf16 bf16x2_t __attribute__((ext_vector_type(2)));
typedef short s16x4 __attribute__((ext_vector_type(4)));
#define LAS __attribute__((address_space(3)))
#define GAS __attribute__((address_space(1)))

constexpr unsigned WS_PH = 0x020000;
constexpr unsigned WS_E2 = 0x003000;
constexpr unsigned WS_ET = 0x004000;
constexpr unsigned WS_ML = 0x008000;
constexpr unsigned WS_ST = 0x010000;
constexpr unsigned WS_QF = 0x040000;
constexpr unsigned WS_PB = 0x080000;
constexpr unsigned WS_PD = 0x0C0000;
constexpr unsigned WS_PA = 0x100000;
constexpr unsigned WS_PC = 0x200000;
constexpr unsigned WS_END = 0x300000;
constexpr unsigned SLOT = 0x400000, WS_ALL = SLOT;
constexpr unsigned OOB = 0x10000000;

constexpr unsigned L_CTL = 131072;
constexpr unsigned L_FULL = L_CTL;
constexpr unsigned L_PROG = L_CTL + 16;
constexpr unsigned L_XF = L_CTL + 32;
constexpr unsigned L_ETA = L_CTL + 64;
constexpr unsigned L_REF = L_CTL + 68;
constexpr unsigned L_EA = L_CTL + 72;
constexpr unsigned L_OWN = L_CTL + 80;
constexpr unsigned L_VEC = L_CTL + 256;
constexpr unsigned L_XN = L_CTL + 1024;
constexpr unsigned L_Q = L_CTL + 3072;
constexpr unsigned LDS_BYTES = L_CTL + 5120;

#define INFL 4
#define INFL0 4
#define INFLQ 14
#define INFLP 4
#define INFLD 6
#define QAT 28
#define PF 5
#define QATQ 40
#define CPF 1
#define NE 4
#define KE 215
#define DA 72
#define DB 258
#define DC 384
#define DD 515
#define LG 65
#define LW0 30
#define LW1 60
#define LWA 45
#define NSA 1
#define NSB 1
#define PT 0
#define LWG 0
#define CPF2 1
#define GAPQ 4
#define NREP 2
#define FB 1
#define FB_R 4
#define FB_0 20
#define FB_1 124
#define FB_P 256
#define FB_L 20
#define FB_W 100
#define FB_OFF 30
#define FB_MIN 14
#define SLEW 1
#define ET_1 140
#define ET_2 265
#define GAPA 0
#define GAPB 0
#define CTL 1
#define CT_A 200
#define CT_E 2300
#define CT_G 30
#define CT_LO 3
#define CT_HI 8
constexpr int U_POST = 152;
constexpr int RATE = (CT_E - CT_A) * 256 / U_POST;
constexpr int BUDGET = 1 << 20;

__device__ __forceinline__ float asf(unsigned u) { return __builtin_bit_cast(float, u); }
__device__ __forceinline__ unsigned asu(float f) { return __builtin_bit_cast(unsigned, f); }
__device__ __forceinline__ unsigned pk2(float lo, float hi) {
  return __builtin_bit_cast(unsigned, __builtin_convertvector(f32x2{lo, hi}, bf16x2_t));
}
__device__ __forceinline__ float rbf(float f) { return asf(pk2(f, f) << 16); }
__device__ __forceinline__ float lo16(unsigned u) { return asf(u << 16); }
__device__ __forceinline__ float hi16(unsigned u) { return asf(u & 0xFFFF0000u); }
__device__ __forceinline__ f32x4 mfma(v4u a, v4u b, f32x4 c) {
  return __builtin_amdgcn_mfma_f32_16x16x32_bf16(__builtin_bit_cast(bf16x8_t, a), __builtin_bit_cast(bf16x8_t, b), c, 0, 0, 0);
}
template <int CTRL> __device__ __forceinline__ float dpp(float x) {
  return asf((unsigned)__builtin_amdgcn_mov_dpp((int)asu(x), CTRL, 0xf, 0xf, true));
}
constexpr int XOR1 = 0xB1, XOR2 = 0x4E, ROR4 = 0x124, ROR8 = 0x128;
__device__ __forceinline__ float xrow_max(float x) {
  auto s = __builtin_amdgcn_permlane16_swap(asu(x), asu(x), false, false);
  x = fmaxf(asf(s[0]), asf(s[1]));
  auto t = __builtin_amdgcn_permlane32_swap(asu(x), asu(x), false, false);
  return fmaxf(asf(t[0]), asf(t[1]));
}
__device__ __forceinline__ float xrow_sum(float x) {
  auto s = __builtin_amdgcn_permlane16_swap(asu(x), asu(x), false, false);
  x = asf(s[0]) + asf(s[1]);
  auto t = __builtin_amdgcn_permlane32_swap(asu(x), asu(x), false, false);
  return asf(t[0]) + asf(t[1]);
}
__device__ __forceinline__ float row_sum(float x) {
  x += dpp<XOR1>(x); x += dpp<XOR2>(x); x += dpp<ROR4>(x); x += dpp<ROR8>(x); return x;
}
__device__ __forceinline__ float row_max(float x) {
  x = fmaxf(x, dpp<XOR1>(x)); x = fmaxf(x, dpp<XOR2>(x)); x = fmaxf(x, dpp<ROR4>(x)); x = fmaxf(x, dpp<ROR8>(x)); return x;
}
__device__ __forceinline__ float red_scatter(f32x4 a) {
  auto s0 = __builtin_amdgcn_permlane32_swap(asu(a[0]), asu(a[2]), false, false);
  auto s1 = __builtin_amdgcn_permlane32_swap(asu(a[1]), asu(a[3]), false, false);
  const float b0 = asf(s0[0]) + asf(s0[1]), b1 = asf(s1[0]) + asf(s1[1]);
  auto t = __builtin_amdgcn_permlane16_swap(asu(b0), asu(b1), false, false);
  float c = asf(t[0]) + asf(t[1]);
  c += dpp<ROR4>(c); c += dpp<ROR8>(c);
  return c;
}
__device__ __forceinline__ void lds_st32(unsigned a, unsigned v) { asm volatile("ds_write_b32 %0, %1" ::"v"(a), "v"(v) : "memory"); }
template <unsigned M> __device__ __forceinline__ void lds_st16(unsigned a, unsigned v) {
  asm volatile("s_mov_b32 exec_lo, %2\n\ts_mov_b32 exec_hi, %2\n\tds_write_b16 %0, %1\n\ts_mov_b64 exec, -1" ::"v"(a), "v"(v), "n"(M) : "memory");
}
__device__ __forceinline__ void lds_st64(unsigned a, v2u v) { asm volatile("ds_write_b64 %0, %1" ::"v"(a), "v"(v) : "memory"); }
__device__ __forceinline__ v4u lds_ld128_wait(unsigned a) {
  v4u r; asm volatile("ds_read_b128 %0, %1\n\ts_waitcnt lgkmcnt(0)" : "=v"(r) : "v"(a) : "memory"); return r;
}
__device__ __forceinline__ unsigned lds_ld32(unsigned a) { return *(const volatile LAS unsigned*)(uintptr_t)a; }
__device__ __forceinline__ v4u lds128(unsigned a) { return *(const LAS v4u*)(uintptr_t)a; }
__device__ __forceinline__ v2u ldstr(unsigned a) {
  return __builtin_bit_cast(v2u, __builtin_amdgcn_ds_read_tr16_b64_v4i16((LAS s16x4*)(uintptr_t)a));
}
__device__ __forceinline__ v4u ldstr2(unsigned a0, unsigned a1) {
  const v2u x = ldstr(a0), y = ldstr(a1); return v4u{x[0], x[1], y[0], y[1]};
}
__device__ __forceinline__ __amdgpu_buffer_rsrc_t mkrs(const void* p, unsigned bytes) {
  return __builtin_amdgcn_make_buffer_rsrc((void*)p, (short)0, (int)bytes, 0x00020000);
}
__device__ __forceinline__ void gst(__amdgpu_buffer_rsrc_t rs, unsigned vo, v4u v, unsigned so = 0) { __builtin_amdgcn_raw_buffer_store_b128(v, rs, (int)vo, (int)so, 16); }
struct Tags { unsigned t, y, h[4]; };
__device__ __forceinline__ Tags mktags(unsigned T) {
  Tags g; g.t = T;
  const unsigned x = T ^ (T >> 16);
  const unsigned c[4] = {0x9E3779B1u, 0x85EBCA77u, 0xC2B2AE3Du, 0x27D4EB2Fu};
  g.y = x * 0x7FEB352Du;
#pragma unroll
  for (int e = 0; e < 4; ++e) g.h[e] = (((x * c[e]) >> 16) & 0xFFFEu) | (T & 1u);
  return g;
}
constexpr unsigned M10 = 0x3FFu, M16 = 0xFFFFu;
__device__ __forceinline__ v4u tag4(f32x4 a, const Tags& T) {
  v4u r;
#pragma unroll
  for (int e = 0; e < 4; ++e) asm("v_bfi_b32 %0, %1, %2, %3" : "=v"(r[e]) : "v"(M10), "s"(T.h[e]), "v"(asu(a[e]) + 0x200u));
  return r;
}

__device__ __forceinline__ void wait_full(int need, int& seen, int& bud) {
  unsigned t;
  asm volatile("s_cmp_ge_i32 %[seen], %[need]\n\ts_cbranch_scc1 2f\n\t"
               "1:\n\tds_read_b32 %[t], %[a]\n\ts_waitcnt lgkmcnt(0)\n\tv_readfirstlane_b32 %[seen], %[t]\n\t"
               "s_cmp_ge_i32 %[seen], %[need]\n\ts_cbranch_scc1 2f\n\t"
               "s_sub_u32 %[cnt], %[cnt], 1\n\ts_cmp_gt_i32 %[cnt], 0\n\ts_cbranch_scc0 2f\n\ts_sleep 1\n\ts_branch 1b\n\t2:"
               : [t] "=&v"(t), [seen] "+s"(seen), [cnt] "+s"(bud) : [need] "s"(need), [a] "v"(L_FULL) : "memory", "scc");
}
__device__ __forceinline__ unsigned gate(int d, int& bud) {
  unsigned e; int n;
  asm volatile("1:\n\tds_read_b32 v233, %[a]\n\ts_memrealtime vcc\n\ts_waitcnt lgkmcnt(0)\n\tv_readfirstlane_b32 %[e], v233\n\t"
               "s_sub_u32 %[cnt], %[cnt], 1\n\ts_cmp_gt_i32 %[cnt], 0\n\ts_cbranch_scc0 3f\n\t"
               "s_cmp_eq_u32 %[e], 0\n\ts_cbranch_scc1 1b\n\t"
               "s_add_u32 %[e], %[e], %[d]\n\t"
               "2:\n\ts_sub_u32 %[n], vcc_lo, %[e]\n\ts_cmp_ge_i32 %[n], 0\n\ts_cbranch_scc1 3f\n\t"
               "s_memrealtime vcc\n\ts_sub_u32 %[cnt], %[cnt], 1\n\ts_cmp_gt_i32 %[cnt], 0\n\ts_waitcnt lgkmcnt(0)\n\ts_cbranch_scc1 2b\n\t3:"
               : [e] "=&s"(e), [n] "=&s"(n), [cnt] "+s"(bud) : [a] "v"(L_ETA), [d] "s"(d) : "memory", "scc", "vcc", "v233");
  return e;
}
__device__ __forceinline__ void until(unsigned e, int& bud) {
  int n;
  asm volatile("1:\n\ts_memrealtime vcc\n\ts_sub_u32 %[cnt], %[cnt], 1\n\ts_cmp_gt_i32 %[cnt], 0\n\ts_waitcnt lgkmcnt(0)\n\ts_cbranch_scc0 2f\n\t"
               "s_sub_u32 %[n], vcc_lo, %[e]\n\ts_cmp_ge_i32 %[n], 0\n\ts_cbranch_scc1 2f\n\ts_sleep 1\n\ts_branch 1b\n\t2:"
               : [n] "=&s"(n), [cnt] "+s"(bud) : [e] "s"(e) : "memory", "scc", "vcc");
}
__device__ __forceinline__ v2u exch(unsigned flags, unsigned a, v4u& f0, v4u& f1, int& bud) {
  v2u th;
  asm volatile("1:\n\tds_read_b128 v[228:231], %[fl]\n\tds_read_b128 %[f0], %[a]\n\tds_read_b128 %[f1], %[a] offset:64\n\t"
               "ds_read_b64 %[th], %[fl] offset:24\n\ts_waitcnt lgkmcnt(3)\n\t"
               "v_and_b32 v228, v228, v229\n\tv_bitop3_b32 v228, v228, v230, v231 bitop3:0x80\n\t"
               "v_cmp_eq_u32 vcc, 0, v228\n\ts_waitcnt lgkmcnt(0)\n\ts_cbranch_vccz 2f\n\t"
               "s_sub_u32 %[cnt], %[cnt], 1\n\ts_cmp_gt_i32 %[cnt], 0\n\ts_cbranch_scc1 1b\n\t2:"
               : [th] "=&v"(th), [f0] "=&v"(f0), [f1] "=&v"(f1), [cnt] "+s"(bud) : [fl] "v"(flags), [a] "v"(a)
               : "memory", "vcc", "scc", "v228", "v229", "v230", "v231");
  return th;
}
#define G_LD(R, OFF) "buffer_load_dwordx4 " R ", %[vo], %[rs], %[so] offen offset:" #OFF " sc1\n\t"
#define G_LDX "buffer_load_dwordx4 v[224:227], %[xo], %[rs], %[so] offen sc1\n\t"
#define G_CHK(W, A0, A1, A2, A3, R0, R1, R2, R3, B)                                                             \
  "s_waitcnt vmcnt(" #W ")\n\t"                                                                                 \
  "v_xor_b32 %[" #A0 "], %[tm0], " R0 "\n\tv_xor_b32 %[" #A1 "], %[tm1], " R1 "\n\t"                                 \
  "v_xor_b32 %[" #A2 "], %[tm2], " R2 "\n\tv_xor_b32 %[" #A3 "], %[tm3], " R3 "\n\t"                                 \
  "v_or3_b32 v233, %[" #A0 "], %[" #A1 "], %[" #A2 "]\n\t"                                                        \
  "v_bitop3_b32 " B ", v233, %[mk], %[" #A3 "] bitop3:0xc8\n\t"
#define G_CHKX(W) "s_waitcnt vmcnt(" #W ")\n\tv_xor_b32 v232, %[t], v225\n\tv_bitop3_b32 v232, v232, v227, %[ty] bitop3:0xf6\n\tv_mov_b32 %[x0], v224\n\tv_mov_b32 %[x2], v226\n\t"
#define G_TEST(B) "v_cmp_ne_u32 vcc, 0, " B "\n\ts_cbranch_vccz 2f\n\ts_sub_u32 %[cnt], %[cnt], 1\n\ts_cmp_gt_i32 %[cnt], 0\n\ts_cbranch_scc0 2f\n\t"   \
  ".if %[ns] > 1\n\ts_add_u32 %[so], %[so], %[sl]\n\ts_cmp_eq_u32 %[so], %[ns]*%[sl]\n\ts_cselect_b32 %[so], 0, %[so]\n\ts_add_u32 %[e], %[e], %[gap]\n\t" \
  "4:\n\ts_memrealtime vcc\n\ts_waitcnt lgkmcnt(0)\n\ts_sub_u32 vcc_hi, vcc_lo, %[e]\n\ts_cmp_lt_i32 vcc_hi, 0\n\ts_cbranch_scc1 4b\n\t"      \
  ".else\n\ts_sleep %[gap]\n\t.endif\n\t"
#define G_RE(B, LD) "v_cmp_ne_u32 vcc, 0, " B "\n\ts_cbranch_vccz 3f\n\ts_mov_b64 exec, vcc\n\t" LD "s_mov_b64 exec, -1\n\t3:\n\t"
#define G_END "s_waitcnt vmcnt(0)\n\ts_branch 1b\n\t2:"
#define G_C0(W) G_CHK(W, a0, a1, a2, a3, "v208", "v209", "v210", "v211", "v228")
#define G_C1(W) G_CHK(W, b0, b1, b2, b3, "v212", "v213", "v214", "v215", "v229")
#define G_C2(W) G_CHK(W, c0, c1, c2, c3, "v216", "v217", "v218", "v219", "v230")
#define G_C3(W) G_CHK(W, d0, d1, d2, d3, "v220", "v221", "v222", "v223", "v231")
#define G_L0 G_LD("v[208:211]", 0)
#define G_L1 G_LD("v[212:215]", 1024)
#define G_L2 G_LD("v[216:219]", 2048)
#define G_L3 G_LD("v[220:223]", 3072)
#define G_OA [a0] "=&v"(o[0]), [a1] "=&v"(o[1]), [a2] "=&v"(o[2]), [a3] "=&v"(o[3])
#define G_OB [b0] "=&v"(o[4]), [b1] "=&v"(o[5]), [b2] "=&v"(o[6]), [b3] "=&v"(o[7])
#define G_OC [c0] "=&v"(o[8]), [c1] "=&v"(o[9]), [c2] "=&v"(o[10]), [c3] "=&v"(o[11]), \
             [d0] "=&v"(o[12]), [d1] "=&v"(o[13]), [d2] "=&v"(o[14]), [d3] "=&v"(o[15])
#define G_OX [x0] "=&v"(x0), [x2] "=&v"(x2)
#define G_IN [vo] "v"(vo), [rs] "s"(rs), [tm0] "s"(T.h[0] & MASK), [tm1] "s"(T.h[1] & MASK), [tm2] "s"(T.h[2] & MASK), [tm3] "s"(T.h[3] & MASK), [mk] "s"(MASK), [gap] "n"(GAP), [ns] "n"(NS), [sl] "n"(SLOT)
#define G_INX [xo] "v"(xo), [t] "s"(T.t), [ty] "s"(T.y)
#define G_CLOB "memory", "vcc", "scc", "v208", "v209", "v210", "v211", "v212", "v213", "v214", "v215", "v216", "v217", "v218", "v219", \
               "v220", "v221", "v222", "v223", "v224", "v225", "v226", "v227", "v228", "v229", "v230", "v231", "v232", "v233"
template <int N, bool X, unsigned MASK, int GAP, int NS = 1>
__device__ __forceinline__ void gather(__amdgpu_buffer_rsrc_t rs, unsigned vo, unsigned xo, const Tags& T, v4u (&v)[N], float& x0, float& x2, int& cnt, unsigned e = 0) {
  unsigned o[16], so = 0;
  if constexpr (N == 1) {
    asm volatile(G_L0 "1:\n\t" G_C0(0) G_TEST("v228") G_RE("v228", G_L0) G_END
                 : G_OA, [cnt] "+s"(cnt), [so] "+s"(so), [e] "+s"(e) : G_IN : G_CLOB);
  } else if constexpr (N == 2 && !X) {
    asm volatile(G_L0 G_L1 "1:\n\t" G_C0(1) G_C1(0) "v_or_b32 v233, v228, v229\n\t" G_TEST("v233") G_RE("v228", G_L0) G_RE("v229", G_L1) G_END
                 : G_OA, G_OB, [cnt] "+s"(cnt), [so] "+s"(so), [e] "+s"(e) : G_IN : G_CLOB);
  } else if constexpr (N == 2 && X) {
    asm volatile(G_LDX G_L0 G_L1 "1:\n\t" G_CHKX(2) G_C0(1) G_C1(0) "v_or3_b32 v233, v228, v229, v232\n\t" G_TEST("v233")
                 G_RE("v232", G_LDX) G_RE("v228", G_L0) G_RE("v229", G_L1) G_END
                 : G_OA, G_OB, G_OX, [cnt] "+s"(cnt), [so] "+s"(so), [e] "+s"(e) : G_IN, G_INX : G_CLOB);
  } else if constexpr (N == 4 && !X) {
    asm volatile(G_L0 G_L1 G_L2 G_L3 "1:\n\t" G_C0(3) G_C1(2) G_C2(1) "v_or3_b32 v232, v228, v229, v230\n\t" G_C3(0) "v_or_b32 v233, v232, v231\n\t" G_TEST("v233")
                 G_RE("v228", G_L0) G_RE("v229", G_L1) G_RE("v230", G_L2) G_RE("v231", G_L3) G_END
                 : G_OA, G_OB, G_OC, [cnt] "+s"(cnt), [so] "+s"(so), [e] "+s"(e) : G_IN : G_CLOB);
  } else {
    asm volatile(G_LDX G_L0 G_L1 G_L2 G_L3 "1:\n\t" G_CHKX(4) G_C0(3) G_C1(2) G_C2(1) G_C3(0)
                 "v_or3_b32 v233, v228, v229, v230\n\tv_or3_b32 v233, v233, v231, v232\n\t" G_TEST("v233")
                 G_RE("v232", G_LDX) G_RE("v228", G_L0) G_RE("v229", G_L1) G_RE("v230", G_L2) G_RE("v231", G_L3) G_END
                 : G_OA, G_OB, G_OC, G_OX, [cnt] "+s"(cnt), [so] "+s"(so), [e] "+s"(e) : G_IN, G_INX : G_CLOB);
  }
#pragma unroll
  for (int i = 0; i < N; ++i) v[i] = v4u{o[4 * i], o[4 * i + 1], o[4 * i + 2], o[4 * i + 3]};
}
__device__ __forceinline__ f32x4 asf4(v4u v) { return __builtin_bit_cast(f32x4, v); }


#define Q_LD(R, O, S)                                                                                  \
  "buffer_load_dwordx4 %[" O "0], %[" R "], %[" S "] offen nt lds\n\t"                                    \
  "buffer_load_dwordx4 %[" O "1], %[" R "], %[" S "] offen offset:1024 nt lds\n\t"                        \
  "buffer_load_dwordx4 %[" O "2], %[" R "], %[" S "] offen offset:2048 nt lds\n\t"                        \
  "buffer_load_dwordx4 %[" O "3], %[" R "], %[" S "] offen offset:3072 nt lds\n\t"
#define Q_PRE "s_max_i32 %[full], %[full], %[f]\n\tv_mov_b32 v233, %[full]\n\ts_add_i32 %[f], %[f], 1\n\ts_waitcnt vmcnt(%[n])\n\tds_write_b32 %[af], v233\n\t"
#define Q_NXT "s_add_u32 m0, m0, 0x1000\n\t"
template <int D>
__device__ __forceinline__ unsigned quad(__amdgpu_buffer_rsrc_t ra, __amdgpu_buffer_rsrc_t rb, const unsigned (&a)[4], const unsigned (&c)[4],
                                         unsigned s0, unsigned s1, unsigned s2, unsigned s3, int& u, int& full, unsigned& ref) {
  unsigned long long t; int f = u - D;
  asm volatile("s_mov_b32 m0, %[m]\n\t" Q_PRE Q_LD("ra", "a", "s0") Q_NXT Q_PRE Q_LD("ra", "a", "s1") Q_NXT Q_PRE Q_LD("rb", "c", "s2")
               Q_NXT Q_PRE "s_memrealtime %[t]\n\tds_read_b32 v232, %[af] offset:68\n\t" Q_LD("rb", "c", "s3") "s_waitcnt lgkmcnt(0)\n\tv_readfirstlane_b32 %[ref], v232"
               : [t] "=&s"(t), [f] "+s"(f), [full] "+s"(full), [ref] "=&s"(ref)
               : [m] "s"((unsigned)(u & 31) * 4096u), [n] "n"(4 * D), [af] "v"(L_FULL), [ra] "s"(ra), [rb] "s"(rb),
                 [a0] "v"(a[0]), [a1] "v"(a[1]), [a2] "v"(a[2]), [a3] "v"(a[3]), [c0] "v"(c[0]), [c1] "v"(c[1]), [c2] "v"(c[2]), [c3] "v"(c[3]),
                 [s0] "s"(s0), [s1] "s"(s1), [s2] "s"(s2), [s3] "s"(s3)
               : "memory", "scc", "v232", "v233");
  u += 4;
  return (unsigned)t;
}
template <int FROM, int TO> __device__ __forceinline__ void settle(int u, int& full) {
  if constexpr (FROM >= TO) {
    asm volatile("s_max_i32 %0, %0, %2\n\tv_mov_b32 v233, %0\n\ts_waitcnt vmcnt(%3)\n\tds_write_b32 %1, v233"
                 : "+s"(full) : "v"(L_FULL), "s"(u - FROM), "n"(4 * FROM) : "memory", "scc", "v233");
    settle<FROM - 1, TO>(u, full);
  }
}

typedef unsigned v32u __attribute__((ext_vector_type(32)));
#define D_K(RB, K, H, B)                                                                                          \
  "v_xor_b32 v[208+" #K "], 32*" #K ", %[" B "]\n\t"                                                              \
  "ds_read_b64_tr_b16 v[" #RB "+8*" #K "+" #H ":" #RB "+8*" #K "+" #H "+1], v[208+" #K "]\n\t"                       \
  "ds_read_b64_tr_b16 v[" #RB "+8*" #K "+" #H "+2:" #RB "+8*" #K "+" #H "+3], v[208+" #K "] offset:16384\n\t"
#define D_H(RB, H, B) D_K(RB, 0, H, B) D_K(RB, 1, H, B) D_K(RB, 2, H, B) D_K(RB, 3, H, B) D_K(RB, 4, H, B) D_K(RB, 5, H, B) D_K(RB, 6, H, B) D_K(RB, 7, H, B)
#define D_ALL(RB) "s_cmp_eq_u32 %[on], 0\n\ts_cbranch_scc1 1f\n\t" D_H(RB, 0, "b0") D_H(RB, 4, "b1") "s_waitcnt lgkmcnt(0)\n\t1:"
#define D_CLOB "memory", "scc", "v208", "v209", "v210", "v211", "v212", "v213", "v214", "v215"
template <int P, int U> __device__ __forceinline__ void drain(bool on, unsigned wa, v32u (&W)[4]) {
  const unsigned b0 = wa + (U & 31) * 4096u, b1 = wa + ((U + 8) & 31) * 4096u;
  if constexpr (P == 0) asm volatile(D_ALL(64) : "+{v[64:95]}"(W[0]), "+{v[96:127]}"(W[1]) : [on] "s"(on ? 7 : 0), [b0] "v"(b0), [b1] "v"(b1) : D_CLOB);
  else asm volatile(D_ALL(128) : "+{v[128:159]}"(W[2]), "+{v[160:191]}"(W[3]) : [on] "s"(on ? 7 : 0), [b0] "v"(b0), [b1] "v"(b1) : D_CLOB);
}
template <int O> __device__ __forceinline__ v4u sub4(const v32u& v) { return __builtin_shufflevector(v, v, O, O + 1, O + 2, O + 3); }
__device__ __forceinline__ void vecmat(v4u f0, v4u f1, const v32u (&W)[4], f32x4 (&d)[4], f32x4& s, const unsigned long long (&m)[4]) {
  asm volatile(
               "v_mfma_f32_16x16x32_bf16 %[s], v[240:243], v[240:243], 0\n\tv_cndmask_b32 v208, 0, v240, %[m0]\n\t"
               "v_cndmask_b32 v209, 0, v241, %[m0]\n\tv_cndmask_b32 v210, 0, v242, %[m0]\n\t"
               "v_cndmask_b32 v211, 0, v243, %[m0]\n\tv_mfma_f32_16x16x32_bf16 %[s], v[244:247], v[244:247], %[s]\n\t"
               "v_cndmask_b32 v212, 0, v244, %[m0]\n\tv_cndmask_b32 v213, 0, v245, %[m0]\n\t"
               "v_mfma_f32_16x16x32_bf16 %[d0], v[208:211], v[64:67], 0\n\tv_cndmask_b32 v214, 0, v246, %[m0]\n\t"
               "v_mfma_f32_16x16x32_bf16 %[d1], v[208:211], v[128:131], 0\n\tv_cndmask_b32 v215, 0, v247, %[m0]\n\t"
               "v_mfma_f32_16x16x32_bf16 %[d2], v[208:211], v[72:75], 0\n\tv_cndmask_b32 v216, 0, v240, %[m1]\n\t"
               "v_mfma_f32_16x16x32_bf16 %[d3], v[208:211], v[136:139], 0\n\tv_cndmask_b32 v217, 0, v241, %[m1]\n\t"
               "v_mfma_f32_16x16x32_bf16 %[d0], v[212:215], v[68:71], %[d0]\n\tv_cndmask_b32 v218, 0, v242, %[m1]\n\t"
               "v_mfma_f32_16x16x32_bf16 %[d1], v[212:215], v[132:135], %[d1]\n\tv_cndmask_b32 v219, 0, v243, %[m1]\n\t"
               "v_mfma_f32_16x16x32_bf16 %[d2], v[212:215], v[76:79], %[d2]\n\tv_cndmask_b32 v220, 0, v244, %[m1]\n\t"
               "v_mfma_f32_16x16x32_bf16 %[d3], v[212:215], v[140:143], %[d3]\n\tv_cndmask_b32 v221, 0, v245, %[m1]\n\t"
               "v_mfma_f32_16x16x32_bf16 %[d0], v[216:219], v[80:83], %[d0]\n\tv_cndmask_b32 v222, 0, v246, %[m1]\n\t"
               "v_mfma_f32_16x16x32_bf16 %[d1], v[216:219], v[144:147], %[d1]\n\tv_cndmask_b32 v223, 0, v247, %[m1]\n\t"
               "v_mfma_f32_16x16x32_bf16 %[d2], v[216:219], v[88:91], %[d2]\n\tv_cndmask_b32 v224, 0, v240, %[m2]\n\t"
               "v_mfma_f32_16x16x32_bf16 %[d3], v[216:219], v[152:155], %[d3]\n\tv_cndmask_b32 v225, 0, v241, %[m2]\n\t"
               "v_mfma_f32_16x16x32_bf16 %[d0], v[220:223], v[84:87], %[d0]\n\tv_cndmask_b32 v226, 0, v242, %[m2]\n\t"
               "v_mfma_f32_16x16x32_bf16 %[d1], v[220:223], v[148:151], %[d1]\n\tv_cndmask_b32 v227, 0, v243, %[m2]\n\t"
               "v_mfma_f32_16x16x32_bf16 %[d2], v[220:223], v[92:95], %[d2]\n\tv_cndmask_b32 v228, 0, v244, %[m2]\n\t"
               "v_mfma_f32_16x16x32_bf16 %[d3], v[220:223], v[156:159], %[d3]\n\tv_cndmask_b32 v229, 0, v245, %[m2]\n\t"
               "v_mfma_f32_16x16x32_bf16 %[d0], v[224:227], v[96:99], %[d0]\n\tv_cndmask_b32 v230, 0, v246, %[m2]\n\t"
               "v_mfma_f32_16x16x32_bf16 %[d1], v[224:227], v[160:163], %[d1]\n\tv_cndmask_b32 v231, 0, v247, %[m2]\n\t"
               "v_mfma_f32_16x16x32_bf16 %[d2], v[224:227], v[104:107], %[d2]\n\tv_cndmask_b32 v232, 0, v240, %[m3]\n\t"
               "v_mfma_f32_16x16x32_bf16 %[d3], v[224:227], v[168:171], %[d3]\n\tv_cndmask_b32 v233, 0, v241, %[m3]\n\t"
               "v_mfma_f32_16x16x32_bf16 %[d0], v[228:231], v[100:103], %[d0]\n\tv_cndmask_b32 v234, 0, v242, %[m3]\n\t"
               "v_mfma_f32_16x16x32_bf16 %[d1], v[228:231], v[164:167], %[d1]\n\tv_cndmask_b32 v235, 0, v243, %[m3]\n\t"
               "v_mfma_f32_16x16x32_bf16 %[d2], v[228:231], v[108:111], %[d2]\n\tv_cndmask_b32 v236, 0, v244, %[m3]\n\t"
               "v_mfma_f32_16x16x32_bf16 %[d3], v[228:231], v[172:175], %[d3]\n\tv_cndmask_b32 v237, 0, v245, %[m3]\n\t"
               "v_mfma_f32_16x16x32_bf16 %[d0], v[232:235], v[112:115], %[d0]\n\tv_cndmask_b32 v238, 0, v246, %[m3]\n\t"
               "v_mfma_f32_16x16x32_bf16 %[d1], v[232:235], v[176:179], %[d1]\n\tv_cndmask_b32 v239, 0, v247, %[m3]\n\t"
               "v_mfma_f32_16x16x32_bf16 %[d2], v[232:235], v[120:123], %[d2]\n\tv_mfma_f32_16x16x32_bf16 %[d3], v[232:235], v[184:187], %[d3]\n\t"
               "v_mfma_f32_16x16x32_bf16 %[d0], v[236:239], v[116:119], %[d0]\n\tv_mfma_f32_16x16x32_bf16 %[d1], v[236:239], v[180:183], %[d1]\n\t"
               "v_mfma_f32_16x16x32_bf16 %[d2], v[236:239], v[124:127], %[d2]\n\tv_mfma_f32_16x16x32_bf16 %[d3], v[236:239], v[188:191], %[d3]\n\t"
               : [d0] "=&v"(d[0]), [d1] "=&v"(d[1]), [d2] "=&v"(d[2]), [d3] "=&v"(d[3]), [s] "=&v"(s)
               : "{v[240:243]}"(f0), "{v[244:247]}"(f1), "{v[64:95]}"(W[0]), "{v[96:127]}"(W[1]), "{v[128:159]}"(W[2]), "{v[160:191]}"(W[3]),
                 [m0] "s"(m[0]), [m1] "s"(m[1]), [m2] "s"(m[2]), [m3] "s"(m[3])
               : "v208", "v209", "v210", "v211", "v212", "v213", "v214", "v215", "v216", "v217", "v218", "v219", "v220", "v221", "v222", "v223", "v224", "v225", "v226", "v227", "v228", "v229", "v230", "v231", "v232", "v233", "v234", "v235", "v236", "v237", "v238", "v239");
}
__device__ __forceinline__ void red_gu(f32x4 a, float& G, float& U) {
  auto s0 = __builtin_amdgcn_permlane32_swap(asu(a[0]), asu(a[2]), false, false);
  auto s1 = __builtin_amdgcn_permlane32_swap(asu(a[1]), asu(a[3]), false, false);
  G = asf(s0[0]) + asf(s0[1]); U = asf(s1[0]) + asf(s1[1]);
  auto t0 = __builtin_amdgcn_permlane16_swap(asu(G), asu(G), false, false);
  auto t1 = __builtin_amdgcn_permlane16_swap(asu(U), asu(U), false, false);
  G = asf(t0[0]) + asf(t0[1]); U = asf(t1[0]) + asf(t1[1]);
  G += dpp<ROR8>(G); U += dpp<ROR8>(U);
}

typedef unsigned v16u __attribute__((ext_vector_type(16)));
__device__ __forceinline__ const void* mkptr(unsigned lo, unsigned hi) { return (const void*)(((unsigned long long)hi << 32) | lo); }

__global__ void __launch_bounds__(384, 2)
mk(const void*, const void*, const void*, const void*, const void*, const void*, const void*, const void*, void*, void*) {
  int b = blockIdx.x;
  v16u ka; v4u kb; unsigned long long t00, pc0, w1;
  {
    unsigned t, o;
    asm volatile("s_load_dwordx16 %0, %8, 0x0\n\ts_and_b32 %6, %5, 63\n\ts_cmp_lt_u32 %6, 36\n\ts_cselect_b32 %7, 56, 40\n\t"
                 "s_cmp_lt_u32 %6, 32\n\ts_cselect_b32 %7, 24, %7\n\ts_cmp_lt_u32 %6, 16\n\ts_cselect_b32 %7, 32, %7\n\t"
                 "s_load_dwordx2 %4, %8, %7\n\ts_load_dwordx4 %1, %8, 0x40\n\ts_memrealtime %2\n\ts_getpc_b64 %3\n\tmk_code_begin:"
                 : "=&s"(ka), "=&s"(kb), "=&s"(t00), "=&s"(pc0), "=&s"(w1), "+s"(b), "=&s"(t), "=&s"(o) : "s"(__builtin_amdgcn_kernarg_segment_ptr()) : "scc");
  }
  const int lane = threadIdx.x & 63;
  const int wv = __builtin_amdgcn_readfirstlane(threadIdx.x >> 6);
  const int g4 = lane >> 4, c16 = lane & 15;
  const int grp = b >> 6, j = b & 63;
  const bool isO = j < 16, isQ = j >= 16 && j < 32, isG = j >= 36;
  const int jq = (j - 2 * grp) & 15;
  const int jd = isQ ? jq + 4 : j - 32;
  const bool isD = (unsigned)jd < (isQ ? 14u : 4u);
  const int per = isG ? 28 : isO ? 16 : 14, loc = isG ? j - 36 : isO ? j : jd;
  const int idx = per * grp + loc;
  const int r16 = idx & 15, cb = idx >> 4;
  const int ukv = j < 32 ? 32 : 16;
  const int qc = 8 * (j & 7) + 2 * grp + ((j >> 3) & 1);

  asm volatile("s_cmp_eq_u32 %4, 0\n\ts_cbranch_scc1 1f\n\ts_waitcnt lgkmcnt(0)\n\t1:" : "+s"(ka), "+s"(kb), "+s"(t00), "+s"(w1) : "s"(wv) : "scc");
  if (__builtin_expect(wv == 0, 1)) {
    const unsigned stride = isG ? 7168u : 2048u;
    const unsigned s1 = isG ? 512u * 7168u : 16u * 2048u, s2 = isG ? 16u * 7168u : 32u * 2048u;
    const unsigned base0 = (unsigned)r16 * (isG ? 32u * 7168u : 64u * 2048u) + (unsigned)cb * (isG ? 1024u : 16u * 64u * 2048u);
    const unsigned base1 = base0 + (isG ? 0u : 1024u);
    unsigned o1[4];
    const unsigned st1 = isQ ? 8192u : stride, d1 = (isQ ? 65536u : 4u * stride) - 1024u, us1 = isQ ? 32u * 8192u : 256u;
    unsigned so1 = isQ ? (unsigned)qc * 128u : base0;
    {
      const unsigned sw = 16u * ((lane & (isQ ? 7 : 15)) ^ ((lane >> 3) & 6)), r = (lane >> (isQ ? 3 : 4)) * st1;
      o1[0] = r + sw; o1[1] = r + (sw ^ (isQ ? 0u : 128u)) + d1; o1[2] = o1[0] + 2 * d1; o1[3] = o1[1] + 2 * d1;
    }
    unsigned so1b = so1 + us1, so1c = so1 + 2 * us1, so1d = so1 + 3 * us1, wb1 = j < 32 ? 4096u * 2048u : 3584u * 2048u;
    asm volatile("s_waitcnt lgkmcnt(0)" : "+s"(ka), "+s"(kb), "+s"(t00), "+s"(w1), "+s"(so1), "+s"(so1b), "+s"(so1c), "+s"(so1d), "+s"(wb1)
                 : "v"(o1[0]), "v"(o1[1]), "v"(o1[2]), "v"(o1[3]));
    const __amdgpu_buffer_rsrc_t r1 = mkrs((const void*)w1, wb1);
    asm volatile("s_mov_b32 m0, 0\n\ts_nop 0\n\t" Q_LD("ra", "a", "s0") Q_NXT "s_nop 0\n\t" Q_LD("ra", "a", "s1") Q_NXT "s_nop 0\n\t" Q_LD("ra", "a", "s2") Q_NXT "s_nop 0\n\t" Q_LD("ra", "a", "s3")
                 :: [ra] "s"(r1), [a0] "v"(o1[0]), [a1] "v"(o1[1]), [a2] "v"(o1[2]), [a3] "v"(o1[3]),
                    [s0] "s"(so1), [s1] "s"(so1b), [s2] "s"(so1c), [s3] "s"(so1d) : "memory", "scc");
    const unsigned ct0 = (unsigned)t00;
    lds_st32(L_CTL + 4 * lane, lane == 17 ? ct0 + CT_A : 0u);
    lds_st32(L_VEC + 4 * lane, 0x3F803F80u);
    unsigned ref;
    int u = 4, freed = 0, full = 0;
    int bud = BUDGET;
    auto space = [&]() {
      if (freed < u - 28) {
        for (unsigned spins = 0;; ++spins) {
          const v4u p = lds_ld128_wait(L_PROG);
          freed = (int)__builtin_amdgcn_readfirstlane(min(min(p[0], p[1]), min(p[2], p[3])));
          if (freed >= u - 28) break;
          if (spins == 0) settle<0, 0>(u, full);
          if (bud <= 0) break;
          --bud;
          __builtin_amdgcn_s_sleep(1);
        }
      }
    };
    if (isQ) {
#pragma unroll 1
      while (u < 32) {
        so1 += 128u * 8192u;
        const unsigned t = quad<INFLQ>(r1, r1, o1, o1, so1, so1 + 32u * 8192u, so1 + 64u * 8192u, so1 + 96u * 8192u, u, full, ref);
        if (u == 8) __builtin_amdgcn_s_barrier();
      }
      settle<INFLQ, INFL + 1>(u, full);
    } else {
      asm volatile("s_waitcnt lgkmcnt(0)" ::: "memory");
      __builtin_amdgcn_s_barrier();
#pragma unroll 1
      while (u < ukv) {
        const unsigned so = base0 + (u >> 4) * 1024u + ((u >> 2) & 1) * s1 + ((u >> 3) & 1) * s2;
        const unsigned t = quad<INFL0>(r1, r1, o1, o1, so, so + 256, so + 512, so + 768, u, full, ref);
      }
    }
    unsigned chV[4];
#pragma unroll
    for (int i = 0; i < 4; ++i) chV[i] = 16 * (c16 ^ (8 * (i & 1) + 2 * g4));
    {
      unsigned oK[4], oV[4];
#pragma unroll
      for (int i = 0; i < 4; ++i) {
        oK[i] = g4 * 256 + 16 * (c16 ^ (4 * i + g4));
        oV[i] = g4 * 256 + chV[i];
      }
      const __amdgpu_buffer_rsrc_t rk = mkrs(mkptr(ka[2], ka[3]), 1u << 26), rv = mkrs(mkptr(ka[4], ka[5]), 1u << 26);
      unsigned so = ((unsigned)grp << 24) + (unsigned)(j < 32 ? 30 * j : 34 * j - 128) * 8192u;
      int d = INFL;
#pragma unroll 1
      for (; u < U_POST; so += 8192) {
        space();
        unsigned t;
#define KVQ(D) t = quad<D>(rk, rv, oK, oV, so, so + 4096, so, so + 4096, u, full, ref)
        if (!CTL) { KVQ(INFL); }
        else {
          if (d <= CT_LO + 2) { if (d <= CT_LO) KVQ(CT_LO); else if (d == CT_LO + 1) KVQ(CT_LO + 1); else KVQ(CT_LO + 2); }
          else { if (d == CT_LO + 3) KVQ(CT_LO + 3); else if (d == CT_LO + 4) KVQ(CT_LO + 4); else KVQ(CT_LO + 5); }
          const int lag = (int)(t - ref) - ((full * RATE) >> 8);
          d = min(INFL + ((lag * (4096 / CT_G) + 2048) >> 12), min(max(d, CT_LO) + SLEW, CT_HI));
        }
        if (u == 36) settle<4, 4>(u, full);
      }
    }
    constexpr int DMAX = CTL ? CT_LO + 5 : INFL;
    if (isG || isD) {
      const __amdgpu_buffer_rsrc_t r = mkrs(mkptr(isG ? ka[12] : ka[14], isG ? ka[13] : ka[15]), 3584u * 2048u);
      unsigned oW[4];
#pragma unroll
      for (int i = 0; i < 4; ++i) oW[i] = (4 * i + g4) * stride + chV[i] - 1024 * i;
      settle<DMAX, INFLD + 1>(u, full);
      if (!isQ) settle<INFLD, INFLP + 1>(u, full);
      const int end = U_POST + (isQ ? 32 : 16);
#pragma unroll 1
      while (u < end) {
        space();
        const int i = u - U_POST;
        const unsigned so = base1 - (i >> 4) * 1024u + ((i >> 2) & 1) * s1 + ((i >> 3) & 1) * s2;
        unsigned t;
        if (isQ) t = quad<INFLD>(r, r, oW, oW, so, so + 256, so + 512, so + 768, u, full, ref);
        else t = quad<INFLP>(r, r, oW, oW, so, so + 256, so + 512, so + 768, u, full, ref);
      }
    }
    settle<(DMAX > INFLD ? DMAX : INFLD), 0>(u, full);
    return;
  }

  const int w = wv - 1;
  const GAS unsigned short* x = (const GAS unsigned short*)mkptr(ka[0], ka[1]);
  char* ws = (char*)mkptr(kb[2], kb[3]);
  const __amdgpu_buffer_rsrc_t rs = mkrs(ws, WS_ALL);
  v4u xa = *(const GAS v4u*)(x + 16 * lane), xb = *(const GAS v4u*)(x + 16 * lane + 8);
  unsigned tprev;
  asm volatile("s_load_dword %0, %1, 0x0" : "=&s"(tprev) : "s"(ws));
  if (!isQ) {
    const int rb = 4 * (j >> 3) + grp + (isO ? 8 : 0);
    if (PF == 5 && w == 0) asm volatile("global_load_dword v249, %0, %1 nt" ::"v"((32 * rb + (lane >> 1)) * 8192 + (b & 7) * 1024 + 384 + (lane & 1) * 512), "s"(mkptr(ka[6], ka[7])) : "v249");
    if (CPF && w == 1) asm volatile("v_min_u32 v233, mk_code_end-mk_code_begin, %0\n\tglobal_load_dword v249, v233, %1" ::"v"((b & 24) * 512 + (b & 64) * 256 + lane * 64), "s"(pc0) : "v233", "v249");
  }
  __builtin_amdgcn_s_barrier();
  asm volatile("s_waitcnt lgkmcnt(0)" : "+s"(tprev));
  const Tags T = mktags(tprev + 1u);
  int bud = BUDGET;
  int seen = 0;
  if (w == 4) {
    auto tagged = [&](unsigned t) { return v4u{(t << 16) | T.h[0], (t << 16) | T.h[1], (t << 16) | T.h[2], (t << 16) | T.h[3]}; };
    if (FB) {
      const bool rep = grp == 2 ? (j >> 3) == 5 : grp < 2 && (j & 0x39) == 17 * grp;
      const unsigned aS = rep && lane < 4 * FB_R ? WS_PH + (lane >> 2) * 2048 + ((grp & 2) * 4 + (j & 7)) * 128 + (lane & 3) * 16 : OOB;
      const unsigned aL = WS_PH + ((b >> 3) & (FB_R - 1)) * 2048 + c16 * 128;
      wait_full(FB_0, seen, bud);
      unsigned te = (unsigned)__builtin_amdgcn_s_memrealtime() & ~(FB_P - 1u);
      asm volatile("v_mov_b32 v232, %0" ::"s"(~T.h[0]) : "v232");
#pragma unroll 1
      for (int n = 0; n < 64; ++n) {
        unsigned long long now; unsigned v;
        te += FB_P;
        const unsigned ep = ((te / FB_P) & 15u) * 8192u;
        until(te, bud);
        asm volatile("ds_read_b32 v233, %2\n\ts_memrealtime %0\n\ts_waitcnt lgkmcnt(0)\n\tv_readfirstlane_b32 %1, v233" : "=&s"(now), "=&s"(seen) : "v"(L_FULL) : "memory", "v233");
        const unsigned p = (unsigned)now - ((seen * RATE) >> 8);
        gst(rs, (int)((unsigned)now - te) < FB_L ? aS + ep : OOB, tagged(p));
        if (FB_P > 128) {
          if (seen >= FB_1) break;
          until(te + FB_W, bud);
          asm volatile("buffer_load_dword %0, %1, %2, %3 offen sc1\n\ts_waitcnt vmcnt(0)" : "=v"(v) : "v"(aL), "s"(rs), "s"(ep) : "memory");
        } else {
          asm volatile("s_waitcnt vmcnt(1)\n\tv_mov_b32 %0, v232" : "=v"(v) :: "memory");
        }
        const bool ok = (v & M16) == T.h[0];
        const int cnt = __builtin_popcount((unsigned)__builtin_amdgcn_ballot_w64(ok) & 0xFFFFu);
        const float dl = ok ? (float)min(max((int)(v - (p << 16)) >> 16, -500), 500) : 0.f;
        const float mean = row_sum(dl) * __builtin_amdgcn_rcpf((float)cnt);
        if (cnt >= FB_MIN) lds_st32(L_REF, p + (unsigned)((int)mean - FB_OFF));
        if (FB_P <= 128) {
          if (seen >= FB_1) break;
          until(te + FB_W, bud);
          asm volatile("buffer_load_dword v232, %0, %1, %2 offen sc1" ::"v"(aL), "s"(rs), "s"(ep) : "memory", "v232");
        }
      }
    }
    wait_full(U_POST - 4 * NE + 2, seen, bud);
    const unsigned eta = (unsigned)__builtin_amdgcn_s_memrealtime() + KE;
    gst(rs, lane < 4 ? WS_ET + b * 64 + lane * 16 : OOB, tagged(eta));
    lds_st32(L_OWN, eta);
    if (PT) asm volatile("buffer_load_dword v249, %0, %1, 0 offen" ::"v"(lane < 8 ? (lane >> 1) * SLOT + (lane & 1) * 0x3EE000u + 0x2000u : OOB), "s"(rs) : "v249");
    if (CPF2) asm volatile("v_min_u32 v233, mk_code_end-mk_code_begin, %0\n\tglobal_load_dword v249, v233, %1" ::"v"((b & 24) * 512 + (b & 64) * 256 + lane * 64), "s"(pc0) : "v233", "v249");
    const unsigned tp = (unsigned)__builtin_amdgcn_readfirstlane((int)lds_ld128_wait(L_ETA)[1]) + (((U_POST - 4 * NE + 2) * RATE) >> 8) + FB_OFF;
    unsigned now = eta;
    const unsigned dn = isO ? DB : isG ? DC - LWG : DD;
#pragma unroll 1
    for (int all = isO ? 0 : isQ && !isD ? 2 : 1; all < 2; ++all) {
      until(tp + (all ? ET_2 : ET_1), bud);
      v4u v[1]; float d0, d1;
      gather<1, false, M16, 8>(rs, all ? WS_E2 + (b & 15) * 256 + (lane & 3) * 64 : WS_ET + grp * 4096 + lane * 64, 0, T, v, d0, d1, bud);
      now = (unsigned)__builtin_amdgcn_s_memrealtime();
      const unsigned E = (now + (unsigned)min((int)xrow_max(row_max((float)((int)(v[0][0] - (now << 16)) >> 16))), 300)) | 1u;
      lds_st64(L_EA, v2u{E + (all ? dn - LW0 : 10000u), E + dn - LW1});
      if (all != (int)isO) lds_st32(L_ETA, E);
      if (!all) gst(rs, lane < 4 ? WS_E2 + j * 256 + grp * 64 + lane * 16 : OOB, tagged(E));
    }
    return;
  }
  const int sub = lane >> 2, p4 = lane & 3;
  const unsigned trrow = 256u * sub + 8u * p4;
  const unsigned hcol = (g4 & 1) * 512 + 32 * r16 + (g4 >> 1) * 16 + 4 * w + p4;
  float xres = 0.f;
  if (isQ) {
    float s = 0.f;
#pragma unroll
    for (int e = 0; e < 4; ++e) s += (lo16(xa[e]) + hi16(xa[e])) + (lo16(xb[e]) + hi16(xb[e]));
    const float mean = xrow_sum(row_sum(s)) * (1.f / 1024.f);
    float q = 0.f;
#pragma unroll
    for (int e = 0; e < 4; ++e) {
      const float d0 = lo16(xa[e]) - mean, d1 = hi16(xa[e]) - mean, d2 = lo16(xb[e]) - mean, d3 = hi16(xb[e]) - mean;
      q += (d0 * d0 + d1 * d1) + (d2 * d2 + d3 * d3);
    }
    const float var = xrow_sum(row_sum(q)) * (1.f / 1024.f);
    const float mb = rbf(mean), rsd = __builtin_amdgcn_rcpf(rbf(__builtin_amdgcn_sqrtf(rbf(rbf(var) + 1e-5f))));
    auto nrm = [&](unsigned p) { return pk2(rbf(lo16(p) - mb) * rsd, rbf(hi16(p) - mb) * rsd); };
    const unsigned xl = L_XN + (lane >> 1) * 64 + (lane & 1) * 8;
    lds_st64(xl, v2u{nrm(xa[0]), nrm(xa[1])});
    lds_st64(xl + 16, v2u{nrm(xa[2]), nrm(xa[3])});
    lds_st64(xl + 32, v2u{nrm(xb[0]), nrm(xb[1])});
    lds_st64(xl + 48, v2u{nrm(xb[2]), nrm(xb[3])});
    asm volatile("s_waitcnt lgkmcnt(0)" ::: "memory");
    unsigned a = sub * 128u + 16u * ((2 * w + (p4 >> 1)) ^ (sub & 6)) + 8u * (p4 & 1), xo = L_XN + g4 * 16;
    f32x4 ac[4] = {};
#pragma unroll 1
    for (int u0 = 0; u0 < 32; u0 += 4, a += 16384, xo += 256) {
      v4u xf[4], wf[4];
#pragma unroll
      for (int i = 0; i < 4; ++i) xf[i] = lds128(xo + 64 * i);
      wait_full(u0 + 4, seen, bud);
#pragma unroll
      for (int i = 0; i < 4; ++i) wf[i] = ldstr2(a + 4096 * i, a + 4096 * i + 2048);
      lds_st32(L_PROG + 4 * w, (unsigned)(u0 + 4));
#pragma unroll
      for (int i = 0; i < 4; ++i) ac[i] = mfma(wf[i], xf[i], ac[i]);
    }
    const f32x4 acc = (ac[0] + ac[1]) + (ac[2] + ac[3]);
    v4u r;
#pragma unroll
    for (int e = 0; e < 4; ++e) r[e] = (pk2(acc[e], acc[e]) << 16) | T.h[e];
    gst(rs, c16 < NREP ? WS_QF + c16 * 16384 + qc * 256 + w * 64 + g4 * 16 : OOB, r);
  } else {
    xres = lo16(x[hcol]);
    if (isG && w == cb) gst(rs, WS_PD + (r16 * 4 + w) * 4096u + 3584u + (lane & 31) * 16, tag4(f32x4{0.f, 0.f, 0.f, 0.f}, T));
  }
  asm volatile("s_waitcnt vmcnt(0)" ::: "memory");
  const unsigned wa = w * 4096u + trrow + 32u * (sub & 7);
  v32u W[4];
  asm volatile("" : "={v[64:95]}"(W[0]), "={v[96:127]}"(W[1]), "={v[128:159]}"(W[2]), "={v[160:191]}"(W[3]));
  wait_full(16, seen, bud);
  drain<0, 0>(!isQ, wa, W);
  lds_st32(L_PROG + 4 * w, isQ ? 32u : 16u);
  wait_full(ukv, seen, bud);
  drain<1, 16>(isO, wa, W);
  lds_st32(L_PROG + 4 * w, (unsigned)ukv);
  wait_full(isQ ? QATQ : QAT, seen, bud);
  v4u qf[4];
  {
    v4u v[1]; float d0, d1;
    gather<1, false, M16, GAPQ>(rs, WS_QF + ((b & (NREP - 1)) * 32 + grp * 8 + 2 * w) * 512 + lane * 16, 0, T, v, d0, d1, bud);
    lds_st64(L_Q + w * 512 + lane * 8, v2u{(v[0][0] >> 16) | v[0][1], (v[0][2] >> 16) | v[0][3]});
    lds_st32(L_XF + 4 * w, 1u);
    exch(L_XF, L_Q + (c16 & 7) * 256 + g4 * 16, qf[0], qf[1], bud);
    qf[2] = lds128(L_Q + (c16 & 7) * 256 + g4 * 16 + 128);
    qf[3] = lds128(L_Q + (c16 & 7) * 256 + g4 * 16 + 192);
  }

  {
    unsigned ka[4], va[2];
#pragma unroll
    for (int u = 0; u < 4; ++u) ka[u] = 256u * c16 + 16u * ((4 * u + g4) ^ c16);
#pragma unroll
    for (int i = 0; i < 2; ++i) va[i] = 8192u + trrow + 32u * ((2 * w + i) ^ (sub & 7));
    const bool up = c16 >= 8;
    const float LOG2E = 1.4426950408889634f, scale = 0.08838834764831845f;
    float m = -INFINITY, l = 0.f;
    f32x4 acc[2] = {f32x4{0.f, 0.f, 0.f, 0.f}, f32x4{0.f, 0.f, 0.f, 0.f}};
    const unsigned aPA = WS_PA + (((grp * 8 + (c16 & 7)) * 8 + 2 * w + (c16 >> 3)) * 64 + j) * 64 + g4 * 16;
    const unsigned aML = 8 * w + lane < 8 ? WS_ML + (b * 8 + lane) * 16 : OOB;
#pragma unroll 1
    for (int u0 = ukv; u0 < U_POST; u0 += 4) {
      const unsigned tb = (unsigned)(u0 & 31) * 4096u;
      wait_full(u0 + 2, seen, bud);
      f32x4 s0 = {0.f, 0.f, 0.f, 0.f}, s1 = {0.f, 0.f, 0.f, 0.f};
#pragma unroll
      for (int u = 0; u < 4; ++u) {
        s0 = mfma(lds128(tb + ka[u]), qf[u], s0);
        s1 = mfma(lds128(tb + ka[u] + 4096), qf[u], s1);
      }
      float s[4];
#pragma unroll
      for (int r = 0; r < 4; ++r) s[r] = rbf(rbf(up ? s1[r] : s0[r]) * scale);
      const float mn = fmaxf(m, xrow_max(fmaxf(fmaxf(s[0], s[1]), fmaxf(s[2], s[3]))));
      const float al = __builtin_amdgcn_exp2f((m - mn) * LOG2E);
      m = mn;
      float p[4];
#pragma unroll
      for (int r = 0; r < 4; ++r) p[r] = __builtin_amdgcn_exp2f((s[r] - mn) * LOG2E);
      l = l * al + ((p[0] + p[1]) + (p[2] + p[3]));
      const unsigned p01 = pk2(p[0], p[1]), p23 = pk2(p[2], p[3]);
      v4u pb = up ? v4u{0u, 0u, p01, p23} : v4u{p01, p23, 0u, 0u};
      acc[0] *= al; acc[1] *= al;
      asm volatile("" : "+v"(pb), "+v"(acc[0]), "+v"(acc[1]), "+v"(l));
      wait_full(u0 + 4, seen, bud);
      const v4u v0 = ldstr2(tb + va[0], tb + va[0] + 4096), v1 = ldstr2(tb + va[1], tb + va[1] + 4096);
      acc[0] = mfma(v0, pb, acc[0]);
      acc[1] = mfma(v1, pb, acc[1]);
      asm volatile("s_waitcnt lgkmcnt(0)" ::: "memory");
      lds_st32(L_PROG + 4 * w, (unsigned)(u0 + 4));
    }
    const unsigned eta = lds_ld32(L_OWN), now = (unsigned)__builtin_amdgcn_s_memrealtime();
    const float M = fmaxf(m, dpp<ROR8>(m));
    const float f = __builtin_amdgcn_exp2f((m - M) * LOG2E);
    float lt = xrow_sum(l) * f;
    lt += dpp<ROR8>(lt);
    f32x4 o;
#pragma unroll
    for (int e = 0; e < 4; ++e) {
      const float a0 = acc[0][e] * f, a1 = acc[1][e] * f;
      o[e] = (up ? a1 : a0) + dpp<ROR8>(up ? a0 : a1);
    }
    gst(rs, aPA, tag4(o, T));
    gst(rs, aML, v4u{asu(M), T.t, asu(lt), T.y});
    if (NSA > 1 && (int)(now - (unsigned)__builtin_amdgcn_readfirstlane((int)eta)) > DA - LWA) {
      gst(rs, aPA, tag4(o, T), SLOT);
      gst(rs, aML, v4u{asu(M), T.t, asu(lt), T.y}, SLOT);
    }
  }

  if (isO || isG || isD) {
    const unsigned aPC = (((8 * cb + 2 * w) * 4 + 2 * g4 + (c16 >> 3)) * 32 + r16) * 128 + (c16 & 7) * 16;
    unsigned aOut = isG ? WS_PC + aPC : (isO ? WS_PB : WS_PD) + (((4 * w + g4) * 4 + (c16 >> 2)) * 64 + idx) * 64 + (c16 & 3) * 16;
    unsigned aCS = isG ? aOut + 2048 : OOB;
    unsigned aST = isG && (lane & 0x31) == 0 ? WS_ST + ((8 * cb + 2 * w + (lane >> 3)) * 16 + r16) * 64 + (lane & 6) * 8 : OOB;
    unsigned aFl = L_XF + 16 + 4 * w, one = 1u, aX = L_XF + 16, aV = L_VEC + (c16 & 1) * 128 + g4 * 16;
    unsigned long long gm[4] = {0x000F000F000F000Full, 0x00F000F000F000F0ull, 0x0F000F000F000F00ull, 0xF000F000F000F000ull};
    asm volatile("" : "+s"(gm[0]), "+s"(gm[1]), "+s"(gm[2]), "+s"(gm[3]));
    asm volatile("" : "+v"(aFl), "+v"(one), "+v"(aX), "+v"(aV), "+v"(aOut), "+v"(aCS), "+v"(aST));
    float h = 0.f;
    wait_full(isO ? 0 : U_POST + 16, seen, bud);
    drain<1, U_POST>(!isO, wa, W);
    wait_full(isQ ? U_POST + 32 : 0, seen, bud);
    drain<0, U_POST + 16>(isQ, wa, W);
#define NF0
#define NF1(sh)
    if (isO) {
      v4u v[4]; float ms, ls;
      unsigned a0 = WS_PA + (idx * 4 + w) * 4096u + lane * 16, a1 = WS_ML + ((grp * 64 + 16 * p4 + sub) * 8 + (j >> 1)) * 16;
      asm volatile("" : "+v"(a0), "+v"(a1));
      const unsigned e = gate(DA, bud);
      NF0;
      gather<4, true, M10, (NSA > 1 ? LG : 0), NSA>(rs, a0, a1, T, v, ms, ls, bud, e);
      NF1(0);
      const float LOG2E = 1.4426950408889634f;
      const float M = xrow_max(row_max(ms));
      const float f = __builtin_amdgcn_exp2f((ms - M) * LOG2E);
      const float L = xrow_sum(row_sum(f * ls));
      f32x4 a = asf4(v[0]) * dpp<0x00>(f);
      a += asf4(v[1]) * dpp<0x55>(f);
      a += asf4(v[2]) * dpp<0xAA>(f);
      a += asf4(v[3]) * dpp<0xFF>(f);
      const float o = red_scatter(a) * __builtin_amdgcn_rcpf(L);
      lds_st16<0x000F000Fu>(L_VEC + (w >> 1) * 64 + p4 * 16 + (w & 1) * 8 + g4 * 2, pk2(o, o));
    } else {
      if (isG) {
        v4u v[4]; float d0, d1;
        unsigned a0 = WS_PB + (r16 * 4 + w) * 4096u + lane * 16;
        asm volatile("" : "+v"(a0));
        const unsigned e = gate(DB, bud);
        NF0;
        gather<4, false, M10, (NSB > 1 ? LG : 0), NSB>(rs, a0, 0, T, v, d0, d1, bud, e);
        NF1(0);
        const f32x4 a = (asf4(v[0]) + asf4(v[1])) + (asf4(v[2]) + asf4(v[3]));
        h = rbf(xres + rbf(red_scatter(a)));
        lds_st16<0x000F000Fu>(L_VEC + (g4 >> 1) * 64 + w * 16 + (g4 & 1) * 8 + p4 * 2, asu(h) >> 16);
      } else {
        v4u v[4]; float sv0, sv2;
        unsigned a0 = WS_PC + (idx * 4 + w) * 4096u + lane * 16, a1 = WS_ST + (idx * 16 + c16) * 64 + w * 16;
        asm volatile("" : "+v"(a0), "+v"(a1));
        const unsigned e = gate(DC, bud);
        NF0;
        gather<4, true, M10, (NSB > 1 ? LG : 0), NSB>(rs, a0, a1, T, v, sv0, sv2, bud, e);
        NF1(0);
        const float mean = row_sum(sv0) * (1.f / 1024.f);
        const float var = fmaxf(row_sum(sv2) * (1.f / 1024.f) - mean * mean, 0.f);
        const float mb = rbf(mean), sd = rbf(__builtin_amdgcn_sqrtf(rbf(rbf(var) + 1e-5f)));
        const float rsd = __builtin_amdgcn_rcpf(sd);
        float G, U;
        red_gu((asf4(v[0]) + asf4(v[1])) - mb * (asf4(v[2]) + asf4(v[3])), G, U);
        const float g = rbf(G * rsd), u = rbf(U * rsd);
        const float y = 0.7978845608028654f * (g + 0.044715f * g * g * g);
        const float ge = rbf(g * __builtin_amdgcn_rcpf(1.f + __builtin_amdgcn_exp2f(y * (-2.f * 1.4426950408889634f))));
        const float act = ge * u;
        lds_st16<0xFFu>(L_VEC + (w >> 1) * 64 + (2 * (w & 1) + ((lane >> 2) & 1)) * 16 + (lane >> 5) * 8 + p4 * 2, pk2(act, act));
      }
    }
    lds_st32(aFl, one);
    v4u f0, f1;
    const v2u th = exch(aX, aV, f0, f1, bud);
    unsigned long long now;
    asm volatile("s_memrealtime %0" : "=s"(now));
    f32x4 d[4], s;
    vecmat(f0, f1, W, d, s, gm);
    asm volatile("s_waitcnt lgkmcnt(0)" : "+s"(now));
    auto slot = [&](unsigned e) {
      unsigned r; int t;
      asm volatile("v_readfirstlane_b32 %1, %3\n\ts_sub_u32 %1, %2, %1\n\ts_cmp_gt_i32 %1, 0\n\ts_cselect_b32 %0, %4, 0\n\ts_cmpk_gt_i32 %1, %7\n\ts_cselect_b32 %0, %5, %0\n\t"
                   "s_cmpk_gt_i32 %1, 2*%7\n\ts_cselect_b32 %0, %6, %0"
                   : "=&s"(r), "=&s"(t) : "s"((unsigned)now), "v"(e), "n"(NSB > 1 ? SLOT : 0), "n"((NSB > 2 ? 2 : NSB - 1) * SLOT), "n"((NSB - 1) * SLOT), "n"(LG) : "scc");
      return r;
    };
    const unsigned lo = slot(th[0]);
    __builtin_amdgcn_sched_barrier(0);
    const v4u res = tag4(f32x4{d[0][0], d[1][0], d[2][0], d[3][0]}, T);
    gst(rs, aOut, res, lo);
    __builtin_amdgcn_sched_barrier(0);
    const v4u st = {asu(s[1]), T.t, asu(s[0]), T.y}, c4 = tag4(f32x4{d[0][1], d[1][1], d[2][1], d[3][1]}, T);
    gst(rs, aCS, c4, lo);
    gst(rs, aST, st, lo);
    __builtin_amdgcn_sched_barrier(0);
    const unsigned hi = slot(th[1]);
    if (hi != lo) { gst(rs, aOut, res, hi); gst(rs, aCS, c4, hi); gst(rs, aST, st, hi); }
    if (isG) {
      if (idx == 111 && w == 0) __builtin_amdgcn_raw_buffer_store_b32(T.t, rs, lane ? (int)OOB : 0, 0, 16);
      if (w == cb) {
        v4u v[4]; float d0, d1;
        unsigned a0 = WS_PD + (r16 * 4 + w) * 4096u + lane * 16;
        asm volatile("" : "+v"(a0));
        const unsigned e = gate(DD, bud);
        NF0;
        gather<4, false, M10, (NSB > 1 ? LG : 0), NSB>(rs, a0, 0, T, v, d0, d1, bud, e);
        NF1(8);
        const f32x4 a = (asf4(v[0]) + asf4(v[1])) + (asf4(v[2]) + asf4(v[3]));
        const float res = h + rbf(red_scatter(a));
        __builtin_amdgcn_raw_buffer_store_b16((unsigned short)pk2(res, res), mkrs(mkptr(kb[0], kb[1]), 2048), (int)(c16 < 4 ? 2 * hcol : OOB), 0, 16);
        asm volatile("mk_code_end:");
      }
    }
  }
}

extern "C" void kernel_launch(void* const* d_in, const int* in_sizes, int n_in, void* d_out, int out_size, void* d_ws,
                              size_t ws_size, hipStream_t stream) {
  (void)in_sizes; (void)n_in; (void)out_size; (void)ws_size;
  (void)hipFuncSetAttribute((const void*)mk, hipFuncAttributeMaxDynamicSharedMemorySize, (int)LDS_BYTES);
  char* wsb = (char*)d_ws + ((ws_size - WS_ALL) & ~(size_t)0x1FFFFF);
  mk<<<256, 384, LDS_BYTES, stream>>>(d_in[0], d_in[1], d_in[2], d_in[3], d_in[4], d_in[6], d_in[7], d_in[8], d_out, wsb);
}
```
